```python
import torch

def _softmax(x, axis=-1):
    return torch.softmax(x, dim=axis)
def _gelu(x):
    return torch.nn.functional.gelu(x, approximate="tanh")
def _silu(x):
    return torch.nn.functional.silu(x)
def _sigmoid(x):
    return torch.sigmoid(x)
def _layernorm(x, axis=-1, eps=1e-5):
    m = x.mean(dim=axis, keepdim=True)
    v = x.var(dim=axis, keepdim=True, unbiased=False)
    return (x - m) / torch.sqrt(v + eps)
def _scatter_add(tgt, idx, src, axis=0):
    return tgt.clone().index_add_(axis, idx, src)
def _segment_sum(src, seg, n):
    out = torch.zeros((n,) + src.shape[1:], dtype=src.dtype)
    return out.index_add_(0, seg, src)
def _pad(x, pw):
    flat = tuple(v for pair in reversed(pw) for v in pair)
    return torch.nn.functional.pad(x, flat)

kernel_name = 'interleave_layers__sliding_local_dilated__mixer_token_mlp__prompt'

def setup_inputs():

    _w = (lambda a: (lambda t: t.to(torch.bfloat16) if t.is_floating_point() else t)(torch.as_tensor(a)))
    def _A_setup():
        B, S, D = 1, 2048, 1024
        return {
            "x": _w((lambda *s: torch.randn(*s, dtype=torch.bfloat16) * 0.05)(B, S, D)),
            "wqkv": _w((lambda *s: torch.randn(*s, dtype=torch.bfloat16) * 0.05)(D, 3 * D)),
            "wo": _w((lambda *s: torch.randn(*s, dtype=torch.bfloat16) * 0.05)(D, D)),
        }
    _A_in = _A_setup()

    def _B_setup():
        B, S, D, F = 1, 2048, 1024, 3584
        return {
            "x": _w((lambda *s: torch.randn(*s, dtype=torch.bfloat16) * 0.05)(B, S, D)),
            "w1": _w((lambda *s: torch.randn(*s, dtype=torch.bfloat16) * 1.6)(S, F) * (S ** -0.5)),
            "b1": _w((lambda *s: torch.randn(*s, dtype=torch.bfloat16) * 1.6)(F) * 0.02),
            "w2": _w((lambda *s: torch.randn(*s, dtype=torch.bfloat16) * 1.6)(F, S) * (F ** -0.5)),
            "b2": _w((lambda *s: torch.randn(*s, dtype=torch.bfloat16) * 1.6)(S) * 0.02),
        }
    _B_in = _B_setup()

    return {
        "A_x": _A_in["x"],
        "A_wqkv": _A_in["wqkv"],
        "A_wo": _A_in["wo"],
        "B_x": _B_in["x"],
        "B_w1": _B_in["w1"],
        "B_b1": _B_in["b1"],
        "B_w2": _B_in["w2"],
        "B_b2": _B_in["b2"],
    }

def reference(A_x, A_wqkv, A_wo, B_x, B_w1, B_b1, B_w2, B_b2):
    def _A_ref(x, wqkv, wo):
        B, S, D = x.shape
        H = 32; Dh = D // H
        W, dil = 8, 2
        qkv = (lambda x, *d: x.permute(*d))((x @ wqkv).reshape(B, S, 3, H, Dh), 2, 0, 3, 1, 4)
        q, k, v = qkv[0], qkv[1], qkv[2]
        scale = Dh ** -0.5
        pos = torch.arange(S)
        off = torch.arange(W) * dil
        idx = pos[:, None] - off[None, :]
        mask = idx >= 0
        idx_c = torch.where(mask, idx, 0).reshape(-1)
        kg = k[:, :, idx_c, :].reshape(B, H, S, W, Dh)
        vg = v[:, :, idx_c, :].reshape(B, H, S, W, Dh)
        logits = torch.einsum("bhsd,bhswd->bhsw", q, kg) * scale
        logits = torch.where(mask[None, None, :, :], logits, -1e9)
        attn = _softmax(logits, axis=-1)
        o = torch.einsum("bhsw,bhswd->bhsd", attn, vg)
        return (lambda x, *d: x.permute(*d))(o, 0, 2, 1, 3).reshape(B, S, D) @ wo

    def _B_ref(x, w1, b1, w2, b2):
        B, S, D = x.shape
        h = _layernorm(x, axis=-1)
        u = torch.einsum("bsd,sf->bdf", h, w1) + b1
        u = _gelu(u)
        y = torch.einsum("bdf,fs->bds", u, w2) + b2
        return (lambda x, *d: x.permute(*d))(y, 0, 2, 1)

    x = A_x
    x = x + _A_ref(_layernorm(x, axis=-1), A_wqkv, A_wo)
    x = x + _B_ref(_layernorm(x, axis=-1), B_w1, B_b1, B_w2, B_b2)
    x = x + _A_ref(_layernorm(x, axis=-1), A_wqkv, A_wo)
    x = x + _B_ref(_layernorm(x, axis=-1), B_w1, B_b1, B_w2, B_b2)
    return x
```

```cpp
#include <hip/hip_runtime.h>
#include <hip/hip_bf16.h>
#include <utility>
#include <type_traits>

typedef unsigned short u16;
using bf16x8 = __attribute__((ext_vector_type(8))) short;
using s16x4 = __attribute__((ext_vector_type(4))) short;
using f32x2 = __attribute__((ext_vector_type(2))) float;
using f32x4 = __attribute__((ext_vector_type(4))) float;
using f32x16 = __attribute__((ext_vector_type(16))) float;
using u32x2 = __attribute__((ext_vector_type(2))) unsigned;
using u32x4 = __attribute__((ext_vector_type(4))) unsigned;
using u32x12 = __attribute__((ext_vector_type(12))) unsigned;
typedef __bf16 bf16x2_t __attribute__((ext_vector_type(2)));
#define LAS __attribute__((address_space(3)))

constexpr int S_ = 2048, D_ = 1024, F_ = 3584;
template <int N> __device__ __forceinline__ int phys(int r) { return (r & 7) != 3 ? r : N + 4 + ((r >> 5) << 3) + ((r >> 3) & 3); }
constexpr int TB = 160 * 128;

__device__ __forceinline__ unsigned pk2(float a, float b) {
  f32x2 v = {a, b};
  return __builtin_bit_cast(unsigned, __builtin_convertvector(v, bf16x2_t));
}
__device__ __forceinline__ float lo2f(unsigned v) { return __uint_as_float(v << 16); }
__device__ __forceinline__ float hi2f(unsigned v) { return __uint_as_float(v & 0xFFFF0000u); }
__device__ __forceinline__ float bf2f(u16 v) { return __uint_as_float((unsigned)v << 16); }
__device__ __forceinline__ float rbf(float f) { return lo2f(pk2(f, f)); }
__device__ __forceinline__ u16 f2bf(float f) { return (u16)(pk2(f, f) & 0xFFFFu); }

enum { KC = 0, KM = 1 };
#define OPAQUE_BID unsigned BID = blockIdx.x; asm volatile("" : "+s"(BID))
__device__ __forceinline__ int OPQ(int v) { asm volatile("" : "+s"(v)); return v; }
__device__ __forceinline__ unsigned opq_bid() { OPAQUE_BID; return BID; }
__device__ __forceinline__ int lane_id() {
  const unsigned l = __builtin_amdgcn_mbcnt_hi(~0u, __builtin_amdgcn_mbcnt_lo(~0u, OPQ(0)));
  __builtin_assume(l < 64);
  return l;
}
constexpr int L_A = 0, L_B = 73728, L_C = L_B + 65536, L_D = L_C + 16384, L_TAG = L_D + 1280, L_CSQ = L_D + 1408, L_SCR = L_D + 1792, L_BIA = L_D + 2048, L_RS = L_D + 2560, L_MSK = L_RS + 1152, L_SHM = L_MSK + 2048;
typedef unsigned long long u64;
#define NR_O 8
#define NR_H 8
#define NR_U 8
#define NR_X 8
#define RS1 32
#define RS2 64
#define RSP 32
constexpr int C_FLAG = 0, C_UCNT = 1024 * RSP, C_HCNT = C_UCNT + 16 * NR_U * RS1, C_OCNT = C_HCNT + 8 * NR_H * RS2, C_XCNT = C_OCNT + 16 * NR_O * RS1,
              C_UCN2 = C_XCNT + 16 * NR_X * RS1, C_END = C_UCN2 + 16 * NR_U * RS1, C_WORDS = C_END <= 32768 ? 32768 : 131072;
static_assert(C_END <= C_WORDS, "control words");
#define TAG (*(volatile LAS u64*)(size_t)vconst<L_TAG>())

#define A1 16
#define A2 16
#define AC 2
#define CWN 8
#define PF2 5
#define WOT 11
#define FWS 1
#define KD 3
#define CKK 5
#define FBK 1
#define NP2 3
#define NP3 4
#define NP4 2
#define HQP 21
#define WT0 19
#define WSTG 2
#define WPER 18
#define ARMS 13
#define GM 8
#define HSW 1
#define GN 8
#define G0 3
#define GD 1
#define CFENCE asm volatile("" ::: "memory")
#define WAITV(n) asm volatile("s_waitcnt vmcnt(%0)" ::"i"(n) : "memory")
#define WAITL0 do { CFENCE; __builtin_amdgcn_s_waitcnt(0xC07F); CFENCE; } while (0)
#define BARRIER do { CFENCE; __builtin_amdgcn_s_barrier(); CFENCE; } while (0)
#define SCHED0 __builtin_amdgcn_sched_barrier(0)
#define ALIGN4 do { SCHED0; asm volatile(".fill (((. - k_all) >> 2) & 1) ^ 1, 4, 0xBF800000"); SCHED0; } while (0)
#define ALIGN8 do { SCHED0; asm volatile(".p2align 3"); SCHED0; } while (0)

struct Operand {
  const u16* base;
  int ld;
  int off0;
  int cstride;
  int bytes;
  int kstep0;
  bool holes;
  const u16* alt;
  int alt_nt;
};
constexpr int CW_BLK = 80 * 128;

template <int L, int MS>
__device__ __forceinline__ int swz(int line) {
  if constexpr (L == KC) {
    return MS == 16 ? (line & 7) : ((line >> 1) & 7);
  } else {
    return MS == 16 ? ((((line >> 1) & 1) | (((line >> 3) & 1) << 1)) << 1) : (((line >> 1) & 1) << 2);
  }
}
template <int N> __device__ __forceinline__ void waitv() { WAITV(N); }
__device__ __forceinline__ unsigned pow2sel(bool c, int a, int b) { unsigned e = c ? a : b; asm volatile("" : "+v"(e)); return 1u << e; }
template <unsigned C> __device__ __forceinline__ unsigned vconst() { unsigned v; asm volatile("v_mov_b32 %0, %1" : "=v"(v) : "i"(C)); return v; }

__device__ __forceinline__ u64 vz() { u64 v; asm volatile("v_mov_b64 %0, 0" : "=v"(v)); return v; }

__device__ __forceinline__ void st16(void* p, u32x4 v) {
  asm volatile("global_store_dwordx4 %0, %1, off sc1\n\ts_nop 1" ::"v"(p), "v"(v) : "memory");
}

__device__ __forceinline__ const char* noslow(const void* base, unsigned line) {
  return (const char*)base + (size_t)((((unsigned)((size_t)base >> 7) + line) & 7) == 3 ? line ^ 1 : line) * 128;
}
__device__ __forceinline__ void touch_line(const void* p, unsigned lds_scratch) {
  unsigned keep;
  asm volatile("s_mov_b32 %0, m0\n\ts_mov_b32 m0, %2\n\ts_nop 0\n\tglobal_load_lds_dword %1, off\n\ts_mov_b32 m0, %0"
               : "=&s"(keep) : "v"(p), "s"(lds_scratch) : "memory");
}

struct NoHook {
  static constexpr int EXTRA = 0, NFIRST = 0, AUX = 0, NEXT = 0;
  static constexpr bool EVOD = false;
  static constexpr bool OWNP = false;
  template <int T> __device__ __forceinline__ void next() {}
  static constexpr bool PREALL = false;
  static constexpr bool PIN = false, UNROLL = false, COLD = false, PSW = false;
  __device__ __forceinline__ bool cold() const { return false; }
  template <int S> __device__ __forceinline__ void stepc() {}
  template <int S, int N> __device__ __forceinline__ void wait() { waitv<N>(); }
  template <int T> __device__ __forceinline__ void tail() {}
  __device__ __forceinline__ void ready() {}
  __device__ __forceinline__ void first() {}
  __device__ __forceinline__ void afterr() {}
  __device__ __forceinline__ void last() {}
  __device__ __forceinline__ void mid() {}
  __device__ __forceinline__ void step(int) {}
};

template <int L, int MS, int B, bool PSW = false>
struct Stream {
  static constexpr int LW = B / 16, STG = B * 128;
  static constexpr int NSUB = (L == KM && B == 128) ? 2 : 1;
  unsigned voff[LW];
  const void* gbase[NSUB];
  int gbytes[NSUB], so[NSUB], inc[NSUB], jmp[NSUB];
  __device__ __forceinline__ void init(const Operand& X, int gw, int lane) {
    const int r8 = lane >> 3, c8 = lane & 7, l0 = gw * 8 + r8;
    const unsigned sw = (unsigned)((c8 ^ (PSW ? (r8 & 3) | (gw << 2) : swz<L, MS>(l0))) * 16);
    if constexpr (L == KC) {
      gbase[0] = X.base; gbytes[0] = X.bytes;
      if (X.holes) {
        jmp[0] = 27 * TB;
        inc[0] = TB; so[0] = ((X.off0 >> 7) * X.ld + X.kstep0) * TB;
        {
          const bool iso = X.ld == 16;
          const int ln = !iso ? l0 >> 1 : ((l0 >> 1) & ~1) | (l0 & 1), ps = !iso ? l0 & 1 : (l0 >> 1) & 1;
          const bool h = ln == 3;
          const unsigned v0 = (h ? 68 : ln) * 128 + ps * 64 + (sw & 48) + (sw >> 6) * (TB / 2), s1 = pow2sel(h, 7, 10), s4 = pow2sel(h, 10, 12);
#pragma unroll
          for (int i = 0; i < LW; ++i) voff[i] = i == 0 ? v0 : (i == 4 ? v0 + s4 : voff[i - 1] + s1);
        }
      } else {
        inc[0] = 128; so[0] = X.kstep0 * 128;
#pragma unroll
        for (int i = 0; i < LW; ++i)
          voff[i] = (unsigned)(i == 0 ? max(X.off0 + 2 * r8 + gw, 0) : X.off0 + 16 + 2 * l0 + 32 * ((i - 1) & 3) + ((i - 1) >> 2)) * (unsigned)X.ld * 2u + sw;
      }
    } else if constexpr (NSUB == 1) {
      gbase[0] = X.base; gbytes[0] = X.bytes;
      inc[0] = 128 * X.ld; so[0] = X.kstep0 * inc[0] + X.off0 * 2;
#pragma unroll
      for (int i = 0; i < LW; ++i) voff[i] = (unsigned)((l0 + 16 * (i & 3)) * X.ld + (i >> 2) * X.cstride) * 2u + sw;
    } else {
      const bool isw2 = X.ld == S_;
      const unsigned sx = isw2 ? sw : ((sw & 48) << 1) | ((sw & 64) >> 2);
#pragma unroll
      for (int t = 0; t < NSUB; ++t) {
        const int lc = (X.off0 >> 6) ^ t;
        const bool slow = (((int)((size_t)X.base >> 7) + lc) & 7) == 3;
        gbase[t] = slow ? (const void*)X.alt : (const void*)X.base;
        gbytes[t] = slow ? (X.ld >> 9) * X.alt_nt * CW_BLK : X.bytes;
        inc[t] = slow ? CW_BLK : 128 * X.ld;
        so[t] = X.kstep0 * inc[t] + (slow ? (lc >> 3) * X.alt_nt * CW_BLK : lc * 128);
        const unsigned rowb = slow ? 128 : X.ld * 2;
        const bool h = (slow ? r8 : 0) == 3;
        const unsigned grpb = isw2 && !slow ? 2 * rowb : rowb;
        const unsigned v0 = (h ? 68 + gw : r8) * rowb + (h ? 0 : gw * 8 * grpb) + sx, s2 = (h ? 2 : 16) * grpb, s1 = (h ? 8 : 32) * grpb;
        jmp[t] = slow ? 27 * CW_BLK : (28 * 64 - 8) * rowb;
        voff[4 * t] = v0; voff[4 * t + 1] = v0 + s2; voff[4 * t + 2] = v0 + s1; voff[4 * t + 3] = v0 + s1 + s2;
      }
    }
  }
  __device__ __forceinline__ void pin() {
#pragma unroll
    for (int i = 0; i < LW; ++i) asm volatile("" : "+v"(voff[i]));
#pragma unroll
    for (int t = 0; t < NSUB; ++t) asm volatile("" : "+s"(so[t]));
  }
  __device__ __forceinline__ void skip(int n) {
#pragma unroll
    for (int t = 0; t < NSUB; ++t) so[t] += n * inc[t];
  }
  __device__ __forceinline__ void twice() {
#pragma unroll
    for (int t = 0; t < NSUB; ++t) inc[t] *= 2;
  }
  __device__ __forceinline__ void back() {
#pragma unroll
    for (int t = 0; t < NSUB; ++t) so[t] -= jmp[t];
  }
  template <int AUX, int NT_ = NSUB>
  __device__ __forceinline__ void stage(LAS char* dst, bool act0 = true) {
#pragma unroll
    for (int t = 0; t < NT_; ++t) {
      const auto rsrc = __builtin_amdgcn_make_buffer_rsrc((void*)gbase[t], 0, gbytes[t], 0x00020000);
#pragma unroll
      for (int i = t * (LW / NSUB); i < (t + 1) * (LW / NSUB); ++i) {
#if defined(__HIP_DEVICE_COMPILE__)
        if (i > 0 || act0) __builtin_amdgcn_raw_ptr_buffer_load_lds(rsrc, (LAS void*)(dst + i * 2048), 16, voff[i], so[t], 0, AUX);
#endif
      }
      so[t] += inc[t];
    }
  }
};

template <int N, class F>
__device__ __forceinline__ void static_for(F&& f) {
  [&]<int... I>(std::integer_sequence<int, I...>) { (f(std::integral_constant<int, I>{}), ...); }(std::make_integer_sequence<int, N>{});
}

template <int L, int MS, int B, int NS, int NT, int KID, bool SKIP0, class Hook>
__device__ __forceinline__ void g_run(LAS char* ring, Stream<L, MS, B, Hook::PSW>& st, int gw, int lane, Hook& hook, bool filled) {
  constexpr int LW = B / 16, STG = B * 128;
  constexpr int NPRE = Hook::PIN ? (Hook::COLD ? 2 : NT == 16 ? NP2 : NT == 32 ? NP3 : NP4) : NS;
  static_assert(Hook::PREALL, "cfg");
  static_assert(B % 16 == 0 && NS >= 3 && NT >= NS, "cfg");
  const bool act0 = !SKIP0 || lane >= 8;
  auto stage = [&](int slot) { st.template stage<Hook::AUX>(ring + slot * STG + gw * 1024, act0); };
  hook.ready();
  if (Hook::COLD && !Hook::PIN ? hook.cold() : !filled) {
    if (Hook::COLD && hook.cold()) st.skip(1); else stage(0);
    stage(1);
    if constexpr (Hook::PREALL) {
      if (!hook.cold()) {
#pragma unroll
        for (int i = 2; i < NPRE; ++i) stage(i);
      }
    }
  }
  SCHED0;
  if constexpr (Hook::COLD && !Hook::PIN) {
    if (hook.cold()) {
      waitv<LW>();
      BARRIER;
#pragma unroll
      for (int i = 2; i < NS; ++i) stage(i);
    }
    SCHED0;
    hook.first();
    if (!hook.cold()) {
      BARRIER;
      if (!filled) { stage(0); stage(1); stage(2); }
      BARRIER;
      waitv<(NS - 1) * LW>();
      BARRIER;
    }
  } else {
    if constexpr (!Hook::PIN) hook.first();
    if constexpr (!Hook::OWNP) {
      waitv<(NPRE - 1) * LW + Hook::NFIRST>();
      BARRIER;
    }
#pragma unroll
    for (int i = NPRE; i < NS; ++i) stage(i);
    if constexpr (Hook::PIN) hook.first();
  }
  if constexpr (Hook::UNROLL) {
    [&]<int... S>(std::integer_sequence<int, S...>) {
      (([&] {
         hook.template wait<S, (NS - 2) * LW>();
         BARRIER;
         stage(S % NS);
         hook.step(S);
         hook.template stepc<S>();
         SCHED0;
       }()), ...);
    }(std::make_integer_sequence<int, NT - NS>{});
  } else {
    int slot = 0;
#pragma unroll 1
    for (int s = 0; s < NT - NS - (Hook::EVOD ? 2 * KD : 0); ++s) {
      waitv<(NS - 2) * LW>();
      BARRIER;
      if (Hook::EVOD && s == 14 - NS) st.back();
      stage(slot);
      slot = (slot == NS - 1) ? 0 : slot + 1;
      hook.step(s);
    }
  }
  constexpr int LH = Hook::EVOD && L == KM ? LW / 2 : LW;
  if constexpr (Hook::EVOD) {
    static_for<2 * KD>([&](auto kc) {
      constexpr int k = decltype(kc)::value, n = NT - 2 * KD + k;
      waitv<(k > 1 ? LH : LW) + (k > 0 ? LH : LW)>();
      BARRIER;
      if constexpr (k == KD) st.so[0] += (hook.kh ? -14 - KD : 14 - KD) * st.inc[0];
      st.template stage<Hook::AUX, 1>(ring + (n % NS) * STG + gw * 1024);
    });
  }
  hook.last();
  [&]<int... T>(std::integer_sequence<int, T...>) {
    (([&] {
       hook.template tail<T>();
       if constexpr (T < NS - 2) hook.template wait<NT - NS + T, (NS - 2 - T) * LH + Hook::EXTRA + T * Hook::NEXT>();
       if constexpr (T == NS - 2) hook.template wait<NT - NS + T, T * Hook::NEXT>();
       BARRIER;
       hook.template next<T>();
       if constexpr (T == NS - 2) hook.mid();
     }()), ...);
  }(std::make_integer_sequence<int, NS>{});
}

template <int L, int MS, int B, int NS, int NT, int KID, bool SKIP0, class Hook>
__device__ __forceinline__ void g_loader(LAS char* ring, const Operand& X, int gw, int lane, Hook& hook, bool filled = false) {
  Stream<L, MS, B, Hook::PSW> st;
  st.init(X, gw, lane);
  if constexpr (Hook::PIN) st.pin();
  if (filled) st.skip(NS);
  g_run<L, MS, B, NS, NT, KID, SKIP0>(ring, st, gw, lane, hook, filled);
}

template <int RW, int RSTRIDE>
struct ResHook : NoHook {
  static constexpr bool EARLY = RW == 4;
  static constexpr int EXTRA = EARLY ? 0 : 8, AUX = RW == 2 ? A2 : 0;
  const u16* base; int row0, col0; LAS char* dst; int gw, lane;
  unsigned vo;
  __device__ __forceinline__ void first() {
    const int r8 = lane >> 3, c8 = lane & 7;
    vo = ((unsigned)(row0 + gw * 8 + r8) * D_ + col0) * 2u + (unsigned)((c8 ^ swz<KC, 32>(gw * 8 + r8)) * 16);
    asm volatile("" : "+v"(vo));
    if constexpr (EARLY) { fetch(); WAITV(0); BARRIER; }
  }
  __device__ __forceinline__ void last() { if constexpr (!EARLY) fetch(); }
  __device__ __forceinline__ void fetch() {
    const auto rsrc = __builtin_amdgcn_make_buffer_rsrc((void*)base, 0, S_ * D_ * 2, 0x00020000);
    LAS char* d = dst + gw * 1024;
#pragma unroll
    for (int i = 0; i < 8; ++i)
      __builtin_amdgcn_raw_ptr_buffer_load_lds(rsrc, (LAS void*)(d + i * 2048), 16, vo, ((((i >> 1) % RW) * RSTRIDE + (i & 1) * 16) * D_ + ((i >> 1) / RW) * 64) * 2, 0, 16);
  }
};

template <class Base, int NG, int GS, int NC, int NW, int NT>
struct SlowColHook : Base {
  const char* p;
  int st, inc8;
  unsigned scr;
  const u16* base_; int ld_, cb0_, kstep0_, j_, lane_; bool leader_;
  __device__ __forceinline__ void setup(const u16* base, int ld, int cb0, int kstep0, int j, int lane, bool leader, unsigned scratch) {
    base_ = base; ld_ = ld; cb0_ = cb0; kstep0_ = kstep0; j_ = j; lane_ = lane; leader_ = leader; scr = scratch;
  }
  __device__ __forceinline__ void setup2(const u16* base, int ld, int cb0, int kstep0, int j, int lane, bool leader, unsigned scratch) {
    asm volatile("" : "+v"(lane));
    constexpr int MAXC = (NC + 7) / 8, PER = (NG * MAXC * 64 + NW - 1) / NW;
    static_assert(PER * 8 <= 64, "lanes");
    const int cf = (3 - (int)(((size_t)base + cb0) >> 7)) & 7;
    const int li = j * PER + lane % PER;
    st = lane / PER;
    const int ci = li >> 6, k = li & 63, g = ci / MAXC, c = cf + 8 * (ci % MAXC);
    const bool act = leader && st < 8 && g < NG && c < NC;
    p = act ? (const char*)base + (size_t)((kstep0 + st) * 64 + k) * ld * 2 + cb0 + g * GS + c * 128 : nullptr;
    inc8 = 8 * 64 * ld * 2;
    scr = scratch;
  }
  __device__ __forceinline__ void touch(bool go = true) {
    if (go && p != nullptr && st < NT) touch_line(p, scr);
    p = p != nullptr ? p + inc8 : p;
    st += 8;
  }
  __device__ __forceinline__ void first() { setup2(base_, ld_, cb0_, kstep0_, j_, lane_, leader_, scr); touch(!Base::cold()); Base::first(); }
  __device__ __forceinline__ void step(int s) { if ((s & 7) == 0 && s + 8 < NT) touch(); Base::step(s); }
};

#define PSLP 3
__device__ __forceinline__ void flag_set(u64* p, u64 tag) { asm volatile("global_atomic_swap_x2 %0, %1, off sc1" ::"v"(p), "v"(tag) : "memory"); }
#define POLL1(r) "global_atomic_or_x2 %[" #r "], %[q], %[z], off sc0 sc1\n\ts_sleep %[sl]\n\t"
#define POLL2(r) "s_waitcnt vmcnt(3)\n\tv_cmp_ne_u64 vcc, %[tag], %[" #r "]\n\ts_cbranch_vccz 2f\n\t" POLL1(r)
#define POLL_LOOP "s_mov_b64 %[sv], exec\n\ts_mov_b64 exec, %[em]\n\ts_mov_b32 %[n], 0\n\t" POLL1(a) POLL1(b) POLL1(c) POLL1(d) POLL_BODY
#define POLL_BODY \
  "1:\n\t" POLL2(a) POLL2(b) POLL2(c) POLL2(d) \
  "s_add_u32 %[n], %[n], 1\n\ts_cmp_lt_u32 %[n], 0x8000\n\ts_cbranch_scc1 1b\n\t" \
  "2:\n\ts_mov_b64 exec, %[sv]\n\t"
#define PCHK(r, l) "v_cmp_ne_u64 vcc, %[tag], %[" #r "]\n\ts_and_b64 %[t], vcc, %[m1]\n\ts_cbranch_scc1 " #l "f\n\ts_cbranch_vccz 2f\n\ts_barrier\n\ts_mov_b64 %[m1], -1\n" #l ":\n\t"
#define POLL3(r, l) "s_waitcnt vmcnt(3)\n\t" PCHK(r, l) POLL1(r)
#define PEND "s_add_u32 %[n], %[n], 1\n\ts_cmp_lt_u32 %[n], 0x8000\n\ts_cbranch_scc1 1b\n" \
             "2:\n\ts_mov_b64 exec, -1\n\ts_cmp_eq_u64 %[m1], -1\n\ts_cbranch_scc1 8f\n\ts_barrier\n8:"
#define POLL0(r) "global_atomic_or_x2 %[" #r "], %[q], %[z], off sc0 sc1\n\t"
struct Gate {
  const u64* p; u64 em, sm, on, tag, a, b, c, d;
  __device__ __forceinline__ void prep(int wid, const u64* set, u64 sm_, const u64* get, u64 em_, u64 on_) {
    p = wid == 0 ? set : get; em = wid == 1 ? em_ : 0; sm = wid == 0 ? sm_ : 0; on = on_;
    tag = TAG;
    asm volatile("" : "+v"(p), "+v"(tag));
  }
  __device__ __forceinline__ void seam() {
    u64 z = vz();
    asm volatile("s_cmp_eq_u64 %[on], 0\n\ts_cbranch_scc1 7f\n\ts_waitcnt vmcnt(0)\n\ts_barrier\n\t"
                 "s_cmp_eq_u64 %[sm], 0\n\ts_cbranch_scc1 6f\n\ts_mov_b64 exec, %[sm]\n\tglobal_atomic_swap_x2 %[q], %[tag], off sc1\n"
                 "6:\n\ts_cmp_eq_u64 %[em], 0\n\ts_cbranch_scc1 7f\n\ts_mov_b64 exec, %[em]\n\t" POLL0(a) "\n7:\n\ts_mov_b64 exec, -1"
                 : [a] "=&v"(a) : [q] "v"(p), [z] "v"(z), [tag] "v"(tag), [sm] "s"(sm), [em] "s"(em), [on] "s"(on) : "memory", "scc");
  }
  u64 m1;
  __device__ __forceinline__ void pass() {
    u64 z = vz(), t;
    unsigned n;
    asm volatile("s_cmp_eq_u64 %[on], 0\n\ts_cbranch_scc1 8f\n\ts_cmp_eq_u64 %[em], 0\n\ts_cbranch_scc1 2f\n\ts_mov_b64 exec, %[em]\n\ts_mov_b32 %[n], 0\n\t"
                 POLL0(b) "s_waitcnt vmcnt(1)\n\t" PCHK(a, 3) POLL1(c) POLL1(d) POLL0(a)
                 "1:\n\t" POLL3(b, 4) POLL3(c, 5) POLL3(d, 6) POLL3(a, 7) PEND
                 : [a] "+v"(a), [b] "=&v"(b), [c] "=&v"(c), [d] "=&v"(d), [n] "=&s"(n), [t] "=&s"(t), [m1] "+s"(m1)
                 : [q] "v"(p), [z] "v"(z), [tag] "v"(tag), [em] "s"(em), [on] "s"(on), [sl] "i"(PSLP) : "memory", "vcc", "scc");
  }
  __device__ __forceinline__ void done() {
    asm volatile("s_cmp_eq_u64 %[em], 0\n\ts_cbranch_scc1 9f\n\ts_waitcnt vmcnt(0)\n9:" :: "v"(a), "v"(b), "v"(c), "v"(d), [em] "s"(em) : "memory", "scc");
  }
};
struct DrainGate {
  __device__ __forceinline__ void pass() { BARRIER; WAITV(0); }
  __device__ __forceinline__ void done() {}
};
struct WaitHook : NoHook {
  static constexpr int AUX = 16;
  static constexpr bool PIN = true;
  static constexpr bool PREALL = true;
  __device__ __forceinline__ void ready() {
    BARRIER;
  }
};
struct WaitHookX : WaitHook {
  __device__ __forceinline__ void ready() { WAITV(0); BARRIER; BARRIER; }
};
struct WaitHookP : WaitHook { static constexpr bool PSW = true; };
template <class Base, int NBAR>
struct SyncHook : Base {
  static constexpr bool PREALL = true;
  __device__ __forceinline__ void first() {
    Base::first();
#pragma unroll
    for (int i = 0; i < NBAR; ++i) {
      BARRIER;
    }
    Base::afterr();
  }
};

template <int L, int MS, int NF, bool PERM = false, int FS = 1, bool HS = false>
struct FragAddr {
  static constexpr int NA = (L == KC) ? (MS == 16 ? 2 : 4) : (HS ? 2 * NF : NF);
  unsigned a[NA];
  __device__ __forceinline__ void init(LAS char* buf, int f0, int lane, int fx = 0) {
    const unsigned base = (unsigned)(size_t)buf;
    if constexpr (MS == 16) {
      const int fr = lane & 15, fq = lane >> 4, tq = (lane & 15) >> 2, tp = lane & 3;
      if constexpr (L == KC) {
        const int row = f0 * 16 + (PERM ? tq * 8 + tp : fr);
        a[0] = base + row * 128 + ((fq ^ (fr & 7)) * 16);
        a[1] = base + row * 128 + (((4 + fq) ^ (fr & 7)) * 16);
      } else {
        const int gl = ((tq >> 1) & 1) | ((fq & 1) << 1);
#pragma unroll
        for (int i = 0; i < NF; ++i) {
          const int cb = HS ? (i >> 1) * 4 + f0 + (i & 1) : (f0 + i * FS) ^ fx;
          if constexpr (PERM) a[i] = base + (fq * 8 + tq) * 128 + (cb >> 2) * 8192 + ((((cb & 2) * 2 + tp) ^ (gl * 2)) * 16) + ((cb ^ (tp >> 1)) & 1) * 8;
          else a[i] = base + (fq * 8 + tq) * 128 + tp * 8 + (cb >> 2) * 8192 + (((cb & 3) ^ gl) * 32);
          if constexpr (HS) { a[i] += (fq >> 1) * 512; a[NF + i] = a[i] ^ 512; }
        }
      }
    } else {
      const int r = lane & 31, h = lane >> 5, g = lane >> 4, tq = (lane & 15) >> 2, tp = lane & 3;
      if constexpr (L == KC) {
#pragma unroll
        for (int t = 0; t < 4; ++t) a[t] = base + (f0 * 32 + r) * 128 + (((2 * t + h) ^ ((r >> 1) & 7)) * 16);
      } else {
#pragma unroll
        for (int i = 0; i < NF; ++i) {
          const int cb = (f0 + i) ^ fx;
          a[i] = base + (8 * (g >> 1) + tq) * 128 + (cb >> 1) * 8192 + (((cb & 1) ^ ((tq >> 1) & 1)) * 64) + (g & 1) * 32 + tp * 8;
        }
      }
    }
#pragma unroll
    for (int i = 0; i < NA; ++i) asm volatile("" : "+v"(a[i]));
  }
  __device__ __forceinline__ bf16x8 read(int so, int i, int t) const {
    if constexpr (L == KC) {
      return *(const LAS bf16x8*)((LAS char*)(size_t)a[t] + (so + (PERM ? (i >> 1) * 4096 + (i & 1) * 512 : i * (MS * 128))));
    } else {
      LAS char* p = (LAS char*)(size_t)a[i] + (so + t * (MS == 16 ? 4096 : 2048));
      s16x4 lo = __builtin_amdgcn_ds_read_tr16_b64_v4i16((LAS s16x4*)(p));
      s16x4 hi = __builtin_amdgcn_ds_read_tr16_b64_v4i16((LAS s16x4*)(HS ? (LAS char*)(size_t)a[HS ? NF + i : i] + (so + t * 4096) : p + 512));
      return __builtin_shufflevector(lo, hi, 0, 1, 2, 3, 4, 5, 6, 7);
    }
  }
};

template <int NM, int PER>
__device__ __forceinline__ void interleave() {
#pragma unroll
  for (int i = 0; i < NM; ++i) {
    __builtin_amdgcn_sched_group_barrier(0x8, 1, 0);
    __builtin_amdgcn_sched_group_barrier(0x100, PER, 0);
  }
}

template <int PL, int QL, int FP, int FQ, int NS, int PSTG, int QSTG, int NT, bool SPLIT, class G, class Late, class Mid, class Tick>
__device__ __forceinline__ void consumer16w(LAS char* pbuf, LAS char* qbuf, f32x4 (&acc)[FP][FQ], int fp0, int fq0, int lane, G& gate, Late&& late, Mid&& mid, Tick&& tick) {
  FragAddr<PL, 16, FP, true> pa; pa.init(pbuf, fp0, lane);
  FragAddr<QL, 16, FQ, false, 1, QL == KM && HSW> qa; qa.init(qbuf, fq0, lane);
  struct Fr { bf16x8 p[2][FP]; bf16x8 q[2][FQ]; };
  auto rdt = [&](Fr& f, int slot, int t) {
#pragma unroll
    for (int i = 0; i < FP; ++i) f.p[t][i] = pa.read(slot * PSTG, i, t);
#pragma unroll
    for (int j = 0; j < FQ; ++j) f.q[t][j] = qa.read(slot * QSTG, j, t);
  };
  auto rd = [&](Fr& f, int slot) { rdt(f, slot, 0); rdt(f, slot, 1); };
  auto mmr = [&](const Fr& f, int j0, int j1, int t0 = 0, int t1 = 2) {
#pragma unroll
    for (int t = t0; t < t1; ++t)
#pragma unroll
      for (int j = j0; j < j1; ++j)
#pragma unroll
        for (int ii = 0; ii < FP; ++ii) { const int i = (j & 1) ? FP - 1 - ii : ii; acc[i][j] = __builtin_amdgcn_mfma_f32_16x16x32_bf16(f.p[t][i], f.q[t][j], acc[i][j], 0, 0, 0); }
  };
  constexpr int U = NS;
  constexpr int NG = (NT - 2 - SPLIT) / U, REM = (NT - 1) % U;
  Fr f[2];
  auto step = [&](auto uc) {
    constexpr int u = (decltype(uc)::value + 1) % U;
    SCHED0; WAITL0; BARRIER; SCHED0;
    rd(f[(u + 1) & 1], (u + 1) % NS);
    mmr(f[u & 1], 0, FQ);
    interleave<2 * FP * FQ, FQ < 4 ? 2 : 1>();
  };
  if constexpr (FQ < 4) {
    gate.pass();
    late();
  }
#pragma unroll
  for (int i = 0; i < FP; ++i)
#pragma unroll
    for (int j = 0; j < FQ; ++j) asm volatile("" : "+v"(acc[i][j]));
  if constexpr (FQ == 4) gate.pass();
  BARRIER;
  rdt(f[0], 0, 0);
  SCHED0;
  rdt(f[0], 0, 1);
  SCHED0;
  mmr(f[0], 0, FQ, 0, 1);
  SCHED0; gate.done(); WAITL0; BARRIER; SCHED0;
  rd(f[1], 1);
  mmr(f[0], 0, FQ, 1, 2);
  interleave<FP * FQ, FQ < 4 ? 3 : 2>();
#pragma unroll 1
  for (int g = 0; g < NG; ++g) static_for<U>(step);
  if constexpr (!SPLIT) {
    static_for<REM - 1>(step);
    SCHED0; WAITL0; BARRIER; SCHED0;
    if constexpr (FQ < 4) {
      mmr(f[REM & 1], 0, 1);
      mmr(f[REM & 1], 1, 2);
    } else mid([&](int n0, int n1) {
#pragma unroll
      for (int n = n0; n < n1; ++n) {
        const int t = (n >> 2) & 1, i = (n >> 4) * 2 + (n & 1), j = ((n >> 3) & 1) * 2 + ((n >> 1) & 1);
        acc[i][j] = __builtin_amdgcn_mfma_f32_16x16x32_bf16(f[REM & 1].p[t][i], f[REM & 1].q[t][j], acc[i][j], 0, 0, 0);
      }
    });
  } else {
    static_for<(NT - 3) % U>(step);
    SCHED0; WAITL0; BARRIER; SCHED0;
    rd(f[REM & 1], REM % NS);
    mmr(f[(REM - 1) & 1], FQ / 2, FQ);
    interleave<FP * FQ, 2>();
    SCHED0; WAITL0; BARRIER; SCHED0;
    auto m1 = [&](const Fr& g, int t, int i, int j) { acc[i][j] = __builtin_amdgcn_mfma_f32_16x16x32_bf16(g.p[t][i], g.q[t][j], acc[i][j], 0, 0, 0); };
#pragma unroll
    for (int i = 0; i <= FP; ++i) {
      SCHED0;
#pragma unroll
      for (int n = 0; n < FQ; ++n) {
        if (i < FP) m1(f[REM & 1], n / (FQ / 2), i, FQ / 2 + n % (FQ / 2));
        else m1(f[(REM - 1) & 1], 0, n, 0);
      }
      if (i > 0) {
        mid(i - 1);
        __builtin_amdgcn_sched_group_barrier(0x8, 2, 0);
        __builtin_amdgcn_sched_group_barrier(0x2, 2, 0);
        __builtin_amdgcn_sched_group_barrier(0x8, 1, 0);
        __builtin_amdgcn_sched_group_barrier(0x2, 2, 0);
        __builtin_amdgcn_sched_group_barrier(0x8, 1, 0);
        __builtin_amdgcn_sched_group_barrier(0x2, 1, 0);
        __builtin_amdgcn_sched_group_barrier(0x40, 1, 0);
      }
    }
    SCHED0;
#pragma unroll
    for (int i = 0; i < FP; ++i) m1(f[(REM - 1) & 1], 1, i, 0);
    mmr(f[(REM - 1) & 1], 1, FQ / 2);
    static_for<2 * KD + 1>([&](auto kc) {
      constexpr int k = decltype(kc)::value, n = NT - 1 + k;
      SCHED0;
      if constexpr (k > 0) { tick(k - 1); WAITL0; BARRIER; SCHED0; }
      if constexpr (k < 2 * KD) {
#pragma unroll
        for (int t = 0; t < 2; ++t) {
#pragma unroll
          for (int i = 0; i < FP; ++i) f[(n + 1) & 1].p[t][i] = pa.read(((n + 1) % NS) * PSTG, i, t);
#pragma unroll
          for (int j = 0; j < FQ / 2; ++j) f[(n + 1) & 1].q[t][j] = qa.read(((n + 1) % NS) * QSTG, j, t);
        }
      } else late();
      mmr(f[n & 1], 0, FQ / 2);
      if constexpr (k < 2 * KD) interleave<FP * FQ, 2>();
    });
    SCHED0;
    tick(2 * KD);
  }
}

template <int NQT, int NPT, int RQ, int RP>
__device__ __forceinline__ void tile_map(unsigned BID, int& tq, int& tp) {
  static_assert((NQT / RQ) * (NPT / RP) == 8 && NQT % RQ == 0 && NPT % RP == 0, "8 regions");
  const int x = BID & 7, j = BID >> 3;
  constexpr int GP = NPT / RP;
  tq = (x / GP) * RQ + j % RQ;
  tp = (x % GP) * RP + j / RQ;
}

__device__ __forceinline__ void k4_map(unsigned BID, int& kh, int& tp, int& tq) {
  const int x8 = BID & 7, j8 = BID >> 3;
  kh = x8 & 1; tp = ((x8 >> 1) & 1) * 4 + (j8 >> 3); tq = (x8 >> 2) * 8 + (j8 & 7);
}

constexpr int K1_NS = 3, K1_PSTG = 192 * 128, K1_QSTG = 144 * 128;
constexpr int K1_QOFF = L_B, K1_ST = L_D;

__device__ __forceinline__ float max2(float a, float b) { return __builtin_elementwise_maximum(a, b); }
__device__ __forceinline__ float max3(float a, float b, float c) { return max2(max2(a, b), c); }
__device__ __forceinline__ float rows_sum(float v) {
  auto a = __builtin_amdgcn_permlane16_swap(__float_as_uint(v), __float_as_uint(v), false, false);
  v = __uint_as_float(a[0]) + __uint_as_float(a[1]);
  auto b = __builtin_amdgcn_permlane32_swap(__float_as_uint(v), __float_as_uint(v), false, false);
  return __uint_as_float(b[0]) + __uint_as_float(b[1]);
}

__device__ __forceinline__ float half_sum(float v) {
  auto b = __builtin_amdgcn_permlane32_swap(__float_as_uint(v), __float_as_uint(v), false, false);
  return __uint_as_float(b[0]) + __uint_as_float(b[1]);
}
__device__ __forceinline__ f32x2 ln_coef(float sm, float sq) {
  const float mean = sm * (1.0f / D_);
  const float var = fmaxf(__builtin_fmaf(-mean, mean, sq * (1.0f / D_)), 0.f);
  const float rs = __builtin_amdgcn_rcpf(rbf(__builtin_amdgcn_sqrtf(rbf(rbf(var) + 1e-5f))));
  return f32x2{rs, -rbf(mean) * rs};
}

constexpr u64 win_mask(int r, int lo, int hi) {
  u64 m = 0;
  for (int l = 0; l < 64; ++l) { const int d = 4 * (l >> 4) + r - (l & 15); if (d >= lo && d <= hi) m |= 1ull << l; }
  return m;
}
template <int R> __device__ __forceinline__ void win_bias(unsigned ninf, float& a, float& b) {
  constexpr u64 ma = ~win_mask(R, 9, 64), mb = ~win_mask(R, -7, 0);
  static_assert((unsigned)ma == ~0u, "mask");
  asm volatile("s_mov_b32 vcc_lo, %3\n\ts_mov_b32 vcc_hi, %4\n\tv_cndmask_b32 %1, 0, %2, vcc\n\t"
               "s_mov_b32 vcc_lo, -1\n\ts_mov_b32 vcc_hi, %5\n\tv_cndmask_b32 %0, 0, %2, vcc"
               : "=&v"(a), "=&v"(b) : "v"(ninf), "i"((int)mb), "i"((int)(mb >> 32)), "i"((int)(ma >> 32)) : "vcc");
}

template <class Pre>
__device__ __forceinline__ void k1_consumer(LAS char* lds, int lane, int wv, bool l0, int tq, const char* ob, Gate& gate, Pre&& pre) {
  u64 qm = l0 ? 0x0001000100010001ull : 0ull;
  asm volatile("" : "+v"(lane), "+s"(ob), "+s"(qm));
  const int p = wv & 1;
  f32x4 acc[6][4], hk, hv, hq;
  FragAddr<KM, 16, 3, false, 4> pa[2];
  pa[0].init(lds + L_A, wv, lane); pa[1].init(lds + L_A, wv ^ 1, lane);
  FragAddr<KC, 16, 4> qa, ha;
  qa.init(lds + K1_QOFF, 1 + 4 * p, lane);
#pragma unroll
  for (int t = 0; t < 2; ++t) { ha.a[t] = (qa.a[t] - (1 + 4 * p) * 2048) ^ ((1 - p) * 1024); asm volatile("" : "+v"(ha.a[t])); }
  struct Fr { bf16x8 p[6], q[4], h; };
  auto rd = [&](Fr& f, int slot, int t) {
    f.q[0] = qa.read(slot * K1_QSTG, 0, t);
#pragma unroll
    for (int i = 0; i < 6; ++i) f.p[i] = pa[i & 1].read(slot * K1_PSTG, i >> 1, t);
#pragma unroll
    for (int j = 1; j < 4; ++j) f.q[j] = qa.read(slot * K1_QSTG, j, t);
    f.h = ha.read(slot * K1_QSTG, 0, t);
  };
  auto m1 = [&](const Fr& f, int n) {
    const int j = n / 6, i = (j & 1) ? 5 - n % 6 : n % 6;
    acc[i][j] = i < 4 ? __builtin_amdgcn_mfma_f32_16x16x32_bf16(f.p[i], f.q[j], acc[i][j], 0, 0, 0) : __builtin_amdgcn_mfma_f32_16x16x32_bf16(f.q[j], f.p[i], acc[i][j], 0, 0, 0);
  };
  auto mh = [&](const Fr& f) {
    hk = __builtin_amdgcn_mfma_f32_16x16x32_bf16(f.p[2], f.h, hk, 0, 0, 0);
    hv = __builtin_amdgcn_mfma_f32_16x16x32_bf16(f.h, f.p[4], hv, 0, 0, 0);
  };
  auto mm = [&](const Fr& f) {
#pragma unroll
    for (int n = 0; n < HQP; ++n) m1(f, n);
    interleave<HQP, 1>();
    SCHED0;
    if (l0) hq = __builtin_amdgcn_mfma_f32_16x16x32_bf16(f.p[0], f.h, hq, 0, 0, 0);
    SCHED0;
#pragma unroll
    for (int n = HQP; n < 24; ++n) m1(f, n);
    mh(f);
    SCHED0;
  };
  const f32x4 z4 = {0.f, 0.f, 0.f, 0.f};
#pragma unroll
  for (int i = 0; i < 6; ++i)
#pragma unroll
    for (int j = 0; j < 4; ++j) { acc[i][j] = z4; asm volatile("" : "+v"(acc[i][j])); }
  hk = z4; hv = z4; hq = z4;
  asm volatile("" : "+v"(hk), "+v"(hv), "+v"(hq));
  Fr f0, f1;
  gate.pass();
  pre();
  BARRIER;
  rd(f0, 0, 0);
  SCHED0;
  gate.done(); WAITL0; SCHED0;
#pragma unroll 1
  for (int g = 0; g < 5; ++g)
    static_for<3>([&](auto uc) {
      constexpr int u = decltype(uc)::value;
      rd(f1, u, 1);
      mm(f0);
      if constexpr (u == 1) WAITV(0);
      WAITL0; BARRIER; SCHED0;
      rd(f0, (u + 1) % 3, 0);
      mm(f1);
      WAITL0;
      SCHED0;
    });
  constexpr int L_HX = L_B + 57344;
  asm volatile("" : "+v"(lane));
  const int fr = lane & 15, fq16 = lane & 48;
  unsigned dw = L_HX + wv * 2048 + (lane & 63) * 16, cq0 = L_CSQ + wv * 64 + fq16;
  f1.h = ha.read(0, 0, 1);
#pragma unroll
  for (int i = 0; i < 5; ++i) { const int k = (2 * i + 2) % 6 + (i >= 3); f1.p[k] = pa[k & 1].read(0, k >> 1, 1); }
  mh(f0);
#pragma unroll
  for (int n = 0; n < 9; ++n) m1(f0, n);
#pragma unroll
  for (int n = 0; n < 11; ++n) { __builtin_amdgcn_sched_group_barrier(0x8, 1, 0); __builtin_amdgcn_sched_group_barrier(0x100, 1, 0); __builtin_amdgcn_sched_group_barrier(0x2, 1, 0); }
  asm volatile("" : "+v"(dw), "+v"(cq0));
  SCHED0;
  mh(f1);
  if (l0) hq = __builtin_amdgcn_mfma_f32_16x16x32_bf16(f1.p[0], f1.h, __builtin_amdgcn_mfma_f32_16x16x32_bf16(f0.p[0], f0.h, hq, 0, 0, 0), 0, 0, 0);
  SCHED0;
  f1.p[1] = pa[1].read(0, 0, 1);
  f1.q[0] = qa.read(0, 0, 1);
#pragma unroll
  for (int n = 9; n < 12; ++n) m1(f0, n);
  interleave<3, 1>();
  SCHED0;
  *(LAS f32x4*)(size_t)dw = hk;
  m1(f0, 12); m1(f0, 13);
  SCHED0;
  *(LAS f32x4*)((LAS char*)(size_t)dw + 1024) = hv;
  m1(f0, 14); m1(f0, 15);
  SCHED0;
  asm volatile("s_mov_b64 exec, %2\n\tds_write_b128 %0, %1\n\ts_mov_b64 exec, -1" ::"v"(cq0), "v"(hq), "s"(qm) : "memory");
  SCHED0;
  unsigned a2 = K1_ST + (16 + 64 * p) * 8 + fr * 8, a4 = L_RS + (16 + 64 * p) * 4 + fq16;
  unsigned h2 = K1_ST + ((fr ^ ((1 - p) * 8)) * 8), h4 = L_RS + (fq16 ^ ((1 - p) * 32));
  unsigned ma = L_MSK + (lane & 63) * 16, ck0 = L_HX + wv * 2048 + fq16 * 16, cv0 = L_HX + 1024 + wv * 2048 + fr * 16;
#pragma unroll
  for (int j = 1; j < 4; ++j) f1.q[j] = qa.read(0, j, 1);
#pragma unroll
  for (int n = 16; n < 24; ++n) m1(f0, n);
#pragma unroll
  for (int n = 0; n < 8; ++n) { __builtin_amdgcn_sched_group_barrier(0x8, 1, 0); __builtin_amdgcn_sched_group_barrier(0x100, 1, 0); __builtin_amdgcn_sched_group_barrier(0x2, 2, 0); }
  asm volatile("" : "+v"(a2), "+v"(a4), "+v"(h2), "+v"(h4), "+v"(ma), "+v"(ck0), "+v"(cv0));
  SCHED0; WAITL0; BARRIER; SCHED0;
  unsigned oo[4];
  {
    const bool hole = ((p + (fr & 14)) & 7) == 3;
    oo[0] = phys<64>(p + (fr & 14)) * 128 + (fr & 1) * 64 + (((lane & 16) * 2) ^ (p * 32)) + ((lane & 32) >> 1);
    const unsigned e1 = hole ? 8 : 11, e2 = hole ? 10 : 12;
    oo[1] = oo[0] + (1u << e1); oo[2] = oo[0] + (1u << e2); oo[3] = oo[2] + (1u << e1);
  }
  f32x2 s2[5];
  f32x4 rs[5], mr[5], cs[4], pk_, pv_, bA, bB, b0;
  float cv[2];
  bA = *(const LAS f32x4*)(size_t)ma;
  cs[0] = *(const LAS f32x4*)(size_t)cq0; cs[1] = *(const LAS f32x4*)(size_t)(cq0 ^ 64);
  cs[2] = *(const LAS f32x4*)(size_t)ck0; cs[3] = *(const LAS f32x4*)(size_t)(ck0 ^ 2048);
#pragma unroll
  for (int j = 0; j < 5; ++j) s2[j] = j < 4 ? *(const LAS f32x2*)((LAS char*)(size_t)a2 + j * 128) : *(const LAS f32x2*)(size_t)h2;
  pk_ = *(const LAS f32x4*)(size_t)(dw ^ (2048 + 128)); pv_ = *(const LAS f32x4*)(size_t)(dw ^ (2048 + 1024 + 512));
  bB = *(const LAS f32x4*)((LAS char*)(size_t)ma + 1024);
  cv[0] = *(const LAS float*)(size_t)cv0; cv[1] = *(const LAS float*)(size_t)(cv0 ^ 2048);
#pragma unroll
  for (int j = 0; j < 5; ++j) {
    rs[j] = j < 4 ? *(const LAS f32x4*)((LAS char*)(size_t)a4 + j * 64) : *(const LAS f32x4*)(size_t)h4;
    mr[j] = j < 4 ? *(const LAS f32x4*)((LAS char*)(size_t)a4 + j * 64 + 576) : *(const LAS f32x4*)((LAS char*)(size_t)h4 + 576);
  }
  const unsigned nm = (unsigned)((tq - 1) >> 31) & 0xFF61B1E6u;
  int o1 = OPQ(0x3F803F80);
  bf16x8 ones = __builtin_bit_cast(bf16x8, u32x4{(unsigned)o1, (unsigned)o1, (unsigned)o1, (unsigned)o1});
#pragma unroll
  for (int n = 0; n < 24; ++n) m1(f1, n);
#pragma unroll
  for (int n = 0; n < 24; ++n) { __builtin_amdgcn_sched_group_barrier(0x8, 1, 0); __builtin_amdgcn_sched_group_barrier(0x2, 1, 0); __builtin_amdgcn_sched_group_barrier(0x100, 1, 0); __builtin_amdgcn_sched_group_barrier(0x2, 1, 0); }
  asm volatile("" : "+v"(oo[0]), "+v"(oo[1]), "+v"(oo[2]), "+v"(oo[3]), "+v"(ones));
  SCHED0;
  __builtin_amdgcn_s_waitcnt(0xCA7F);
  ALIGN8;
  auto qk = [&](const f32x4& a, const f32x4& c, const f32x2& s) { const f32x4 v = a * s[0] + c * s[1]; return u32x2{pk2(v[0], v[1]), pk2(v[2], v[3])}; };
  auto cat = [](const u32x2& a, const u32x2& b) { return __builtin_bit_cast(bf16x8, u32x4{a[0], a[1], b[0], b[1]}); };
  bf16x8 kf[5], qf[4];
#pragma unroll
  for (int j = 0; j < 4; ++j) {
    kf[j + 1] = cat(qk(acc[2][j], cs[2], s2[j]), qk(acc[3][j], cs[3], s2[j]));
    qf[j] = cat(qk(acc[0][j], cs[0], s2[j]), qk(acc[1][j], cs[1], s2[j]));
  }
  kf[0] = cat(qk(hk, cs[2], s2[4]), qk(pk_, cs[3], s2[4]));
  SCHED0;
  const float scale = 0.17677669529663687f, l2e = 1.4426950408889634f;
  f32x2 l[4][4];
  float mx[4];
#pragma unroll
  for (int r = 0; r < 4; ++r) b0[r] = __uint_as_float(__float_as_uint(bA[r]) | nm);
#pragma unroll
  for (int jj = 0; jj < 4; ++jj) {
    const int j = (jj + 1) & 3;
    const f32x4 sA = __builtin_amdgcn_mfma_f32_16x16x32_bf16(kf[j], qf[j], j == 0 ? b0 : bA, 0, 0, 0);
    const f32x4 sB = __builtin_amdgcn_mfma_f32_16x16x32_bf16(kf[j + 1], qf[j], bB, 0, 0, 0);
    l[j][0] = f32x2{sA[0], sA[1]}; l[j][1] = f32x2{sA[2], sA[3]}; l[j][2] = f32x2{sB[0], sB[1]}; l[j][3] = f32x2{sB[2], sB[3]};
    mx[j] = max3(max3(l[j][0][0], l[j][0][1], l[j][1][0]), max3(l[j][1][1], l[j][2][0], l[j][2][1]), max2(l[j][3][0], l[j][3][1]));
  }
  __builtin_amdgcn_sched_group_barrier(0x8, 2, 0);
#pragma unroll
  for (int n = 0; n < 6; ++n) { __builtin_amdgcn_sched_group_barrier(0x2, 2, 0); __builtin_amdgcn_sched_group_barrier(0x8, 1, 0); }
  SCHED0;
  WAITL0;
  ALIGN8;
  u32x12 vp[2];
  auto vv = [&](int e, int k, const f32x4& a, int j) { const f32x4 v = a * rs[j] + mr[j] * cv[e]; vp[e][2 * k] = pk2(v[0], v[1]); vp[e][2 * k + 1] = pk2(v[2], v[3]); };
  asm volatile("" : "=v"(vp[0]), "=v"(vp[1]));
  vv(0, 0, hv, 4); vv(1, 0, pv_, 4);
#pragma unroll
  for (int j = 0; j < 4; ++j) { vv(0, j + 1, acc[4][j], j); vv(1, j + 1, acc[5][j], j); }
  {
    auto mx2 = [](auto r) { return __float_as_uint(max2(__uint_as_float(r[0]), __uint_as_float(r[1]))); };
    const unsigned a = mx2(__builtin_amdgcn_permlane16_swap(__float_as_uint(mx[0]), __float_as_uint(mx[1]), false, false));
    const unsigned b = mx2(__builtin_amdgcn_permlane16_swap(__float_as_uint(mx[2]), __float_as_uint(mx[3]), false, false));
    const unsigned c = __float_as_uint(__uint_as_float(mx2(__builtin_amdgcn_permlane32_swap(a, b, false, false))) * (-scale * l2e));
    const auto d = __builtin_amdgcn_permlane32_swap(c, c, false, false);
    const auto e0 = __builtin_amdgcn_permlane16_swap(d[0], d[0], false, false), e1 = __builtin_amdgcn_permlane16_swap(d[1], d[1], false, false);
    mx[0] = __uint_as_float(e0[0]); mx[1] = __uint_as_float(e0[1]); mx[2] = __uint_as_float(e1[0]); mx[3] = __uint_as_float(e1[1]);
  }
#pragma unroll
  for (int j = 0; j < 4; ++j)
#pragma unroll
    for (int e = 0; e < 4; ++e) l[j][e] = __builtin_elementwise_fma(l[j][e], f32x2{scale * l2e, scale * l2e}, f32x2{mx[j], mx[j]});
  asm volatile("" : "+v"(vp[0]), "+v"(vp[1]));
  SCHED0;
  f32x4 oA[4], oB[4], zs[4];
  bf16x8 pf[4];
  auto ex = [&](int j) {
    u32x4 pp;
#pragma unroll
    for (int e = 0; e < 4; ++e) pp[e] = pk2(__builtin_amdgcn_exp2f(l[j][e][0]), __builtin_amdgcn_exp2f(l[j][e][1]));
    pf[j] = __builtin_bit_cast(bf16x8, pp);
  };
  auto pv = [&](auto jc) {
    constexpr int j = decltype(jc)::value;
    zs[j] = __builtin_amdgcn_mfma_f32_16x16x32_bf16(ones, pf[j], z4, 0, 0, 0);
    oA[j] = __builtin_amdgcn_mfma_f32_16x16x32_bf16(__builtin_bit_cast(bf16x8, __builtin_shufflevector(vp[0], vp[0], 2 * j, 2 * j + 1, 2 * j + 2, 2 * j + 3)), pf[j], z4, 0, 0, 0);
    oB[j] = __builtin_amdgcn_mfma_f32_16x16x32_bf16(__builtin_bit_cast(bf16x8, __builtin_shufflevector(vp[1], vp[1], 2 * j, 2 * j + 1, 2 * j + 2, 2 * j + 3)), pf[j], z4, 0, 0, 0);
  };
  auto out = [&](int j) {
    const float iz = __builtin_amdgcn_rcpf(zs[j][0]);
    asm volatile("" ::"v"(zs[j]));
    const f32x4 a = oA[j] * iz, b = oB[j] * iz;
    const auto x0 = __builtin_amdgcn_permlane16_swap(pk2(a[0], a[1]), pk2(b[0], b[1]), false, false);
    const auto x1 = __builtin_amdgcn_permlane16_swap(pk2(a[2], a[3]), pk2(b[2], b[3]), false, false);
    const u32x4 ov = {x0[0], x1[0], x0[1], x1[1]};
    asm volatile("global_store_dwordx4 %0, %1, %2 sc1\n\ts_nop 1" ::"v"(oo[j]), "v"(ov), "s"(ob) : "memory");
    SCHED0;
  };
  ex(0);
  SCHED0;
  static_for<3>([&](auto jc) {
    constexpr int j = decltype(jc)::value;
    pv(jc);
    ex(j + 1);
#pragma unroll
    for (int n = 0; n < 3; ++n) { __builtin_amdgcn_sched_group_barrier(0x8, 1, 0); __builtin_amdgcn_sched_group_barrier(0x400, 1, 0); }
    SCHED0;
    if constexpr (j == 2) { pv(std::integral_constant<int, 3>{}); SCHED0; }
    out(j);
  });
  out(3);
}

constexpr int L_FLG = L_A + 65536;
template <int OFF> __device__ __forceinline__ void fetch_flags(const void* p) {
  asm volatile("s_mov_b32 m0, %1\n\ts_nop 0\n\tglobal_load_lds_dwordx4 %0, off sc1" ::"v"(p), "i"(L_FLG + OFF) : "memory", "m0");
}
__device__ __forceinline__ bool flags_set(unsigned a, u64 tag) {
  u32x4 v;
  asm volatile("ds_read_b128 %0, %1\n\ts_waitcnt lgkmcnt(0)" : "=v"(v) : "v"(a) : "memory");
  const unsigned lo = (unsigned)tag, hi = (unsigned)(tag >> 32);
  return __builtin_amdgcn_ballot_w64((((v[0] ^ lo) | (v[1] ^ hi)) | ((v[2] ^ lo) | (v[3] ^ hi))) != 0) == 0;
}
template <int N>
__device__ __forceinline__ void wait_tags(const u64* p, u64 tag) {
#pragma unroll 1
  for (int spin = 0; spin < (1 << 16); ++spin) {
    bool ok = true;
#pragma unroll
    for (int i = 0; i < N; ++i) ok &= __hip_atomic_load(p + i, __ATOMIC_RELAXED, __HIP_MEMORY_SCOPE_AGENT) == tag;
    if (__all(ok)) break;
    __builtin_amdgcn_s_sleep(8);
  }
  CFENCE;
}
__device__ __forceinline__ const u64* first_flags(const u64* ctl0, int lane) {
  OPAQUE_BID;
  return ctl0 + C_OCNT + ((lane >> 2) * NR_O + (BID & 3) % NR_O) * RS1 + (lane & 3) * 4;
}
__device__ __forceinline__ void wait_first_phase(LAS char* lds, const u64* ctl0, int lane) { wait_tags<4>(first_flags(ctl0, lane), TAG); }
__device__ __forceinline__ const u64* x_flag(const u64* ctl, int tq, int tp, int lane) {
  return ctl + C_XCNT + (max(tq - ((lane >> 4) & 1), 0) * NR_X + ((tp + ((lane >> 4) & 1) * (NR_X / 2)) % NR_X)) * RS1 + (lane & 15);
}

__device__ __forceinline__ f32x2 uadd(f32x2 a, f32x2 b) {
  float x, y;
  asm("v_add_f32 %0, %2, %4\n\tv_add_f32 %1, %3, %5" : "=&v"(x), "=v"(y) : "v"(a[0]), "v"(a[1]), "v"(b[0]), "v"(b[1]));
  return f32x2{x, y};
}
#define SF1 5
#define SCW 13
#define SCO (SCW < 12 ? SCW + 3 : 99)
template <bool ISQ>
struct K1Hook : NoHook {
  static constexpr int AUX = ISQ ? 16 : 0;
  Stream<KM, 16, 64> so;
  static constexpr bool PREALL = true, PIN = ISQ, UNROLL = true, COLD = true;
  __device__ __forceinline__ bool cold() const { return l0; }
  static constexpr int SG = SF1 + 3;
  static constexpr int NG = ISQ ? 4 : 1;
  bool l0; int tq, tp, lw, lane; LAS char* lds;
  const u32x2* pstat; u64* ctl; u32x4* xch;
  const u16* wo; const u16* w1; const u16* w2; const u16* wq;
  const u32x2* sp; unsigned sa; const char* tw;
  f32x2 g[4 * NG];
  u64 fx; const u64* fp;
  __device__ __forceinline__ void ready() {
    if constexpr (ISQ) { if (!l0) { BARRIER; BARRIER; } }
  }
  __device__ __forceinline__ void first() {
    asm volatile("" : "+v"(lane));
    if constexpr (ISQ) {
      OPAQUE_BID;
      const int b = BID, l24 = lane % 24, r = l24 / 12, c = l24 - r * 12;
      tw = (const char*)wq + (256 + (((b >> 2) & 1) * 32 + (b >> 3)) * 4 + (lw - 2) * 2 + r) * 6144 + (c >> 2) * 2048 + ((b & 3) * 4 + (c & 3)) * 128;
      asm volatile("" : "+v"(tw));
    }
  }
  static constexpr int ng(int s) { return (s >= SG && s < SG + 4) ? NG : 0; }
  static constexpr int tstep(int i) { return i == 3 ? 3 : 0; }
  static constexpr int nt(int s) { int n = 0; for (int i = 0; i < 4; ++i) n += ISQ && tstep(i) == s; return n; }
  static constexpr int gs(int s) { return (!ISQ && s >= G0 && (s - G0) % GD == 0 && (s - G0) / GD < GN) ? (s - G0) / GD : -1; }
  int grp, duty; unsigned go;
  static constexpr int lx(int s) { return nt(s) + (!ISQ && s == FWS ? 1 : 0); }
  template <int S, int N> __device__ __forceinline__ void wait() {
    constexpr int A = N + (S < 13 ? ng(S - 1) + ng(S - 2) : 0), X = lx(S - 1) + lx(S - 2);
    if constexpr (S < 13 && (gs(S - 1) >= 0 || gs(S - 2) >= 0)) {
      if (grp == gs(S - 1) || grp == gs(S - 2)) waitv<A + X + 4>();
      else if (X > 0 && l0) waitv<A + X>();
      else waitv<A>();
    } else if constexpr (S == SCW + 1 || S == SCW + 2) {
      static_assert(X == 0, "copy step");
      if (duty >= 0) waitv<A + 4>(); else waitv<A>();
    } else {
      if (X > 0 && l0) waitv<A + X>(); else waitv<A>();
    }
  }
  template <int I> __device__ __forceinline__ f32x2 sum4() {
    asm volatile("" : "+v"(g[I]), "+v"(g[I + 1]), "+v"(g[I + 2]), "+v"(g[I + 3]));
    return uadd(uadd(g[I], g[I + 1]), uadd(g[I + 2], g[I + 3]));
  }
  template <int S> __device__ __forceinline__ void stepc() {
    if constexpr (nt(S) > 0) {
      if (l0) {
#pragma unroll
        for (int i = 0; i < 3; ++i) if (tstep(i) == S) touch_line(tw + i * 256 * 6144, (unsigned)(size_t)lds + L_SCR);
        if (tstep(3) == S) touch_line((const char*)wo + (size_t)((opq_bid() * 2 + lw - 2) * 32 + (lane & 31)) * 128, (unsigned)(size_t)lds + L_SCR);
      }
    }
    if constexpr (!ISQ && PF2 && S == 1) {
      OPAQUE_BID;
      int tq2, tp2;
      tile_map<16, 16, 2, 16>(BID, tq2, tp2);
      so.init(Operand{wo, D_, tp2 * 64, 64, D_ * D_ * 2, 0, false, nullptr, 0}, lw, lane);
      so.pin();
    }
    if constexpr (!ISQ && PF2 && S == WOT) {
      if (lw == 1) {
        OPAQUE_BID;
        const int cb0 = (lane >> 5) * 1024, cf = (3 - (int)(((size_t)wo + cb0) >> 7)) & 7;
        touch_line((const char*)wo + (size_t)((BID >> 3) * 32 + (lane & 31)) * (D_ * 2) + cb0 + cf * 128, (unsigned)(size_t)lds + L_SCR);
      }
    }
    if constexpr (S == (ISQ ? 4 : 2)) {
      asm volatile("" : "+v"(lane));
      const int trow = ISQ ? (lw - 2) * 64 + lane : 128 + (lane & 15);
      const int row = max(tq * 128 - 16 + trow, 0), line = (trow >> 1) + (trow < 16 ? (trow & 1) * 8 : 8 + (trow & 1) * 64);
      sp = pstat + (size_t)(row >> 5) * 512 + (row & 31) + (ISQ ? 0 : (lane >> 4) * 128);
      sa = (unsigned)(size_t)lds + K1_ST + line * 8;
      asm volatile("" : "+v"(sp), "+v"(sa));
    }
    if constexpr (!ISQ && S == ARMS) {
      if (lw == 0) { const unsigned m = vconst<~0u>(); st16(xch + ((tq * 4 + (lane >> 4)) * 16 + tp) * 16 + (lane & 15), u32x4{m, m, m, m}); }
    }
    if constexpr (!ISQ && S == FWS) {
      if (l0) {
        static_assert(NR_O == 8 && NR_H == 8 && NR_U == 8 && NR_X == 8 && (RS1 == RS2 || RS2 == 2 * RS1), "flag lines");
        const int i = opq_bid() * 7 + min(lane, 6);
        constexpr int N1 = 1024 + (C_END - C_UCNT) / RS1;
        const int wd = i < 1024 ? i * RSP : i < N1 ? C_UCNT + (i - 1024) * RS1 : C_HCNT + 16 + min(i - N1, 63) * RS2;
        touch_line(ctl + lw * C_WORDS + wd, L_SCR);
      }
    }
    if constexpr (S == 0) {
      const int c = (3 - (int)((size_t)wq >> 7)) & 7;
      duty = l0 && (tp & 7) != c ? tq * 14 + tp - (tp > c) - (tp > c + 8) : -1;
      asm volatile("" : "+s"(duty));
    }
    if constexpr (!ISQ && S == 0) {
      grp = duty >= 0 ? (int)((opq_bid() >> 3) % GN) : 99;
      const unsigned line = (duty * 2 + lw) * 64 + lane;
      go = ((((unsigned)((size_t)w1 >> 7) + line) & 7) == 3 ? line ^ 1 : line) * 128;
      asm volatile("" : "+v"(go));
    }
    if constexpr (ISQ && S == SF1) {
      OPAQUE_BID;
      int tq2, tp2;
      tile_map<16, 16, 2, 16>(BID, tq2, tp2);
      fp = x_flag(ctl, lane < 32 ? tq : tq2, tp, lane < 32 ? lane : lane & 15);
      asm volatile("global_load_dwordx2 %0, %1, off sc1" : "=&v"(fx) : "v"(fp) : "memory");
    }
    if constexpr (gs(S) >= 0) {
      if (grp == gs(S)) {
        const char* b = (const char*)w1;
        asm volatile("s_mov_b32 m0, %5\n\ts_nop 0\n\tglobal_load_lds_dword %0, %1\n\tglobal_load_lds_dword %0, %2\n\tglobal_load_lds_dword %0, %3\n\tglobal_load_lds_dword %0, %4"
                     ::"v"(go), "s"(b), "s"(b + 28672 * 128), "s"(b + 2 * 28672 * 128), "s"(b + 3 * 28672 * 128), "i"(L_SCR) : "memory", "m0");
      }
    }
    if constexpr (ISQ && S == 1) { if (l0 && lw == 2 && lane < NR_X) flag_set(ctl + C_XCNT + (tq * NR_X + lane) * RS1 + tp, TAG); }
    if constexpr (S == SCO) copy_out();
    if constexpr (S == SCW) {
      if (duty >= 0) {
        const int q = (duty * 4 + lw) * 2, r8 = lane >> 3, c16 = (lane & 7) * 16;
        const auto r1 = __builtin_amdgcn_make_buffer_rsrc((void*)w1, 0, S_ * F_ * 2, 0x00020000);
        const auto r2 = __builtin_amdgcn_make_buffer_rsrc((void*)w2, 0, S_ * F_ * 2, 0x00020000);
        const int s1 = ((q & 255) * 8 * F_ + (((3 - (int)((size_t)w1 >> 7)) & 7) + 8 * (q >> 8)) * 64) * 2;
        const int s2 = ((q >> 2) * 8 * S_ + (((3 - (int)((size_t)w2 >> 7)) & 7) + 8 * (q & 3)) * 64) * 2;
        const unsigned v1 = r8 * (F_ * 2) + c16, v2 = r8 * (S_ * 2) + c16;
        LAS char* d = lds + L_C + lw * 4096;
#if defined(__HIP_DEVICE_COMPILE__)
        __builtin_amdgcn_raw_ptr_buffer_load_lds(r1, (LAS void*)d, 16, v1, s1, 0, AC);
        __builtin_amdgcn_raw_ptr_buffer_load_lds(r1, (LAS void*)(d + 1024), 16, v1, s1 + 8 * F_ * 2, 0, AC);
        __builtin_amdgcn_raw_ptr_buffer_load_lds(r2, (LAS void*)(d + 2048), 16, v2, s2, 0, AC);
        __builtin_amdgcn_raw_ptr_buffer_load_lds(r2, (LAS void*)(d + 3072), 16, v2, s2 + 1024, 0, AC);
#endif
      }
    }
    if constexpr (ISQ && S == SF1 + 2) {
      asm volatile("" : "+v"(fx));
      if (lw == 3 && !__all(fx == TAG)) wait_tags<1>(fp, TAG);
    }
    if constexpr (ng(S) == 4) {
      constexpr int i = (S - SG) * 4;
      asm volatile(
          "global_load_dwordx2 %0, %4, off offset:%5 sc1\n\tglobal_load_dwordx2 %1, %4, off offset:%5+256 sc1\n\t"
          "global_load_dwordx2 %2, %4, off offset:%5+512 sc1\n\tglobal_load_dwordx2 %3, %4, off offset:%5+768 sc1"
          : "=&v"(g[i]), "=&v"(g[i + 1]), "=&v"(g[i + 2]), "=&v"(g[i + 3]) : "v"(sp), "i"(i * 256) : "memory");
    }
    if constexpr (ng(S) == 1) asm volatile("global_load_dwordx2 %0, %1, off offset:%2 sc1" : "=&v"(g[S - SG]) : "v"(sp), "i"((S - SG) * 256) : "memory");
    if constexpr (ISQ) {
      if constexpr (S == SG + 3) { g[0] = sum4<0>(); asm volatile("" : "+v"(g[0])); }
      if constexpr (S == SG + 4) { g[0] = uadd(g[0], sum4<4>()); asm volatile("" : "+v"(g[0])); }
      if constexpr (S == SG + 5) { g[0] = uadd(g[0], uadd(sum4<8>(), sum4<12>())); asm volatile("" : "+v"(g[0])); }
    } else {
      if constexpr (S == SG + 5) { g[0] = sum4<0>(); float a = rows_sum(g[0][0]); asm volatile("" : "+v"(a)); g[0] = f32x2{a, rows_sum(g[0][1])}; asm volatile("" : "+v"(g[0])); }
    }
    if constexpr (S == SG + 6) {
      asm volatile("" : "+v"(g[0]));
      const f32x2 c = ln_coef(g[0][0], g[0][1]);
      asm volatile("ds_write_b64 %0, %1\n\tds_write2_b32 %2, %3, %4 offset1:144\n\ts_waitcnt lgkmcnt(0)" ::"v"(sa), "v"(c), "v"((sa >> 1) + (L_RS - K1_ST / 2)), "v"(c[0]), "v"(c[1]) : "memory");
    }
  }
  template <int T> __device__ __forceinline__ void next() {
    stepc<13 + T>();
    SCHED0;
    if constexpr (!ISQ && PF2) {
      constexpr int n0 = PF2 == 5 ? 0 : T == 1 ? 0 : 2, n1 = T == 3 ? 4 : T == 1 && PF2 != 5 ? 2 : 0;
#pragma unroll
      for (int i = n0; i < n1; ++i) so.template stage<0>(lds + L_A + 32768 + i * 8192 + lw * 1024);
      SCHED0;
    }
  }
  __device__ __forceinline__ void finish() {
    CFENCE;
    __builtin_amdgcn_s_waitcnt(0x0F70);
    CFENCE;
    if (SCO == 99) copy_out();
  }
  u16* cw;
  __device__ __forceinline__ void copy_out() {
    if (duty >= 0) {
      const int q = (OPQ(duty) * 4 + lw) * 2, r8 = lane >> 3, c16 = (lane & 7) * 16;
      const int k1 = (q & 7) * 8 + r8, k2 = ((q >> 3) & 7) * 8 + r8;
      const unsigned cd1 = (unsigned)((q >> 3) * CW_BLK + phys<64>(k1) * 128 + c16);
      const unsigned cd2 = (unsigned)((224 + (q & 3) * 56 + (q >> 6) * 2 + ((q >> 2) & 1)) * CW_BLK + phys<64>(k2) * 128 + c16);
      const unsigned d1 = pow2sel(r8 == 3, 7, 10);
      u32x4 cv[4];
      asm volatile("ds_read_b128 %0, %4\n\tds_read_b128 %1, %4 offset:1024\n\tds_read_b128 %2, %4 offset:2048\n\tds_read_b128 %3, %4 offset:3072\n\ts_waitcnt lgkmcnt(0)"
                   : "=&v"(cv[0]), "=&v"(cv[1]), "=&v"(cv[2]), "=&v"(cv[3]) : "v"(L_C + lw * 4096 + lane * 16) : "memory");
      asm volatile("s_nop 4\n\tglobal_store_dwordx4 %0, %4, %8 sc1\n\tglobal_store_dwordx4 %1, %5, %8 sc1\n\tglobal_store_dwordx4 %2, %6, %8 sc1\n\tglobal_store_dwordx4 %3, %7, %8 sc1\n\ts_nop 1"
                   ::"v"(cd1), "v"(cd1 + d1), "v"(cd2), "v"(cd2 + 56 * CW_BLK), "v"(cv[0]), "v"(cv[1]), "v"(cv[2]), "v"(cv[3]), "s"(cw) : "memory");
    }
  }
};
static_assert(SF1 + 3 + 5 == 13 && SCW != SF1 && SCO != SF1 && SCW != 11 && SCO != 11, "the fetches end two steps before the tail; statistics in LDS before the last barrier");

constexpr int K2_NSQ = 4, K2_NSP = 4;
__device__ __forceinline__ void k1_body(LAS char* lds, int wid, bool l0, const u16* __restrict__ w, const u16* x, u16* o, u32x4* xch, u64* ctl,
                                        u32x2* pstat, const u16* __restrict__ wo, const u16* __restrict__ w1,
                                        const u16* __restrict__ w2, u16* __restrict__ cw) {
  LAS char* pbuf = lds + L_A;
  LAS char* qbuf = lds + K1_QOFF;
  OPAQUE_BID;
  const int lane = lane_id();
  int tq, tp;
  tile_map<16, 16, 8, 4>(BID, tq, tp);
  const int s0 = tq * 128;
  if (wid >= 4) {
    auto fill = [&](auto& h) {
      h.l0 = l0; h.tq = tq; h.tp = tp; h.lw = wid - 4; h.lane = lane; h.lds = lds; h.pstat = pstat; h.ctl = ctl; h.xch = xch;
      h.wo = wo; h.w1 = w1; h.w2 = w2; h.wq = w; h.cw = cw;
    };
    if (wid >= 6) {
      if (lane < 8 * K1_NS) {
        const unsigned o1 = vconst<0x3F803F80u>();
        *(LAS u32x4*)(qbuf + (wid - 6) * 1024 + (lane >> 3) * K1_QSTG + (lane & 7) * 16) = u32x4{o1, o1, o1, o1};
      }
      WAITL0;
      K1Hook<true> hk;
      fill(hk);
      Operand Q{x, D_, s0 - 16, 64, S_ * D_ * 2, 0, false, nullptr, 0};
      g_loader<KC, 16, 144, K1_NS, 16, -1, true>(qbuf, Q, wid - 6, lane, hk);
      hk.finish();
    } else {
      Operand P{w, 3 * D_, tp * 64, D_, 3 * D_ * D_ * 2, 0, false, nullptr, 0};
      SlowColHook<K1Hook<false>, 3, 2 * D_, 4, 32, 16> hs;
      fill(hs);
      hs.setup(w, 3 * D_, (tp & ~3) * 128, 0, BID >> 3, lane, wid == 4, (unsigned)(size_t)lds + L_SCR);
      g_loader<KM, 16, 192, K1_NS, 16, 0, false>(pbuf, P, wid - 4, lane, hs, !l0 && ((((int)((size_t)w >> 7)) + tp) & 7) != 3);
      hs.template next<3>();
      hs.finish();
    }
  } else {
    Gate gate;
    {
      int kh, tp4, tq4;
      k4_map(BID, kh, tp4, tq4);
      gate.prep(l0 ? 2 : wid, ctl + C_XCNT + (tq4 * NR_X + lane) * RS1 + tp4 * 2 + kh, (1 << NR_X) - 1, x_flag(ctl, tq, tp, lane), 0xFFFFFFFFull, l0 ? 0ull : 5ull);
      gate.m1 = 0x00030003ull;
      gate.seam();
    }
    k1_consumer(lds, lane, wid, l0, tq, (const char*)o + (size_t)(tq * 16 + tp) * TB + (wid >> 1) * (TB / 2), gate, [&] {
      if (l0) {
        int ln = lane;
        asm volatile("" : "+v"(ln));
        const int fr = ln & 15, fq = (ln & 63) >> 4;
        if (wid == 3) {
          f32x4 ta, tb;
          const unsigned ninf = vconst<0xFF61B1E6u>();
          static_for<4>([&](auto rc) { float a, b; win_bias<decltype(rc)::value>(ninf, a, b); ta[decltype(rc)::value] = a; tb[decltype(rc)::value] = b; });
          asm volatile("ds_write_b128 %2, %0\n\tds_write_b128 %2, %1 offset:1024" ::"v"(ta), "v"(tb), "v"(ln * 16 + L_MSK) : "memory");
        }
        const unsigned o1 = vconst<0x3F803F80u>();
        const bf16x8 ones = __builtin_bit_cast(bf16x8, u32x4{o1, o1, o1, o1});
        WAITV(1);
        bf16x8 f[2][2];
#pragma unroll
        for (int k = 0; k < 4; ++k) f[k >> 1][k & 1] = *(const LAS bf16x8*)(lds + L_C + wid * 4096 + k * 1024 + ln * 16);
        const f32x4 z4 = {0.f, 0.f, 0.f, 0.f};
        u64 ps[2];
#pragma unroll
        for (int j = 0; j < 2; ++j) {
          const f32x4 gm = __builtin_amdgcn_mfma_f32_16x16x32_bf16(f[j][1], f[j][1], __builtin_amdgcn_mfma_f32_16x16x32_bf16(f[j][0], f[j][0], z4, 0, 0, 0), 0, 0, 0);
          const f32x4 sm = __builtin_amdgcn_mfma_f32_16x16x32_bf16(ones, f[j][1], __builtin_amdgcn_mfma_f32_16x16x32_bf16(ones, f[j][0], z4, 0, 0, 0), 0, 0, 0);
          const float da = (fr & 1) ? gm[1] : gm[0], db = (fr & 1) ? gm[3] : gm[2], dg = (fr & 2) ? db : da;
          ps[j] = ((u64)__float_as_uint(dg) << 32) | __float_as_uint(sm[0]);
        }
        if (wid == 2) { WAITV(0); TAG = *(volatile LAS u64*)(size_t)vconst<L_RS + 512>() + 1; }
        if (fq == (fr >> 2)) {
#pragma unroll
          for (int j = 0; j < 2; ++j)
            __hip_atomic_store((u64*)pstat + (size_t)(tq * 4 + wid) * 512 + tp * 32 + j * 16 + fr, ps[j], __ATOMIC_RELAXED, __HIP_MEMORY_SCOPE_AGENT);
        }
        SCHED0;
      }
    });
  }
}

struct ResTile {
  LAS char* a;
  int sw;
  LAS char* b;
  int c8, r8;
  __device__ __forceinline__ void init(LAS char* rt, int lane) {
    a = rt + (lane & 15) * 128; sw = (lane & 15) >> 1;
    r8 = lane >> 3; c8 = lane & 7; b = rt + r8 * 128;
  }
  __device__ __forceinline__ void load(u32x2 (&v)[8]) const {
#pragma unroll
    for (int t = 0; t < 8; ++t) v[t] = *(const LAS u32x2*)((LAS char*)(size_t)(wa[t & 3] - (L_C - L_A)) + (t >> 2) * 2048);
  }
  unsigned rb[2];
  __device__ __forceinline__ void stage_rows(const u32x2 (&v)[8], u32x4 (&y)[4]) const {
#pragma unroll
    for (int t = 0; t < 8; ++t) *(LAS u32x2*)((LAS char*)(size_t)wa[t & 3] + (t >> 2) * 2048) = v[t];
    WAITL0;
#pragma unroll
    for (int k = 0; k < 4; ++k) y[k] = *(const LAS u32x4*)((LAS char*)(size_t)rb[k & 1] + k * 1024);
  }
  unsigned wa[4], ra[8], go[4];
  __device__ __forceinline__ void prep_t(LAS char* rt, int d0, int s0, int lane) {
    const int g = lane >> 4, q4 = (lane & 15) >> 2, p4 = lane & 3, fr = lane & 15;
    const int rl = 8 * g + q4, rh = rl + 4;
    const unsigned al = (unsigned)(size_t)rt + rl * 128 + (p4 & 1) * 8;
#pragma unroll
    for (int t = 0; t < 4; ++t) wa[t] = (unsigned)(size_t)a + ((((t >> 1) * 4 + g) ^ sw) * 16) + ((t ^ (g >> 1)) & 1) * 8;
#pragma unroll
    for (int k = 0; k < 4; ++k) {
      const int ch = 2 * k + (p4 >> 1);
      ra[2 * k] = al + ((ch ^ ((rl >> 1) & 7)) * 16);
      ra[2 * k + 1] = al + 512 + ((ch ^ ((rh >> 1) & 7)) * 16);
    }
    {
      const unsigned s1 = pow2sel((fr >> 1) == 3, 7, 10);
      go[0] = phys<64>((d0 + fr) >> 1) * 128 + (fr & 1) * 64 + (s0 >> 5) * (TB / 2) + g * 16;
      go[1] = go[0] + s1; go[2] = go[1] + s1; go[3] = go[2] + s1;
    }
    rb[0] = (unsigned)(size_t)b + ((c8 ^ (r8 >> 1)) * 16); rb[1] = (unsigned)(size_t)b + ((c8 ^ (4 + (r8 >> 1))) * 16);
#pragma unroll
    for (int t = 0; t < 8; ++t) asm volatile("" : "+v"(wa[t & 3]), "+v"(ra[t]), "+v"(go[t & 3]), "+v"(rb[t & 1]));
  }
  __device__ __forceinline__ void load_t(f32x2 (&z)[16]) const {
#pragma unroll
    for (int k = 0; k < 8; ++k) {
      const u32x2 v = __builtin_bit_cast(u32x2, __builtin_amdgcn_ds_read_tr16_b64_v4i16((LAS s16x4*)(size_t)ra[k]));
      z[2 * k] = f32x2{lo2f(v[0]), hi2f(v[0])}; z[2 * k + 1] = f32x2{lo2f(v[1]), hi2f(v[1])};
    }
#pragma unroll
    for (int e = 0; e < 16; ++e) asm volatile("" : "+v"(z[e]));
  }
};

__device__ __forceinline__ u32x2 row_stats(const f32x2 (&z)[16]) {
  unsigned s1[2], q1[2];
#pragma unroll
  for (int j = 0; j < 2; ++j) {
    f32x2 sm[2], sq[2];
#pragma unroll
    for (int e = 0; e < 8; ++e) {
      const f32x2 v = z[8 * j + e];
      if (e < 2) { sm[e] = v; sq[e] = v * v; }
      else { sm[e & 1] += v; sq[e & 1] = __builtin_elementwise_fma(v, v, sq[e & 1]); }
    }
    const f32x2 s2 = sm[0] + sm[1], q2 = sq[0] + sq[1];
    s1[j] = __float_as_uint(s2[0] + s2[1]); q1[j] = __float_as_uint(q2[0] + q2[1]);
  }
  auto add2 = [](auto r) { return __float_as_uint(__uint_as_float(r[0]) + __uint_as_float(r[1])); };
  const unsigned sa = add2(__builtin_amdgcn_permlane16_swap(s1[0], s1[1], false, false)), qa = add2(__builtin_amdgcn_permlane16_swap(q1[0], q1[1], false, false));
  const unsigned t = add2(__builtin_amdgcn_permlane32_swap(sa, qa, false, false));
  const auto c = __builtin_amdgcn_permlane32_swap(t, t, false, false);
  return u32x2{c[0], c[1]};
}
#define XLD \
  "global_load_dwordx4 v[192:195], %[gx], off sc1\n\tglobal_load_dwordx4 v[196:199], %[gx], off offset:256 sc1\n\t" \
  "global_load_dwordx4 v[200:203], %[gx], off offset:512 sc1\n\tglobal_load_dwordx4 v[204:207], %[gx], off offset:768 sc1\n\t"
using u32x16 = __attribute__((ext_vector_type(16))) unsigned;
__device__ __forceinline__ void x_issue(u32x16& g, const char* gx, const u32x4 (&y)[4], unsigned vo, const u16* ob) {
  asm volatile(XLD
               "global_store_dwordx4 %[vo], %[y0], %[ob] sc1\n\tv_add_u32 %[vo], 0x4000, %[vo]\n\t"
               "global_store_dwordx4 %[vo], %[y1], %[ob] sc1\n\tv_add_u32 %[vo], 0x4000, %[vo]\n\t"
               "global_store_dwordx4 %[vo], %[y2], %[ob] sc1\n\tv_add_u32 %[vo], 0x4000, %[vo]\n\t"
               "global_store_dwordx4 %[vo], %[y3], %[ob] sc1\n\ts_nop 1"
               : "=&{v[192:207]}"(g), [vo] "+v"(vo)
               : [gx] "v"(gx), [y0] "v"(y[0]), [y1] "v"(y[1]), [y2] "v"(y[2]), [y3] "v"(y[3]), [ob] "s"(ob) : "memory");
}
template <int SL>
__device__ __forceinline__ void x_gather(u32x16& g, const char* gx, f32x2& s0, f32x2& s1) {
  unsigned n;
  asm volatile("s_mov_b32 %[n], 0x8000\n\ts_waitcnt vmcnt(4)\n"
               "1:\n\tv_max3_u32 v208, v193, v195, v197\n\tv_max3_u32 v209, v199, v201, v203\n\tv_max3_u32 v208, v208, v205, v207\n\t"
               "v_pk_add_f32 v[192:193], v[192:193], v[196:197]\n\tv_max_u32 v208, v208, v209\n\tv_cmp_eq_u32 vcc, -1, v208\n\t"
               "v_pk_add_f32 v[194:195], v[194:195], v[198:199]\n\tv_pk_add_f32 v[200:201], v[200:201], v[204:205]\n\t"
               "v_pk_add_f32 v[202:203], v[202:203], v[206:207]\n\ts_cbranch_vccz 3f\n\t"
               "s_sleep %[sl]\n\t" XLD "s_waitcnt vmcnt(0)\n\t"
               "s_sub_u32 %[n], %[n], 1\n\ts_cbranch_scc0 1b\n"
               "3:\n\tv_pk_add_f32 %[s0], v[192:193], v[200:201]\n\tv_pk_add_f32 %[s1], v[194:195], v[202:203]\n\ts_nop 1"
               : "+{v[192:207]}"(g), [s0] "=&v"(s0), [s1] "=&v"(s1), [n] "=&s"(n)
               : [gx] "v"(gx), [sl] "i"(SL) : "memory", "vcc", "scc", "v208", "v209");
}

#define XSLEEP0 2
#define XSLEEP1 1
constexpr int K2_RES = L_C;
template <class Base>
struct K3NextHook : Base {
  static constexpr int NEXT = 8;
  Stream<KM, 16, 128> sn;
  bool on; int nev; const u16* w1; const u16* cw1; const u64* ctl; LAS char* lds; int gwn, lanen;
  __device__ __forceinline__ void afterr() {
    OPAQUE_BID;
    int tq, tp;
    tile_map<8, 28, 4, 7>(BID, tq, tp);
    sn.init(Operand{w1, F_, min(tp, 27) * 128, 64, S_ * F_ * 2, 0, false, cw1, 32}, gwn, lanen);
    sn.pin();
  }
  __device__ __forceinline__ void step(int s) {
    if (__builtin_expect(s == nev, 0)) {
      if (s == 6) { if (gwn == 0) fetch_flags<0>(first_flags(ctl, lanen)); else fetch_flags<1024>(first_flags(ctl, lanen) + 2); }
      else if (!flags_set(L_FLG + gwn * 1024 + lanen * 16, TAG)) wait_first_phase(lds, ctl, lanen);
      nev += 4;
      asm volatile("" : "+s"(nev));
    }
    Base::step(s);
  }
  template <int S, int N> __device__ __forceinline__ void wait() {
    if (on) waitv<N>(); else waitv<N - (S - 12) * NEXT>();
  }
  template <int T> __device__ __forceinline__ void next() { if (on) sn.template stage<A1>(lds + L_A + gwn * 1024 + T * 16384); }
};
__device__ __forceinline__ void k2_body(LAS char* lds, int wid, const u16* __restrict__ wo, const u16* o, const u16* res, u16* out, u16* hout,
                                        unsigned long long* xch, u64* ctl, bool l0, const u16* w1, const u16* cw1) {
  LAS char* qbuf = lds + L_B;
  LAS char* pbuf = lds + L_A + 32768;
  OPAQUE_BID;
  const int lane = lane_id();
  int tq, tp;
  tile_map<16, 16, 2, 16>(BID, tq, tp);
  const int p0 = tp * 64, q0 = tq * 128;
  if (wid >= 6) {
    Operand P{wo, D_, p0, 64, D_ * D_ * 2, 0, false, nullptr, 0};
    SyncHook<K3NextHook<ResHook<4, 32>>, 1> hk;
    hk.on = BID < 224; hk.nev = l0 ? 6 : 99; hk.w1 = w1; hk.cw1 = cw1; hk.ctl = ctl; hk.lds = lds; hk.gwn = wid - 6; hk.lanen = lane;
    static_cast<ResHook<4, 32>&>(hk) = ResHook<4, 32>{{}, res, q0, p0, lds + L_A, wid - 6, lane};
    g_loader<KM, 16, 64, K2_NSP, 16, -1, false>(pbuf, P, wid - 6, lane, hk, PF2);
    __builtin_amdgcn_s_waitcnt(0x0F70);
    return;
  }
  if (wid >= 4) {
    Operand Q{o, 16, q0, 64, 16 * 16 * TB, 0, true, nullptr, 0};
    WaitHookX hk;
    g_loader<KC, 16, 128, K2_NSQ, 16, 1, false>(qbuf, Q, wid - 4, lane, hk);
    __builtin_amdgcn_s_waitcnt(0x0F70);
    return;
  }
  Gate gate;
  {
    int tq1, tp1;
    tile_map<16, 16, 8, 4>(BID, tq1, tp1);
    gate.prep(wid, ctl + C_OCNT + (tq1 * NR_O + lane) * RS1 + tp1, (1 << NR_O) - 1, ctl + C_OCNT + (tq * NR_O + tp % NR_O) * RS1 + (lane & 15), 0xFFFFull, 1);
    gate.m1 = (1 << NP2) - 1;
    gate.seam();
  }
  f32x4 acc[4][2];
  const int row0 = q0 + wid * 32;
  const size_t obase = (size_t)row0 * D_ + p0;
  ResTile rt;
  rt.init(lds + K2_RES + wid * 4096, lane);
  u32x2 x[8];
  unsigned long long* xw = xch + (size_t)(row0 >> 5) * 512 + tp * 32 + (lane & 15) * 2 + ((lane >> 4) & 1);
  const char* gx = (const char*)(xch + (size_t)(row0 >> 5) * 512) + (lane >> 4) * 1024 + (lane & 15) * 16;
  asm volatile("" : "+v"(xw), "+v"(gx));
  unsigned xo = (unsigned)((lane >> 3) * D_ * 2 + (lane & 7) * 16);
  const u16* xb = out + obase;
  asm volatile("" : "+v"(xo), "+s"(xb));
  rt.prep_t(lds + K2_RES + wid * 4096, p0 & 127, row0 & 63, lane);
  const char* hb = (const char*)hout + (size_t)((p0 >> 7) * 32 + (row0 >> 6)) * TB;
  asm volatile("" : "+s"(hb));
  unsigned cwa = K2_RES + wid * 4096 + lane * 4, cra = K2_RES + wid * 4096 + (lane >> 4) * 32;
  asm volatile("" : "+v"(cwa), "+v"(cra));
  const bool hi = lane >= 32;
  consumer16w<KM, KC, 4, 2, K2_NSP, 64 * 128, 128 * 128, 16, false>(pbuf, qbuf, acc, 0, wid * 2, lane, gate, [&] {
    rt.load(x);
#pragma unroll
    for (int t = 0; t < 8; ++t) acc[t & 3][t >> 2] = f32x4{lo2f(x[t][0]), hi2f(x[t][0]), lo2f(x[t][1]), hi2f(x[t][1])};
  }, [](auto&&) {}, [](int) {});
  f32x2 z[16];
#pragma unroll
  for (int e = 0; e < 16; ++e) z[e] = f32x2{acc[(e >> 1) & 3][e >> 3][2 * (e & 1)], acc[(e >> 1) & 3][e >> 3][2 * (e & 1) + 1]};
  ALIGN4;
  u32x2 ps = row_stats(z);
  ps[1] = min(ps[1], 0xFFFFFFFEu);
  asm volatile("s_mov_b32 exec_hi, 0\n\tglobal_store_dwordx2 %0, %1, off sc1\n\ts_mov_b32 exec_hi, -1" ::"v"(xw), "v"(ps) : "memory");
  SCHED0;
#pragma unroll
  for (int e = 0; e < 16; ++e) x[e >> 1][e & 1] = pk2(z[e][0], z[e][1]);
  u32x4 yr[4];
  rt.stage_rows(x, yr);
  WAITL0;
  if (XSLEEP0) __builtin_amdgcn_s_sleep(XSLEEP0);
  f32x2 s0, s1;
  u32x16 xg;
  x_issue(xg, gx, yr, xo, xb);
  rt.load_t(z);
  x_gather<XSLEEP1>(xg, gx, s0, s1);
  auto add2 = [](auto r) { return __uint_as_float(r[0]) + __uint_as_float(r[1]); };
  const float sm = half_sum(add2(__builtin_amdgcn_permlane16_swap(__float_as_uint(s0[0]), __float_as_uint(s1[0]), false, false)));
  const float sq = half_sum(add2(__builtin_amdgcn_permlane16_swap(__float_as_uint(s0[1]), __float_as_uint(s1[1]), false, false)));
  const float mean = sm * (1.0f / D_);
  const float var = fmaxf(__builtin_fmaf(-mean, mean, sq * (1.0f / D_)), 0.f);
  const float m1 = rbf(mean), i1 = __builtin_amdgcn_rcpf(rbf(__builtin_amdgcn_sqrtf(rbf(rbf(var) + 1e-5f))));
  const float m2 = rbf((mean - m1) * i1), i2 = __builtin_amdgcn_rcpf(rbf(__builtin_amdgcn_sqrtf(rbf(rbf(var * i1 * i1) + 1e-5f))));
  *(LAS float*)(size_t)cwa = (hi ? -(m1 * i1 + m2) : i1) * i2;
  CFENCE;
  f32x4 cf[4];
#pragma unroll
  for (int k = 0; k < 4; ++k) cf[k] = *(const LAS f32x4*)((LAS char*)(size_t)cra + (k & 1) * 16 + (k >> 1) * 128);
#pragma unroll
  for (int k = 0; k < 4; ++k) {
    u32x4 y;
#pragma unroll
    for (int e = 0; e < 4; ++e) {
      const f32x2 v = z[4 * k + e] * f32x2{cf[e >> 1][2 * (e & 1)], cf[e >> 1][2 * (e & 1) + 1]} + f32x2{cf[2 + (e >> 1)][2 * (e & 1)], cf[2 + (e >> 1)][2 * (e & 1) + 1]};
      y[e] = pk2(v[0], v[1]);
    }
    asm volatile("global_store_dwordx4 %0, %1, %2 sc1\n\ts_nop 1" ::"v"(rt.go[k]), "v"(y), "s"(hb) : "memory");
    SCHED0;
  }
}

constexpr int K4_NS = 4;
constexpr int K4_RES = L_C;
__device__ __forceinline__ void w2_init(Stream<KM, 16, 128>& st, const u16* w2, const u16* cw2, int gw, int lane) {
  OPAQUE_BID;
  int kh, tp, tq;
  k4_map(BID, kh, tp, tq);
  st.init(Operand{w2, S_, tq * 128 + kh * 64, 64, F_ * S_ * 2, kh * 28, false, cw2, 56}, gw, lane);
  st.twice();
  st.pin();
}
__device__ __forceinline__ void u_init(Stream<KC, 16, 128, true>& st, const u16* u, int gw, int lane) {
  OPAQUE_BID;
  int kh, tp, tq;
  k4_map(BID, kh, tp, tq);
  st.init(Operand{u, 56, tp * 128, 64, 8 * 56 * TB, kh * 28, true, nullptr, 0}, gw, lane);
  st.twice();
  st.pin();
}
template <class T> __device__ __forceinline__ T* OPV(T* p) { return (T*)(__attribute__((address_space(1))) T*)(size_t)p; }
struct UFlags {
  u64* set; const u64* get; u64 tag;
  __device__ __forceinline__ void init(LAS char* lds, u64* flags, int gw, int lane) {
    OPAQUE_BID;
    int kh, tp, tq, tq3, tp3;
    k4_map(BID, kh, tp, tq);
    tile_map<8, 28, 4, 7>(BID, tq3, tp3);
    u64* b = flags + (gw ? C_UCN2 : C_UCNT);
    set = b + ((tq3 * 2 + tp3 / 14) * NR_U + lane) * RS1 + tp3 % 14;
    get = b + ((tp * 2 + (gw ? (lane >> 3) & 1 : kh)) * NR_U + tq % NR_U) * RS1 + (gw ? min((lane & 7) * 2, 12) : min(lane, 13));
    tag = TAG;
    asm volatile("" : "+v"(set), "+v"(get), "+v"(tag));
  }
  __device__ __forceinline__ void publish() { asm volatile("s_mov_b64 exec, %2\n\tglobal_atomic_swap_x2 %0, %1, off sc1\n\ts_mov_b64 exec, -1" ::"v"(set), "v"(tag), "i"((1 << NR_U) - 1) : "memory"); }
};
struct K4NextHook : NoHook {
  static constexpr int AUX = A1;
  static constexpr int NEXT = 8;
  Stream<KM, 16, 128>* st; LAS char* lds; int gw;
  template <int T> __device__ __forceinline__ void next() { st->stage<A2>(lds + L_A + gw * 1024 + T * 16384); }
};
struct UHook : WaitHookP { static constexpr bool EVOD = true; int kh; };
template <class Base>
struct W2Hook : Base {
  static constexpr bool PREALL = true, EVOD = true, OWNP = true;
  int kh;
  UFlags* uf; bool own; int gww;
  __device__ __forceinline__ void setup() { Base::first(); Base::afterr(); }
  __device__ __forceinline__ void first() {
    if (gww == 0) {
      u64 a, b, c, d, t, z = vz(), m1 = (1 << NP4) - 1;
      unsigned n;
      asm volatile("s_mov_b64 exec, 0x3FFF\n\ts_mov_b32 %[n], 0\n\t" POLL1(a) POLL1(b) POLL1(c) POLL0(d)
                   "1:\n\t" POLL3(a, 4) POLL3(b, 5) POLL3(c, 6) POLL3(d, 7) PEND
                   : [a] "=&v"(a), [b] "=&v"(b), [c] "=&v"(c), [d] "=&v"(d), [n] "=&s"(n), [t] "=&s"(t), [m1] "+s"(m1)
                   : [q] "v"(uf->get), [z] "v"(z), [tag] "v"(uf->tag), [sl] "i"(PSLP) : "memory", "vcc", "scc");
      asm volatile("s_barrier\n\ts_waitcnt vmcnt(0)" ::"v"(a), "v"(b), "v"(c), "v"(d) : "memory");
    } else { BARRIER; WAITV(0); BARRIER; }
  }
  int nev;
  __device__ __forceinline__ void step(int s) {
    if (__builtin_expect(s == nev, 0)) {
      if (s == 0) { if (own) uf->publish(); }
      else if (s == 4) fetch_flags<0>(uf->get);
      else if (!flags_set(L_FLG + Base::lanen * 16, uf->tag)) wait_tags<2>(OPV(uf->get), uf->tag);
      nev = s >= 8 ? 99 : s + 4;
      asm volatile("" : "+s"(nev));
    }
  }
};
template <class Base>
struct K1NextHook : Base {
  static constexpr int NEXT = 8;
  Stream<KM, 16, 192> sn;
  bool on; const u16* wq; LAS char* dstn; int gwn, lanen;
  __device__ __forceinline__ void afterr() {
    OPAQUE_BID;
    int tq, tp;
    tile_map<16, 16, 8, 4>(BID, tq, tp);
    sn.init(Operand{wq, 3 * D_, tp * 64, D_, 3 * D_ * D_ * 2, 0, false, nullptr, 0}, gwn, lanen);
#pragma unroll
    for (int i = 0; i < 4; ++i) asm volatile("" : "+v"(sn.voff[i]));
  }
  template <int U> __device__ __forceinline__ void unit() {
    const auto rsrc = __builtin_amdgcn_make_buffer_rsrc((void*)sn.gbase[0], 0, sn.gbytes[0], 0x00020000);
#pragma unroll
    for (int i = 0; i < 4; ++i) {
#if defined(__HIP_DEVICE_COMPILE__)
      __builtin_amdgcn_raw_ptr_buffer_load_lds(rsrc, (LAS void*)(dstn + U * 8192 + i * 2048), 16, sn.voff[i], sn.so[0] + (U / 3) * sn.inc[0] + (U % 3) * D_ * 2, 0, 0);
#endif
    }
  }
  __device__ __forceinline__ void last() { Base::last(); if (on) unit<8>(); }
  template <int S, int N> __device__ __forceinline__ void wait() {
    if (on) waitv<N + (S < 26 + KD ? 4 : 0)>(); else waitv<N - (S - 24 - KD) * NEXT>();
  }
  template <int T> __device__ __forceinline__ void next() { if (on) { unit<2 * ((KD + T) % 4)>(); unit<2 * ((KD + T) % 4) + 1>(); } }
};
__device__ __forceinline__ void k4_consumer(LAS char* lds, u16* out, u32x2* pstat, float* part, u64* flags, int wid) {
  LAS char* pbuf = lds + L_B;
  LAS char* qbuf = lds + L_A;
  OPAQUE_BID;
  const int lane = lane_id();
  int kh, tp, tq;
  k4_map(BID, kh, tp, tq);
  const int p0 = tp * 128, q0 = tq * 128;
  DrainGate gate;
  const int wp = wid >> 1, wq = wid & 1;
  f32x4 acc[4][4];
  const int row0 = q0 + kh * 64 + wq * 32;
  const size_t obase = (size_t)row0 * D_ + p0 + wp * 64;
#pragma unroll
  for (int j = 0; j < 4; ++j) {
    const float bq = j < 2 ? bf2f(*(const LAS u16*)(lds + L_BIA + 256 + (kh * 64 + wq * 32 + j * 16 + (lane & 15)) * 2)) : 0.f;
#pragma unroll
    for (int i = 0; i < 4; ++i) acc[i][j] = f32x4{bq, bq, bq, bq};
  }
  u32x4 x[2][2];
#define GLD "global_load_dwordx4 %[g0], %[go], %[gp] sc1\n\tglobal_load_dwordx4 %[g1], %[go], %[gp] offset:1024 sc1\n\t" \
            "global_load_dwordx4 %[g2], %[go], %[gp] offset:2048 sc1\n\tglobal_load_dwordx4 %[g3], %[go], %[gp] offset:3072 sc1"
  u64 ptag, a, a2, miss;
  u32x4 g[4];
  const char* gp = (const char*)part + (size_t)((BID ^ 1) * 4 + wid) * 4096;
  unsigned v0;
  const u64* myf = flags + (BID * 4 + wid) * RSP;
  const u64* paf = flags + ((BID ^ 1) * 4 + wid) * RSP;
  u64 zz;
  auto poll1 = [&](u64& r) {
    asm volatile("s_mov_b64 exec, 1\n\tglobal_atomic_or_x2 %0, %1, %2, %3 sc0 sc1\n\ts_mov_b64 exec, -1" : "=&v"(r) : "v"(v0), "v"(zz), "s"(paf) : "memory");
  };
  consumer16w<KC, KM, 4, 4, K4_NS, 128 * 128, 128 * 128, 28 - KD, true>(pbuf, qbuf, acc, wp * 4, wq * 2, lane, gate, [&] {
    const int fr = lane & 15, fq = lane >> 4;
#pragma unroll
    for (int t = 0; t < 4; ++t) x[t >> 1][t & 1] = *(const LAS u32x4*)(lds + K4_RES + wid * 4096 + (t >> 1) * 2048 + fr * 128 + ((((t & 1) * 4 + fq) ^ (fr >> 1)) * 16));
  }, [&](int t) {
    const auto rs = __builtin_amdgcn_make_buffer_rsrc((void*)part, 0, 256 * 16384, 0x00020000);
    const f32x4 a = acc[t][2], b = acc[t][3];
    __builtin_amdgcn_raw_buffer_store_b128(u32x4{pk2(a[0], a[1]), pk2(a[2], a[3]), pk2(b[0], b[1]), pk2(b[2], b[3])}, rs, lane * 16 + t * 1024, (BID * 4 + wid) * 4096, 16);
  }, [&](int k) {
    if (k == 0) asm volatile("s_nop 3\n\tv_mov_b32 %0, %3\n\tv_mov_b64 %2, 0\n\tds_read_b64 %1, %0" : "=&v"(v0), "=&v"(ptag), "=&v"(zz) : "i"(L_TAG) : "memory");
    if (k == FBK) {
      asm volatile("v_mov_b32 %0, 0\n\ts_waitcnt vmcnt(0)\n\ts_mov_b64 exec, 1\n\tglobal_atomic_swap_x2 %0, %1, %2 sc1\n\ts_mov_b64 exec, -1" : "+v"(v0), "+v"(ptag) : "s"(myf) : "memory");
    }
    if (k == CKK - 2) poll1(a);
    if (k == CKK - 1) poll1(a2);
    if (k == CKK)
      asm volatile("s_mov_b64 exec, 1\n\ts_waitcnt vmcnt(1)\n\tv_cmp_ne_u64 vcc, %[tag], %[a]\n\ts_mov_b64 exec, -1\n\ts_mov_b64 %[m], vcc\n\ts_cbranch_vccnz 1f\n\t" GLD "\n1:"
                   : [g0] "=&v"(g[0]), [g1] "=&v"(g[1]), [g2] "=&v"(g[2]), [g3] "=&v"(g[3]), [m] "=&s"(miss) : [tag] "v"(ptag), [a] "v"(a), [go] "v"(lane * 16), [gp] "s"(gp) : "memory", "vcc");
    if (k == CKK + 1)
      asm volatile("s_cmp_eq_u64 %[m], 0\n\ts_cbranch_scc1 1f\n\ts_mov_b64 exec, 1\n\ts_waitcnt vmcnt(0)\n\tv_cmp_ne_u64 vcc, %[tag], %[a]\n\ts_mov_b64 exec, -1\n\ts_mov_b64 %[m], vcc\n\ts_cbranch_vccnz 1f\n\t" GLD "\n1:"
                   : [g0] "+v"(g[0]), [g1] "+v"(g[1]), [g2] "+v"(g[2]), [g3] "+v"(g[3]), [m] "+s"(miss), [a] "+v"(a2) : [tag] "v"(ptag), [go] "v"(lane * 16), [gp] "s"(gp) : "memory", "vcc", "scc");
  });
  SCHED0;
  f32x2 z[16];
#pragma unroll
  for (int e = 0; e < 16; ++e) {
    const unsigned r = x[e >> 3][(e >> 2) & 1][e & 3];
    const f32x4 v = acc[(e >> 1) & 3][e >> 3];
    z[e] = f32x2{lo2f(r), hi2f(r)} + f32x2{v[2 * (e & 1)], v[2 * (e & 1) + 1]};
    asm volatile("" : "+v"(z[e]));
  }
  unsigned vo[4];
#pragma unroll
  for (int k = 0; k < 4; ++k) { vo[k] = (unsigned)(((lane & 7) + 8 * k) * D_ * 2 + (lane & 8) * 8 + (lane >> 4) * 16); asm volatile("" : "+v"(vo[k])); }
  const u16* ob = out + obase;
  asm volatile("" : "+s"(ob));
  {
    u64 c, d, sv;
    unsigned n;
    asm volatile("s_cmp_eq_u64 %[m], 0\n\ts_cbranch_scc1 9f\n\t" POLL_LOOP GLD "\n9:\n\ts_waitcnt vmcnt(0)"
        : [a] "=&v"(a), [b] "+v"(a2), [c] "=&v"(c), [d] "=&v"(d), [sv] "=&s"(sv), [n] "=&s"(n), [g0] "+v"(g[0]), [g1] "+v"(g[1]), [g2] "+v"(g[2]), [g3] "+v"(g[3])
        : [q] "v"(paf), [z] "v"(zz), [tag] "v"(ptag), [em] "s"(1ull), [sl] "i"(2), [m] "s"(miss), [go] "v"(lane * 16), [gp] "s"(gp) : "memory", "vcc", "scc");
  }
  {
#pragma unroll
    for (int e = 0; e < 16; ++e) {
      const unsigned r = g[(e >> 1) & 3][2 * (e >> 3) + (e & 1)];
      z[e] += f32x2{lo2f(r), hi2f(r)};
      x[e >> 3][(e >> 2) & 1][e & 3] = pk2(z[e][0], z[e][1]);
      if ((e & 7) == 7) {
        u32x4 n0, n1;
#pragma unroll
        for (int k = 0; k < 4; ++k) {
          n0[k] = __builtin_amdgcn_update_dpp(x[e >> 3][0][k], x[e >> 3][1][k], 0x128, 0xF, 0xC, false);
          n1[k] = __builtin_amdgcn_update_dpp(x[e >> 3][1][k], x[e >> 3][0][k], 0x128, 0xF, 0x3, false);
        }
        asm volatile("global_store_dwordx4 %0, %2, %4 sc1\n\tglobal_store_dwordx4 %1, %3, %4 sc1\n\ts_nop 1" ::"v"(vo[(e >> 3) * 2]), "v"(vo[(e >> 3) * 2 + 1]), "v"(n0), "v"(n1), "s"(ob) : "memory");
      }
    }
    {
      if (pstat != nullptr) {
        const u32x2 c = row_stats(z);
        if (lane < 32)
          __hip_atomic_store((unsigned long long*)pstat + (size_t)(row0 >> 5) * 512 + (tp * 2 + wp) * 32 + lane, ((unsigned long long)c[1] << 32) | c[0],
                             __ATOMIC_RELAXED, __HIP_MEMORY_SCOPE_AGENT);
      }
    }
  }
}

constexpr int K3_NSP = 4, K3_NSQ = 4;
__device__ __forceinline__ u64* h_flag(u64* ctl, unsigned BID, int lane) {
  int tq, tp;
  tile_map<16, 16, 2, 16>(BID, tq, tp);
  return ctl + C_HCNT + ((tp >> 1) * NR_H + lane) * RS2 + (tp & 1) * 16 + tq;
}
template <class T> __device__ __forceinline__ T* OPQ(T* p) {
  auto q = (__attribute__((address_space(1))) T*)p;
  asm volatile("" : "+s"(q));
  return (T*)q;
}
#define APOS 2
__device__ __forceinline__ void k34_body(LAS char* lds, int wid, bool l0, const u16* __restrict__ w1, const u16* h, u16* u, const u16* __restrict__ cw1, u64* flags,
                                         const u16* __restrict__ w2, const u16* res, u16* out, u32x2* pstat, float* part, const u16* __restrict__ wq_) {
  LAS char* pbuf = lds + L_A;
  LAS char* qbuf = lds + L_B;
  OPAQUE_BID;
  const int lane = lane_id();
  const bool own = BID < 224;
  if (!own) {
    WAITV(0);
    BARRIER;
    if (wid == 0 && lane < NR_H) flag_set(h_flag(flags, BID, lane), TAG);
    if (l0) {
      for (int k = 0; k < WT0; ++k) __builtin_amdgcn_s_sleep(8);
      for (int k = 0; k < wid; ++k) __builtin_amdgcn_s_sleep(WSTG);
#pragma unroll 1
      for (int i = 0; i < 7; ++i) {
        const int g = 2 * i + (lane >> 5), par = g >= 7;
        touch_line(noslow(w2, (((BID >> 4) & 1) * 1792 + (g - 7 * par) * 256 + (BID & 15) * 16 + par * 8 + wid) * 32 + (lane & 31)), L_SCR);
        __builtin_amdgcn_s_sleep(WPER);
      }
    } else if (wid == 4) {
      __builtin_amdgcn_s_sleep(127); __builtin_amdgcn_s_sleep(127); __builtin_amdgcn_s_sleep(70);
    }
  }
  int tq, tp;
  tile_map<8, 28, 4, 7>(BID, tq, tp);
  const int p0 = tp * 128, q0 = tq * 128;
  if (wid >= 6) {
    Stream<KC, 16, 128, true> st;
    u_init(st, u, wid - 6, lane);
    if (own) {
      Operand Q{h, 32, q0, 64, 8 * 32 * TB, 0, true, nullptr, 0};
      WaitHookX hk;
      g_loader<KC, 16, 128, K3_NSQ, 32, -1, false>(qbuf, Q, wid - 6, lane, hk);
      BARRIER;
    }
    UHook hk;
    hk.kh = BID & 1;
    g_run<KC, 16, 128, K4_NS, 28 + KD, 3, false>(qbuf, st, wid - 6, lane, hk, false);
    __builtin_amdgcn_s_waitcnt(0x0F70);
    return;
  }
  if (wid >= 4) {
    Stream<KM, 16, 128> st;
    UFlags uf;
    w2_init(st, w2, cw1 + 224 * CW_BLK / 2, wid - 4, lane);
    uf.init(lds, flags, wid - 4, lane);
    int kh, tp4, tq4;
    k4_map(OPQ((int)BID), kh, tp4, tq4);
    W2Hook<K1NextHook<ResHook<2, 32>>> h4;
    static_cast<ResHook<2, 32>&>(h4) = ResHook<2, 32>{{}, res, tq4 * 128 + kh * 64, tp4 * 128, lds + K4_RES, wid - 4, lane};
    h4.on = l0 && ((((int)((size_t)wq_ >> 7)) + (BID & 1) * 4 + (BID >> 6)) & 7) != 3; h4.wq = wq_; h4.dstn = lds + L_A + (wid - 4) * 1024; h4.gwn = wid - 4; h4.lanen = lane;
    h4.kh = kh; h4.uf = &uf; h4.own = own; h4.gww = wid - 4; h4.nev = wid == 5 ? 0 : 99;
    h4.setup();
    if (own) {
      Operand P{w1, F_, p0, 64, S_ * F_ * 2, 0, false, cw1, 32};
      SyncHook<K4NextHook, 2> hk;
      hk.st = &st; hk.lds = lds; hk.gw = wid - 4;
      g_loader<KM, 16, 128, K3_NSP, 32, 2, false>(pbuf, P, wid - 4, lane, hk, true);
      BARRIER;
      if (wid == 4) {
        uf.publish();
      }
    }
    g_run<KM, 16, 128, K4_NS, 28 + KD, -1, false>(pbuf, st, wid - 4, lane, h4, own);
    __builtin_amdgcn_s_waitcnt(0x0F70);
    return;
  }
  if (own) {
    Gate gate;
    gate.prep(wid, h_flag(flags, BID, lane), (1 << NR_H) - 1, flags + C_HCNT + (tq * NR_H + tp % NR_H) * RS2 + (lane & 31), 0xFFFFFFFFull, 1);
    gate.m1 = NP3 == 2 ? 0x00010001ull : 0x00030003ull;
    gate.seam();
    const int wp = wid >> 1, wq = wid & 1;
    f32x4 acc[4][4];
#pragma unroll
    for (int i = 0; i < 4; ++i) {
      const u32x2 bv = *(const LAS u32x2*)(lds + L_BIA + (wp * 64 + (i >> 1) * 8 + (lane >> 4) * 16 + ((i ^ (lane >> 5)) & 1) * 4) * 2);
#pragma unroll
      for (int j = 0; j < 4; ++j) acc[i][j] = f32x4{lo2f(bv[0]), hi2f(bv[0]), lo2f(bv[1]), hi2f(bv[1])};
    }
    consumer16w<KM, KC, 4, 4, K3_NSP, 128 * 128, 128 * 128, 32, false>(pbuf, qbuf, acc, wp * 4, wq * 4, lane, gate, [&] { }, [&](auto&& mm) {
      int ln = lane;
      asm volatile("" : "+v"(ln));
      const int fr = ln & 15, fq = (ln & 63) >> 4;
      const char* ub = (const char*)u + (size_t)(tq * 56 + tp * 2) * TB + wp * (TB / 2);
      unsigned uo[4];
      {
        const unsigned s1 = pow2sel((fr >> 1) == 3, 7, 10);
        uo[0] = phys<64>(wq * 32 + (fr >> 1)) * 128 + (fr & 1) * 64 + fq * 16; uo[1] = uo[0] + s1; uo[2] = uo[1] + s1; uo[3] = uo[2] + s1;
      }
      mm(0, 8);
#pragma unroll
      for (int n = 0; n < 8; ++n) { __builtin_amdgcn_sched_group_barrier(0x8, 1, 0); __builtin_amdgcn_sched_group_barrier(0x2, 2, 0); }
      asm volatile("" : "+v"(uo[0]), "+v"(uo[1]), "+v"(uo[2]), "+v"(uo[3]));
      const float c0 = -2.0f * 0.7978845608028654f * 1.4426950408889634f, c1 = c0 * 0.044715f;
      auto shade = [&](int n0) {
        if (n0 < 32) {
          mm(n0, n0 + GM);
#pragma unroll
          for (int n = 0; n < GM; ++n) { __builtin_amdgcn_sched_group_barrier(0x8, 1, 0); __builtin_amdgcn_sched_group_barrier(0x400, 1, 0); }
          __builtin_amdgcn_sched_group_barrier(0x400, 16 - GM, 0);
        }
      };
#pragma unroll
      for (int g = 0; g < 4; ++g) {
        f32x2 x[8], z[8];
        auto abar = [&](int pos) {
          if (g == 3 && pos == APOS) {
            if (pos > 0) asm volatile("" : "+v"(z[0]), "+v"(z[1]), "+v"(z[2]), "+v"(z[3]), "+v"(z[4]), "+v"(z[5]), "+v"(z[6]), "+v"(z[7]));
            SCHED0; WAITV(2); BARRIER; SCHED0;
          }
        };
        abar(0);
        ALIGN8;
#pragma unroll
        for (int e = 0; e < 8; ++e) {
          const f32x4 v = acc[(g >> 1) * 2 + ((e >> 1) & 1)][(g & 1) * 2 + (e >> 2)];
          x[e] = f32x2{v[2 * (e & 1)], v[2 * (e & 1) + 1]};
          z[e] = x[e] * x[e];
        }
#pragma unroll
        for (int e = 0; e < 8; ++e) z[e] = z[e] * c1 + c0;
#pragma unroll
        for (int e = 0; e < 8; ++e) z[e] *= x[e];
        SCHED0;
        abar(1);
#pragma unroll
        for (int e = 0; e < 8; ++e) z[e] = f32x2{__builtin_amdgcn_exp2f(z[e][0]), __builtin_amdgcn_exp2f(z[e][1])};
        shade(8 + g * GM);
        abar(2);
        ALIGN8;
#pragma unroll
        for (int e = 0; e < 8; ++e) z[e] += 1.0f;
        SCHED0;
#pragma unroll
        for (int e = 0; e < 8; ++e) z[e] = f32x2{__builtin_amdgcn_rcpf(z[e][0]), __builtin_amdgcn_rcpf(z[e][1])};
        abar(3);
        ALIGN8;
        u32x4 o[2];
#pragma unroll
        for (int e = 0; e < 8; ++e) z[e] *= x[e];
#pragma unroll
        for (int e = 0; e < 8; ++e) o[e >> 2][e & 3] = pk2(z[e][0], z[e][1]);
#pragma unroll
        for (int a = 0; a < 2; ++a)
          asm volatile("global_store_dwordx4 %0, %1, %2 sc1\n\ts_nop 1" ::"v"(uo[(g & 1) * 2 + a]), "v"(o[a]), "s"(ub + (g >> 1) * TB) : "memory");
      }
    }, [](int) {});
  }
  k4_consumer(lds, OPQ(out), pstat, OPQ(part), OPQ(flags), wid);
}

constexpr size_t MB = 1 << 20;
constexpr size_t W_XA = 0, W_H = 4 * MB, W_O = 10 * MB, W_U = 16 * MB, W_PART = 26 * MB;
constexpr size_t W_XCH = 30 * MB, W_CTL = 31 * MB, W_PST = 33 * MB, W_EPOCH = 34 * MB, W_CW = 35 * MB;

extern "C" __global__ __launch_bounds__(512, 2) void k_all(const u16* x0_, const u16* wqkv_, const u16* wo_, const u16* w1_, const u16* b1_, const u16* w2_,
                                                           const u16* b2_, u16* out_, char* ws_) {
  LAS char* lds = (LAS char*)(size_t)0;
  {
    unsigned long long pc;
    unsigned off;
    asm volatile("s_cmp_lt_u32 %3, %5\n\ts_cbranch_scc0 1f\n\ts_getpc_b64 %0\n\tv_lshlrev_b32 %1, 7, %2\n\ts_mov_b32 m0, %4\n\t"
                 "v_min_u32 %1, k9_end-k_all-256, %1\n\tglobal_load_lds_dword %1, %0\n1:"
                 : "=&s"(pc), "=&v"(off) : "v"(threadIdx.x), "s"(blockIdx.x), "i"(L_SCR), "i"(CWN) : "memory", "scc", "m0");
  }
  auto ka = (const __attribute__((address_space(4))) u64*)__builtin_amdgcn_kernarg_segment_ptr();
  asm volatile("" : "+s"(ka));
  typedef __attribute__((address_space(1))) u16 gu16;
#define ARGS auto kb = ka; asm volatile("" : "+s"(kb)); \
  const u16 *x0 = (u16*)(gu16*)kb[0], *wqkv = (u16*)(gu16*)kb[1], *wo = (u16*)(gu16*)kb[2], *w1 = (u16*)(gu16*)kb[3], *b1 = (u16*)(gu16*)kb[4], *w2 = (u16*)(gu16*)kb[5], *b2 = (u16*)(gu16*)kb[6]; \
  u16* out = (u16*)(gu16*)kb[7]; char* w = OPQ(ws)
  char* ws = (char*)(u16*)(gu16*)ka[8];
  const int wid = __builtin_amdgcn_readfirstlane(threadIdx.x >> 6);
  {
    const u16 *x0 = (u16*)(gu16*)ka[0], *wqkv = (u16*)(gu16*)ka[1];
    const int lane = threadIdx.x & 63;
    int tq, tp;
    tile_map<16, 16, 8, 4>(blockIdx.x, tq, tp);
    if (wid >= 6) {
      Stream<KC, 16, 144> st;
      st.init(Operand{x0, D_, tq * 128 - 16, 64, S_ * D_ * 2, 0, false, nullptr, 0}, wid - 6, lane);
      st.stage<16>(lds + K1_QOFF + (wid - 6) * 1024, lane >= 8);
    } else if (wid >= 4) {
      Stream<KM, 16, 192> st;
      st.init(Operand{wqkv, 3 * D_, tp * 64, D_, 3 * D_ * D_ * 2, 0, false, nullptr, 0}, wid - 4, lane);
      st.stage<0>(lds + L_A + (wid - 4) * 1024);
    } else {
      const u16* xb = x0 + (size_t)(tq * 128 + wid * 32) * D_ + tp * 64;
      int t3q, t3p, kh4, t4p, t4q;
      tile_map<8, 28, 4, 7>(blockIdx.x, t3q, t3p);
      k4_map(blockIdx.x, kh4, t4p, t4q);
      const char* bp = wid >= 2 ? ws + W_EPOCH : (const char*)(u16*)(gu16*)ka[4 + 2 * (wid & 1)] + (wid ? t4q : min(t3p, 27)) * 256;
      asm volatile("s_mov_b32 m0, %[lx]\n\ts_nop 0\n\tglobal_load_lds_dwordx4 %[xo], %[xb]\n\ts_add_u32 m0, %[lx], 1024 - 64\n\ts_nop 0\n\tglobal_load_lds_dwordx4 %[xo], %[xb] offset:64\n\t"
                   "s_add_u32 m0, %[lx], 2048\n\ts_nop 0\n\tglobal_load_lds_dwordx4 %[xo], %[xc]\n\ts_add_u32 m0, %[lx], 3072 - 64\n\ts_nop 0\n\tglobal_load_lds_dwordx4 %[xo], %[xc] offset:64\n\t"
                   "s_mov_b32 m0, %[lb]\n\ts_nop 0\n\tglobal_load_lds_dword %[bo], %[bp] sc1"
                   :: [xo] "v"((lane & 15) * (D_ * 2) + (lane & 48)), [xb] "s"(xb), [xc] "s"(xb + 16 * D_), [bo] "v"(lane * 4), [bp] "s"(bp),
                      [lx] "s"(L_C + wid * 4096), [lb] "s"(wid >= 2 ? L_RS + wid * 256 : L_BIA + wid * 256) : "memory", "m0", "scc");
      if (wid == 1) {
        const int b = blockIdx.x, l = min(lane, 35), r = l / 12, c = l - r * 12;
        touch_line((const char*)wqkv + (64 + 64 * r + ((b >> 2) & 1) * 32 + (b >> 3)) * 6144 + (c >> 2) * 2048 + ((b & 3) * 4 + (c & 3)) * 128, L_SCR);
      }
      if (wid == 3) {
        SlowColHook<NoHook, 3, 2 * D_, 4, 32, 16> hs;
        hs.setup(wqkv, 3 * D_, (tp & ~3) * 128, 0, blockIdx.x >> 3, lane, true, L_SCR);
        hs.first();
      }
    }
    CFENCE;
  }
#pragma unroll 1
  for (int layer = 0; layer < 2; ++layer) {
    {
      ARGS;
      const int l = OPQ(layer);
      k1_body(lds, OPQ(wid), l == 0, wqkv, (u16*)(gu16*)kb[l * 7], (u16*)(w + W_O), (u32x4*)(w + W_XCH),
              (u64*)(w + W_CTL) + l * C_WORDS, (u32x2*)(w + W_PST), wo, w1, w2, (u16*)(w + W_CW));
    }
    {
      ARGS;
      const int l = OPQ(layer);
      k2_body(lds, OPQ(wid), wo, (const u16*)(w + W_O), (u16*)(gu16*)kb[l * 7], (u16*)(w + W_XA), (u16*)(w + W_H),
              (unsigned long long*)(w + W_XCH), (u64*)(w + W_CTL) + l * C_WORDS, l == 0, w1, (const u16*)(w + W_CW));
    }
    {
      ARGS;
      const int l = OPQ(layer);
      k34_body(lds, OPQ(wid), l == 0, w1, (const u16*)(w + W_H), (u16*)(w + W_U), (const u16*)(w + W_CW), (u64*)(w + W_CTL) + l * C_WORDS,
               w2, (const u16*)(w + W_XA), out, l == 0 ? (u32x2*)(w + W_PST) : nullptr, (float*)(w + W_PART), wqkv);
    }
  }
  ARGS;
  if (blockIdx.x == 0 && wid == 7 && lane_id() == 0) __hip_atomic_store((u64*)(w + W_EPOCH), TAG, __ATOMIC_RELAXED, __HIP_MEMORY_SCOPE_AGENT);
}

extern "C" __global__ void k9_end() {}

extern "C" void kernel_launch(void* const* d_in, const int* in_sizes, int n_in, void* d_out, int out_size, void* d_ws,
                              size_t ws_size, hipStream_t stream) {
  (void)hipFuncSetAttribute((const void*)k_all, hipFuncAttributeMaxDynamicSharedMemorySize, L_SHM);
  k_all<<<256, 512, L_SHM, stream>>>((const u16*)d_in[0], (const u16*)d_in[1], (const u16*)d_in[2], (const u16*)d_in[4], (const u16*)d_in[5],
                                     (const u16*)d_in[6], (const u16*)d_in[7], (u16*)d_out, (char*)d_ws);
}
```

```cpp
#include <hip/hip_runtime.h>
#include <hip/hip_bf16.h>
#include <utility>
#include <type_traits>

typedef unsigned short u16;
using bf16x8 = __attribute__((ext_vector_type(8))) short;
using s16x4 = __attribute__((ext_vector_type(4))) short;
using f32x2 = __attribute__((ext_vector_type(2))) float;
using f32x4 = __attribute__((ext_vector_type(4))) float;
using f32x16 = __attribute__((ext_vector_type(16))) float;
using u32x2 = __attribute__((ext_vector_type(2))) unsigned;
using u32x4 = __attribute__((ext_vector_type(4))) unsigned;
using u32x12 = __attribute__((ext_vector_type(12))) unsigned;
typedef __bf16 bf16x2_t __attribute__((ext_vector_type(2)));
#define LAS __attribute__((address_space(3)))

constexpr int S_ = 2048, D_ = 1024, F_ = 3584;
template <int N> __device__ __forceinline__ int phys(int r) { return (r & 7) != 3 ? r : N + 4 + ((r >> 5) << 3) + ((r >> 3) & 3); }
constexpr int TB = 160 * 128;

__device__ __forceinline__ unsigned pk2(float a, float b) {
  f32x2 v = {a, b};
  return __builtin_bit_cast(unsigned, __builtin_convertvector(v, bf16x2_t));
}
__device__ __forceinline__ float lo2f(unsigned v) { return __uint_as_float(v << 16); }
__device__ __forceinline__ float hi2f(unsigned v) { return __uint_as_float(v & 0xFFFF0000u); }
__device__ __forceinline__ float bf2f(u16 v) { return __uint_as_float((unsigned)v << 16); }
__device__ __forceinline__ float rbf(float f) { return lo2f(pk2(f, f)); }
__device__ __forceinline__ u16 f2bf(float f) { return (u16)(pk2(f, f) & 0xFFFFu); }

enum { KC = 0, KM = 1 };
#define OPAQUE_BID unsigned BID = blockIdx.x; asm volatile("" : "+s"(BID))
__device__ __forceinline__ int OPQ(int v) { asm volatile("" : "+s"(v)); return v; }
__device__ __forceinline__ unsigned opq_bid() { OPAQUE_BID; return BID; }
__device__ __forceinline__ int lane_id() {
  const unsigned l = __builtin_amdgcn_mbcnt_hi(~0u, __builtin_amdgcn_mbcnt_lo(~0u, OPQ(0)));
  __builtin_assume(l < 64);
  return l;
}
constexpr int L_A = 0, L_B = 73728, L_C = L_B + 65536, L_D = L_C + 16384, L_TAG = L_D + 1280, L_CSQ = L_D + 1408, L_SCR = L_D + 1792, L_BIA = L_D + 2048, L_RS = L_D + 2560, L_MSK = L_RS + 1152, L_SHM = L_MSK + 2048;
typedef unsigned long long u64;
#define NR_O 8
#define NR_H 8
#define NR_U 8
#define NR_X 8
#define RS1 32
#define RS2 64
#define RSP 32
constexpr int C_FLAG = 0, C_UCNT = 1024 * RSP, C_HCNT = C_UCNT + 16 * NR_U * RS1, C_OCNT = C_HCNT + 8 * NR_H * RS2, C_XCNT = C_OCNT + 16 * NR_O * RS1,
              C_UCN2 = C_XCNT + 16 * NR_X * RS1, C_END = C_UCN2 + 16 * NR_U * RS1, C_WORDS = C_END <= 32768 ? 32768 : 131072;
static_assert(C_END <= C_WORDS, "control words");
#define TAG (*(volatile LAS u64*)(size_t)vconst<L_TAG>())

#define A1 16
#define A2 16
#define AC 2
#define CWN 8
#define PF2 5
#define WOT 11
#define FWS 1
#define KD 3
#define CKK 5
#define FBK 1
#define NP2 3
#define NP3 4
#define NP4 2
#define HQP 21
#define WT0 13
#define WSTG 2
#define WPER 16
#define ARMS 13
#define GM 8
#define HSW 1
#define GN 8
#define G0 3
#define GD 1
#define CFENCE asm volatile("" ::: "memory")
#define WAITV(n) asm volatile("s_waitcnt vmcnt(%0)" ::"i"(n) : "memory")
#define WAITL0 do { CFENCE; __builtin_amdgcn_s_waitcnt(0xC07F); CFENCE; } while (0)
#define BARRIER do { CFENCE; __builtin_amdgcn_s_barrier(); CFENCE; } while (0)
#define SCHED0 __builtin_amdgcn_sched_barrier(0)
#define ALIGN4 do { SCHED0; asm volatile(".fill (((. - k_all) >> 2) & 1) ^ 1, 4, 0xBF800000"); SCHED0; } while (0)
#define ALIGN8 do { SCHED0; asm volatile(".p2align 3"); SCHED0; } while (0)

struct Operand {
  const u16* base;
  int ld;
  int off0;
  int cstride;
  int bytes;
  int kstep0;
  bool holes;
  const u16* alt;
  int alt_nt;
};
constexpr int CW_BLK = 80 * 128;

template <int L, int MS>
__device__ __forceinline__ int swz(int line) {
  if constexpr (L == KC) {
    return MS == 16 ? (line & 7) : ((line >> 1) & 7);
  } else {
    return MS == 16 ? ((((line >> 1) & 1) | (((line >> 3) & 1) << 1)) << 1) : (((line >> 1) & 1) << 2);
  }
}
template <int N> __device__ __forceinline__ void waitv() { WAITV(N); }
__device__ __forceinline__ unsigned pow2sel(bool c, int a, int b) { unsigned e = c ? a : b; asm volatile("" : "+v"(e)); return 1u << e; }
template <unsigned C> __device__ __forceinline__ unsigned vconst() { unsigned v; asm volatile("v_mov_b32 %0, %1" : "=v"(v) : "i"(C)); return v; }

__device__ __forceinline__ u64 vz() { u64 v; asm volatile("v_mov_b64 %0, 0" : "=v"(v)); return v; }

__device__ __forceinline__ void st16(void* p, u32x4 v) {
  asm volatile("global_store_dwordx4 %0, %1, off sc1\n\ts_nop 1" ::"v"(p), "v"(v) : "memory");
}

__device__ __forceinline__ const char* noslow(const void* base, unsigned line) {
  return (const char*)base + (size_t)((((unsigned)((size_t)base >> 7) + line) & 7) == 3 ? line ^ 1 : line) * 128;
}
__device__ __forceinline__ void touch_line(const void* p, unsigned lds_scratch) {
  unsigned keep;
  asm volatile("s_mov_b32 %0, m0\n\ts_mov_b32 m0, %2\n\ts_nop 0\n\tglobal_load_lds_dword %1, off\n\ts_mov_b32 m0, %0"
               : "=&s"(keep) : "v"(p), "s"(lds_scratch) : "memory");
}

struct NoHook {
  static constexpr int EXTRA = 0, NFIRST = 0, AUX = 0, NEXT = 0;
  static constexpr bool EVOD = false;
  static constexpr bool OWNP = false;
  template <int T> __device__ __forceinline__ void next() {}
  static constexpr bool PREALL = false;
  static constexpr bool PIN = false, UNROLL = false, COLD = false, PSW = false;
  __device__ __forceinline__ bool cold() const { return false; }
  template <int S> __device__ __forceinline__ void stepc() {}
  template <int S, int N> __device__ __forceinline__ void wait() { waitv<N>(); }
  template <int T> __device__ __forceinline__ void tail() {}
  __device__ __forceinline__ void ready() {}
  __device__ __forceinline__ void first() {}
  __device__ __forceinline__ void afterr() {}
  __device__ __forceinline__ void last() {}
  __device__ __forceinline__ void mid() {}
  __device__ __forceinline__ void step(int) {}
};

template <int L, int MS, int B, bool PSW = false>
struct Stream {
  static constexpr int LW = B / 16, STG = B * 128;
  static constexpr int NSUB = (L == KM && B == 128) ? 2 : 1;
  unsigned voff[LW];
  const void* gbase[NSUB];
  int gbytes[NSUB], so[NSUB], inc[NSUB], jmp[NSUB];
  __device__ __forceinline__ void init(const Operand& X, int gw, int lane) {
    const int r8 = lane >> 3, c8 = lane & 7, l0 = gw * 8 + r8;
    const unsigned sw = (unsigned)((c8 ^ (PSW ? (r8 & 3) | (gw << 2) : swz<L, MS>(l0))) * 16);
    if constexpr (L == KC) {
      gbase[0] = X.base; gbytes[0] = X.bytes;
      if (X.holes) {
        jmp[0] = 27 * TB;
        inc[0] = TB; so[0] = ((X.off0 >> 7) * X.ld + X.kstep0) * TB;
        {
          const bool iso = X.ld == 16;
          const int ln = !iso ? l0 >> 1 : ((l0 >> 1) & ~1) | (l0 & 1), ps = !iso ? l0 & 1 : (l0 >> 1) & 1;
          const bool h = ln == 3;
          const unsigned v0 = (h ? 68 : ln) * 128 + ps * 64 + (sw & 48) + (sw >> 6) * (TB / 2), s1 = pow2sel(h, 7, 10), s4 = pow2sel(h, 10, 12);
#pragma unroll
          for (int i = 0; i < LW; ++i) voff[i] = i == 0 ? v0 : (i == 4 ? v0 + s4 : voff[i - 1] + s1);
        }
      } else {
        inc[0] = 128; so[0] = X.kstep0 * 128;
#pragma unroll
        for (int i = 0; i < LW; ++i)
          voff[i] = (unsigned)(i == 0 ? max(X.off0 + 2 * r8 + gw, 0) : X.off0 + 16 + 2 * l0 + 32 * ((i - 1) & 3) + ((i - 1) >> 2)) * (unsigned)X.ld * 2u + sw;
      }
    } else if constexpr (NSUB == 1) {
      gbase[0] = X.base; gbytes[0] = X.bytes;
      inc[0] = 128 * X.ld; so[0] = X.kstep0 * inc[0] + X.off0 * 2;
#pragma unroll
      for (int i = 0; i < LW; ++i) voff[i] = (unsigned)((l0 + 16 * (i & 3)) * X.ld + (i >> 2) * X.cstride) * 2u + sw;
    } else {
      const bool isw2 = X.ld == S_;
      const unsigned sx = isw2 ? sw : ((sw & 48) << 1) | ((sw & 64) >> 2);
#pragma unroll
      for (int t = 0; t < NSUB; ++t) {
        const int lc = (X.off0 >> 6) ^ t;
        const bool slow = (((int)((size_t)X.base >> 7) + lc) & 7) == 3;
        gbase[t] = slow ? (const void*)X.alt : (const void*)X.base;
        gbytes[t] = slow ? (X.ld >> 9) * X.alt_nt * CW_BLK : X.bytes;
        inc[t] = slow ? CW_BLK : 128 * X.ld;
        so[t] = X.kstep0 * inc[t] + (slow ? (lc >> 3) * X.alt_nt * CW_BLK : lc * 128);
        const unsigned rowb = slow ? 128 : X.ld * 2;
        const bool h = (slow ? r8 : 0) == 3;
        const unsigned grpb = isw2 && !slow ? 2 * rowb : rowb;
        const unsigned v0 = (h ? 68 + gw : r8) * rowb + (h ? 0 : gw * 8 * grpb) + sx, s2 = (h ? 2 : 16) * grpb, s1 = (h ? 8 : 32) * grpb;
        jmp[t] = slow ? 27 * CW_BLK : (28 * 64 - 8) * rowb;
        voff[4 * t] = v0; voff[4 * t + 1] = v0 + s2; voff[4 * t + 2] = v0 + s1; voff[4 * t + 3] = v0 + s1 + s2;
      }
    }
  }
  __device__ __forceinline__ void pin() {
#pragma unroll
    for (int i = 0; i < LW; ++i) asm volatile("" : "+v"(voff[i]));
#pragma unroll
    for (int t = 0; t < NSUB; ++t) asm volatile("" : "+s"(so[t]));
  }
  __device__ __forceinline__ void skip(int n) {
#pragma unroll
    for (int t = 0; t < NSUB; ++t) so[t] += n * inc[t];
  }
  __device__ __forceinline__ void twice() {
#pragma unroll
    for (int t = 0; t < NSUB; ++t) inc[t] *= 2;
  }
  __device__ __forceinline__ void back() {
#pragma unroll
    for (int t = 0; t < NSUB; ++t) so[t] -= jmp[t];
  }
  template <int AUX, int NT_ = NSUB>
  __device__ __forceinline__ void stage(LAS char* dst, bool act0 = true) {
#pragma unroll
    for (int t = 0; t < NT_; ++t) {
      const auto rsrc = __builtin_amdgcn_make_buffer_rsrc((void*)gbase[t], 0, gbytes[t], 0x00020000);
#pragma unroll
      for (int i = t * (LW / NSUB); i < (t + 1) * (LW / NSUB); ++i) {
#if defined(__HIP_DEVICE_COMPILE__)
        if (i > 0 || act0) __builtin_amdgcn_raw_ptr_buffer_load_lds(rsrc, (LAS void*)(dst + i * 2048), 16, voff[i], so[t], 0, AUX);
#endif
      }
      so[t] += inc[t];
    }
  }
};

template <int N, class F>
__device__ __forceinline__ void static_for(F&& f) {
  [&]<int... I>(std::integer_sequence<int, I...>) { (f(std::integral_constant<int, I>{}), ...); }(std::make_integer_sequence<int, N>{});
}

template <int L, int MS, int B, int NS, int NT, int KID, bool SKIP0, class Hook>
__device__ __forceinline__ void g_run(LAS char* ring, Stream<L, MS, B, Hook::PSW>& st, int gw, int lane, Hook& hook, bool filled) {
  constexpr int LW = B / 16, STG = B * 128;
  constexpr int NPRE = Hook::PIN ? (Hook::COLD ? 2 : NT == 16 ? NP2 : NT == 32 ? NP3 : NP4) : NS;
  static_assert(Hook::PREALL, "cfg");
  static_assert(B % 16 == 0 && NS >= 3 && NT >= NS, "cfg");
  const bool act0 = !SKIP0 || lane >= 8;
  auto stage = [&](int slot) { st.template stage<Hook::AUX>(ring + slot * STG + gw * 1024, act0); };
  hook.ready();
  if (Hook::COLD && !Hook::PIN ? hook.cold() : !filled) {
    if (Hook::COLD && hook.cold()) st.skip(1); else stage(0);
    stage(1);
    if constexpr (Hook::PREALL) {
      if (!hook.cold()) {
#pragma unroll
        for (int i = 2; i < NPRE; ++i) stage(i);
      }
    }
  }
  SCHED0;
  if constexpr (Hook::COLD && !Hook::PIN) {
    if (hook.cold()) {
      waitv<LW>();
      BARRIER;
#pragma unroll
      for (int i = 2; i < NS; ++i) stage(i);
    }
    SCHED0;
    hook.first();
    if (!hook.cold()) {
      BARRIER;
      if (!filled) { stage(0); stage(1); stage(2); }
      BARRIER;
      waitv<(NS - 1) * LW>();
      BARRIER;
    }
  } else {
    if constexpr (!Hook::PIN) hook.first();
    if constexpr (!Hook::OWNP) {
      waitv<(NPRE - 1) * LW + Hook::NFIRST>();
      BARRIER;
    }
#pragma unroll
    for (int i = NPRE; i < NS; ++i) stage(i);
    if constexpr (Hook::PIN) hook.first();
  }
  if constexpr (Hook::UNROLL) {
    [&]<int... S>(std::integer_sequence<int, S...>) {
      (([&] {
         hook.template wait<S, (NS - 2) * LW>();
         BARRIER;
         stage(S % NS);
         hook.step(S);
         hook.template stepc<S>();
         SCHED0;
       }()), ...);
    }(std::make_integer_sequence<int, NT - NS>{});
  } else {
    int slot = 0;
#pragma unroll 1
    for (int s = 0; s < NT - NS - (Hook::EVOD ? 2 * KD : 0); ++s) {
      waitv<(NS - 2) * LW>();
      BARRIER;
      if (Hook::EVOD && s == 14 - NS) st.back();
      stage(slot);
      slot = (slot == NS - 1) ? 0 : slot + 1;
      hook.step(s);
    }
  }
  constexpr int LH = Hook::EVOD && L == KM ? LW / 2 : LW;
  if constexpr (Hook::EVOD) {
    static_for<2 * KD>([&](auto kc) {
      constexpr int k = decltype(kc)::value, n = NT - 2 * KD + k;
      waitv<(k > 1 ? LH : LW) + (k > 0 ? LH : LW)>();
      BARRIER;
      if constexpr (k == KD) st.so[0] += (hook.kh ? -14 - KD : 14 - KD) * st.inc[0];
      st.template stage<Hook::AUX, 1>(ring + (n % NS) * STG + gw * 1024);
    });
  }
  hook.last();
  [&]<int... T>(std::integer_sequence<int, T...>) {
    (([&] {
       hook.template tail<T>();
       if constexpr (T < NS - 2) hook.template wait<NT - NS + T, (NS - 2 - T) * LH + Hook::EXTRA + T * Hook::NEXT>();
       if constexpr (T == NS - 2) hook.template wait<NT - NS + T, T * Hook::NEXT>();
       BARRIER;
       hook.template next<T>();
       if constexpr (T == NS - 2) hook.mid();
     }()), ...);
  }(std::make_integer_sequence<int, NS>{});
}

template <int L, int MS, int B, int NS, int NT, int KID, bool SKIP0, class Hook>
__device__ __forceinline__ void g_loader(LAS char* ring, const Operand& X, int gw, int lane, Hook& hook, bool filled = false) {
  Stream<L, MS, B, Hook::PSW> st;
  st.init(X, gw, lane);
  if constexpr (Hook::PIN) st.pin();
  if (filled) st.skip(NS);
  g_run<L, MS, B, NS, NT, KID, SKIP0>(ring, st, gw, lane, hook, filled);
}

template <int RW, int RSTRIDE>
struct ResHook : NoHook {
  static constexpr bool EARLY = RW == 4;
  static constexpr int EXTRA = EARLY ? 0 : 8, AUX = RW == 2 ? A2 : 0;
  const u16* base; int row0, col0; LAS char* dst; int gw, lane;
  unsigned vo;
  __device__ __forceinline__ void first() {
    const int r8 = lane >> 3, c8 = lane & 7;
    vo = ((unsigned)(row0 + gw * 8 + r8) * D_ + col0) * 2u + (unsigned)((c8 ^ swz<KC, 32>(gw * 8 + r8)) * 16);
    asm volatile("" : "+v"(vo));
    if constexpr (EARLY) { fetch(); WAITV(0); BARRIER; }
  }
  __device__ __forceinline__ void last() { if constexpr (!EARLY) fetch(); }
  __device__ __forceinline__ void fetch() {
    const auto rsrc = __builtin_amdgcn_make_buffer_rsrc((void*)base, 0, S_ * D_ * 2, 0x00020000);
    LAS char* d = dst + gw * 1024;
#pragma unroll
    for (int i = 0; i < 8; ++i)
      __builtin_amdgcn_raw_ptr_buffer_load_lds(rsrc, (LAS void*)(d + i * 2048), 16, vo, ((((i >> 1) % RW) * RSTRIDE + (i & 1) * 16) * D_ + ((i >> 1) / RW) * 64) * 2, 0, 16);
  }
};

template <class Base, int NG, int GS, int NC, int NW, int NT>
struct SlowColHook : Base {
  const char* p;
  int st, inc8;
  unsigned scr;
  const u16* base_; int ld_, cb0_, kstep0_, j_, lane_; bool leader_;
  __device__ __forceinline__ void setup(const u16* base, int ld, int cb0, int kstep0, int j, int lane, bool leader, unsigned scratch) {
    base_ = base; ld_ = ld; cb0_ = cb0; kstep0_ = kstep0; j_ = j; lane_ = lane; leader_ = leader; scr = scratch;
  }
  __device__ __forceinline__ void setup2(const u16* base, int ld, int cb0, int kstep0, int j, int lane, bool leader, unsigned scratch) {
    asm volatile("" : "+v"(lane));
    constexpr int MAXC = (NC + 7) / 8, PER = (NG * MAXC * 64 + NW - 1) / NW;
    static_assert(PER * 8 <= 64, "lanes");
    const int cf = (3 - (int)(((size_t)base + cb0) >> 7)) & 7;
    const int li = j * PER + lane % PER;
    st = lane / PER;
    const int ci = li >> 6, k = li & 63, g = ci / MAXC, c = cf + 8 * (ci % MAXC);
    const bool act = leader && st < 8 && g < NG && c < NC;
    p = act ? (const char*)base + (size_t)((kstep0 + st) * 64 + k) * ld * 2 + cb0 + g * GS + c * 128 : nullptr;
    inc8 = 8 * 64 * ld * 2;
    scr = scratch;
  }
  __device__ __forceinline__ void touch(bool go = true) {
    if (go && p != nullptr && st < NT) touch_line(p, scr);
    p = p != nullptr ? p + inc8 : p;
    st += 8;
  }
  __device__ __forceinline__ void first() { setup2(base_, ld_, cb0_, kstep0_, j_, lane_, leader_, scr); touch(!Base::cold()); Base::first(); }
  __device__ __forceinline__ void step(int s) { if ((s & 7) == 0 && s + 8 < NT) touch(); Base::step(s); }
};

#define PSLP 3
__device__ __forceinline__ void flag_set(u64* p, u64 tag) { asm volatile("global_atomic_swap_x2 %0, %1, off sc1" ::"v"(p), "v"(tag) : "memory"); }
#define POLL1(r) "global_atomic_or_x2 %[" #r "], %[q], %[z], off sc0 sc1\n\ts_sleep %[sl]\n\t"
#define POLL2(r) "s_waitcnt vmcnt(3)\n\tv_cmp_ne_u64 vcc, %[tag], %[" #r "]\n\ts_cbranch_vccz 2f\n\t" POLL1(r)
#define POLL_LOOP "s_mov_b64 %[sv], exec\n\ts_mov_b64 exec, %[em]\n\ts_mov_b32 %[n], 0\n\t" POLL1(a) POLL1(b) POLL1(c) POLL1(d) POLL_BODY
#define POLL_BODY \
  "1:\n\t" POLL2(a) POLL2(b) POLL2(c) POLL2(d) \
  "s_add_u32 %[n], %[n], 1\n\ts_cmp_lt_u32 %[n], 0x8000\n\ts_cbranch_scc1 1b\n\t" \
  "2:\n\ts_mov_b64 exec, %[sv]\n\t"
#define PCHK(r, l) "v_cmp_ne_u64 vcc, %[tag], %[" #r "]\n\ts_and_b64 %[t], vcc, %[m1]\n\ts_cbranch_scc1 " #l "f\n\ts_cbranch_vccz 2f\n\ts_barrier\n\ts_mov_b64 %[m1], -1\n" #l ":\n\t"
#define POLL3(r, l) "s_waitcnt vmcnt(3)\n\t" PCHK(r, l) POLL1(r)
#define PEND "s_add_u32 %[n], %[n], 1\n\ts_cmp_lt_u32 %[n], 0x8000\n\ts_cbranch_scc1 1b\n" \
             "2:\n\ts_mov_b64 exec, -1\n\ts_cmp_eq_u64 %[m1], -1\n\ts_cbranch_scc1 8f\n\ts_barrier\n8:"
#define POLL0(r) "global_atomic_or_x2 %[" #r "], %[q], %[z], off sc0 sc1\n\t"
struct Gate {
  const u64* p; u64 em, sm, on, tag, a, b, c, d;
  __device__ __forceinline__ void prep(int wid, const u64* set, u64 sm_, const u64* get, u64 em_, u64 on_) {
    p = wid == 0 ? set : get; em = wid == 1 ? em_ : 0; sm = wid == 0 ? sm_ : 0; on = on_;
    tag = TAG;
    asm volatile("" : "+v"(p), "+v"(tag));
  }
  __device__ __forceinline__ void seam() {
    u64 z = vz();
    asm volatile("s_cmp_eq_u64 %[on], 0\n\ts_cbranch_scc1 7f\n\ts_waitcnt vmcnt(0)\n\ts_barrier\n\t"
                 "s_cmp_eq_u64 %[sm], 0\n\ts_cbranch_scc1 6f\n\ts_mov_b64 exec, %[sm]\n\tglobal_atomic_swap_x2 %[q], %[tag], off sc1\n"
                 "6:\n\ts_cmp_eq_u64 %[em], 0\n\ts_cbranch_scc1 7f\n\ts_mov_b64 exec, %[em]\n\t" POLL0(a) "\n7:\n\ts_mov_b64 exec, -1"
                 : [a] "=&v"(a) : [q] "v"(p), [z] "v"(z), [tag] "v"(tag), [sm] "s"(sm), [em] "s"(em), [on] "s"(on) : "memory", "scc");
  }
  u64 m1;
  __device__ __forceinline__ void pass() {
    u64 z = vz(), t;
    unsigned n;
    asm volatile("s_cmp_eq_u64 %[on], 0\n\ts_cbranch_scc1 8f\n\ts_cmp_eq_u64 %[em], 0\n\ts_cbranch_scc1 2f\n\ts_mov_b64 exec, %[em]\n\ts_mov_b32 %[n], 0\n\t"
                 POLL0(b) "s_waitcnt vmcnt(1)\n\t" PCHK(a, 3) POLL1(c) POLL1(d) POLL0(a)
                 "1:\n\t" POLL3(b, 4) POLL3(c, 5) POLL3(d, 6) POLL3(a, 7) PEND
                 : [a] "+v"(a), [b] "=&v"(b), [c] "=&v"(c), [d] "=&v"(d), [n] "=&s"(n), [t] "=&s"(t), [m1] "+s"(m1)
                 : [q] "v"(p), [z] "v"(z), [tag] "v"(tag), [em] "s"(em), [on] "s"(on), [sl] "i"(PSLP) : "memory", "vcc", "scc");
  }
  __device__ __forceinline__ void done() {
    asm volatile("s_cmp_eq_u64 %[em], 0\n\ts_cbranch_scc1 9f\n\ts_waitcnt vmcnt(0)\n9:" :: "v"(a), "v"(b), "v"(c), "v"(d), [em] "s"(em) : "memory", "scc");
  }
};
struct DrainGate {
  __device__ __forceinline__ void pass() { BARRIER; WAITV(0); }
  __device__ __forceinline__ void done() {}
};
struct WaitHook : NoHook {
  static constexpr int AUX = 16;
  static constexpr bool PIN = true;
  static constexpr bool PREALL = true;
  __device__ __forceinline__ void ready() {
    BARRIER;
  }
};
struct WaitHookX : WaitHook {
  __device__ __forceinline__ void ready() { WAITV(0); BARRIER; BARRIER; }
};
struct WaitHookP : WaitHook { static constexpr bool PSW = true; };
template <class Base, int NBAR>
struct SyncHook : Base {
  static constexpr bool PREALL = true;
  __device__ __forceinline__ void first() {
    Base::first();
#pragma unroll
    for (int i = 0; i < NBAR; ++i) {
      BARRIER;
    }
    Base::afterr();
  }
};

template <int L, int MS, int NF, bool PERM = false, int FS = 1, bool HS = false>
struct FragAddr {
  static constexpr int NA = (L == KC) ? (MS == 16 ? 2 : 4) : (HS ? 2 * NF : NF);
  unsigned a[NA];
  __device__ __forceinline__ void init(LAS char* buf, int f0, int lane, int fx = 0) {
    const unsigned base = (unsigned)(size_t)buf;
    if constexpr (MS == 16) {
      const int fr = lane & 15, fq = lane >> 4, tq = (lane & 15) >> 2, tp = lane & 3;
      if constexpr (L == KC) {
        const int row = f0 * 16 + (PERM ? tq * 8 + tp : fr);
        a[0] = base + row * 128 + ((fq ^ (fr & 7)) * 16);
        a[1] = base + row * 128 + (((4 + fq) ^ (fr & 7)) * 16);
      } else {
        const int gl = ((tq >> 1) & 1) | ((fq & 1) << 1);
#pragma unroll
        for (int i = 0; i < NF; ++i) {
          const int cb = HS ? (i >> 1) * 4 + f0 + (i & 1) : (f0 + i * FS) ^ fx;
          if constexpr (PERM) a[i] = base + (fq * 8 + tq) * 128 + (cb >> 2) * 8192 + ((((cb & 2) * 2 + tp) ^ (gl * 2)) * 16) + ((cb ^ (tp >> 1)) & 1) * 8;
          else a[i] = base + (fq * 8 + tq) * 128 + tp * 8 + (cb >> 2) * 8192 + (((cb & 3) ^ gl) * 32);
          if constexpr (HS) { a[i] += (fq >> 1) * 512; a[NF + i] = a[i] ^ 512; }
        }
      }
    } else {
      const int r = lane & 31, h = lane >> 5, g = lane >> 4, tq = (lane & 15) >> 2, tp = lane & 3;
      if constexpr (L == KC) {
#pragma unroll
        for (int t = 0; t < 4; ++t) a[t] = base + (f0 * 32 + r) * 128 + (((2 * t + h) ^ ((r >> 1) & 7)) * 16);
      } else {
#pragma unroll
        for (int i = 0; i < NF; ++i) {
          const int cb = (f0 + i) ^ fx;
          a[i] = base + (8 * (g >> 1) + tq) * 128 + (cb >> 1) * 8192 + (((cb & 1) ^ ((tq >> 1) & 1)) * 64) + (g & 1) * 32 + tp * 8;
        }
      }
    }
#pragma unroll
    for (int i = 0; i < NA; ++i) asm volatile("" : "+v"(a[i]));
  }
  __device__ __forceinline__ bf16x8 read(int so, int i, int t) const {
    if constexpr (L == KC) {
      return *(const LAS bf16x8*)((LAS char*)(size_t)a[t] + (so + (PERM ? (i >> 1) * 4096 + (i & 1) * 512 : i * (MS * 128))));
    } else {
      LAS char* p = (LAS char*)(size_t)a[i] + (so + t * (MS == 16 ? 4096 : 2048));
      s16x4 lo = __builtin_amdgcn_ds_read_tr16_b64_v4i16((LAS s16x4*)(p));
      s16x4 hi = __builtin_amdgcn_ds_read_tr16_b64_v4i16((LAS s16x4*)(HS ? (LAS char*)(size_t)a[HS ? NF + i : i] + (so + t * 4096) : p + 512));
      return __builtin_shufflevector(lo, hi, 0, 1, 2, 3, 4, 5, 6, 7);
    }
  }
};

template <int NM, int PER>
__device__ __forceinline__ void interleave() {
#pragma unroll
  for (int i = 0; i < NM; ++i) {
    __builtin_amdgcn_sched_group_barrier(0x8, 1, 0);
    __builtin_amdgcn_sched_group_barrier(0x100, PER, 0);
  }
}

template <int PL, int QL, int FP, int FQ, int NS, int PSTG, int QSTG, int NT, bool SPLIT, class G, class Late, class Mid, class Tick>
__device__ __forceinline__ void consumer16w(LAS char* pbuf, LAS char* qbuf, f32x4 (&acc)[FP][FQ], int fp0, int fq0, int lane, G& gate, Late&& late, Mid&& mid, Tick&& tick) {
  FragAddr<PL, 16, FP, true> pa; pa.init(pbuf, fp0, lane);
  FragAddr<QL, 16, FQ, false, 1, QL == KM && HSW> qa; qa.init(qbuf, fq0, lane);
  struct Fr { bf16x8 p[2][FP]; bf16x8 q[2][FQ]; };
  auto rdt = [&](Fr& f, int slot, int t) {
#pragma unroll
    for (int i = 0; i < FP; ++i) f.p[t][i] = pa.read(slot * PSTG, i, t);
#pragma unroll
    for (int j = 0; j < FQ; ++j) f.q[t][j] = qa.read(slot * QSTG, j, t);
  };
  auto rd = [&](Fr& f, int slot) { rdt(f, slot, 0); rdt(f, slot, 1); };
  auto mmr = [&](const Fr& f, int j0, int j1, int t0 = 0, int t1 = 2) {
#pragma unroll
    for (int t = t0; t < t1; ++t)
#pragma unroll
      for (int j = j0; j < j1; ++j)
#pragma unroll
        for (int ii = 0; ii < FP; ++ii) { const int i = (j & 1) ? FP - 1 - ii : ii; acc[i][j] = __builtin_amdgcn_mfma_f32_16x16x32_bf16(f.p[t][i], f.q[t][j], acc[i][j], 0, 0, 0); }
  };
  constexpr int U = NS;
  constexpr int NG = (NT - 2 - SPLIT) / U, REM = (NT - 1) % U;
  Fr f[2];
  auto step = [&](auto uc) {
    constexpr int u = (decltype(uc)::value + 1) % U;
    SCHED0; WAITL0; BARRIER; SCHED0;
    rd(f[(u + 1) & 1], (u + 1) % NS);
    mmr(f[u & 1], 0, FQ);
    interleave<2 * FP * FQ, FQ < 4 ? 2 : 1>();
  };
  if constexpr (FQ < 4) {
    gate.pass();
    late();
  }
#pragma unroll
  for (int i = 0; i < FP; ++i)
#pragma unroll
    for (int j = 0; j < FQ; ++j) asm volatile("" : "+v"(acc[i][j]));
  if constexpr (FQ == 4) gate.pass();
  BARRIER;
  rdt(f[0], 0, 0);
  SCHED0;
  rdt(f[0], 0, 1);
  SCHED0;
  mmr(f[0], 0, FQ, 0, 1);
  SCHED0; gate.done(); WAITL0; BARRIER; SCHED0;
  rd(f[1], 1);
  mmr(f[0], 0, FQ, 1, 2);
  interleave<FP * FQ, FQ < 4 ? 3 : 2>();
#pragma unroll 1
  for (int g = 0; g < NG; ++g) static_for<U>(step);
  if constexpr (!SPLIT) {
    static_for<REM - 1>(step);
    SCHED0; WAITL0; BARRIER; SCHED0;
    if constexpr (FQ < 4) {
      mmr(f[REM & 1], 0, 1);
      mmr(f[REM & 1], 1, 2);
    } else mid([&](int n0, int n1) {
#pragma unroll
      for (int n = n0; n < n1; ++n) {
        const int t = (n >> 2) & 1, i = (n >> 4) * 2 + (n & 1), j = ((n >> 3) & 1) * 2 + ((n >> 1) & 1);
        acc[i][j] = __builtin_amdgcn_mfma_f32_16x16x32_bf16(f[REM & 1].p[t][i], f[REM & 1].q[t][j], acc[i][j], 0, 0, 0);
      }
    });
  } else {
    static_for<(NT - 3) % U>(step);
    SCHED0; WAITL0; BARRIER; SCHED0;
    rd(f[REM & 1], REM % NS);
    mmr(f[(REM - 1) & 1], FQ / 2, FQ);
    interleave<FP * FQ, 2>();
    SCHED0; WAITL0; BARRIER; SCHED0;
    auto m1 = [&](const Fr& g, int t, int i, int j) { acc[i][j] = __builtin_amdgcn_mfma_f32_16x16x32_bf16(g.p[t][i], g.q[t][j], acc[i][j], 0, 0, 0); };
#pragma unroll
    for (int i = 0; i <= FP; ++i) {
      SCHED0;
#pragma unroll
      for (int n = 0; n < FQ; ++n) {
        if (i < FP) m1(f[REM & 1], n / (FQ / 2), i, FQ / 2 + n % (FQ / 2));
        else m1(f[(REM - 1) & 1], 0, n, 0);
      }
      if (i > 0) {
        mid(i - 1);
        __builtin_amdgcn_sched_group_barrier(0x8, 2, 0);
        __builtin_amdgcn_sched_group_barrier(0x2, 2, 0);
        __builtin_amdgcn_sched_group_barrier(0x8, 1, 0);
        __builtin_amdgcn_sched_group_barrier(0x2, 2, 0);
        __builtin_amdgcn_sched_group_barrier(0x8, 1, 0);
        __builtin_amdgcn_sched_group_barrier(0x2, 1, 0);
        __builtin_amdgcn_sched_group_barrier(0x40, 1, 0);
      }
    }
    SCHED0;
#pragma unroll
    for (int i = 0; i < FP; ++i) m1(f[(REM - 1) & 1], 1, i, 0);
    mmr(f[(REM - 1) & 1], 1, FQ / 2);
    static_for<2 * KD + 1>([&](auto kc) {
      constexpr int k = decltype(kc)::value, n = NT - 1 + k;
      SCHED0;
      if constexpr (k > 0) { tick(k - 1); WAITL0; BARRIER; SCHED0; }
      if constexpr (k < 2 * KD) {
#pragma unroll
        for (int t = 0; t < 2; ++t) {
#pragma unroll
          for (int i = 0; i < FP; ++i) f[(n + 1) & 1].p[t][i] = pa.read(((n + 1) % NS) * PSTG, i, t);
#pragma unroll
          for (int j = 0; j < FQ / 2; ++j) f[(n + 1) & 1].q[t][j] = qa.read(((n + 1) % NS) * QSTG, j, t);
        }
      } else late();
      mmr(f[n & 1], 0, FQ / 2);
      if constexpr (k < 2 * KD) interleave<FP * FQ, 2>();
    });
    SCHED0;
    tick(2 * KD);
  }
}

template <int NQT, int NPT, int RQ, int RP>
__device__ __forceinline__ void tile_map(unsigned BID, int& tq, int& tp) {
  static_assert((NQT / RQ) * (NPT / RP) == 8 && NQT % RQ == 0 && NPT % RP == 0, "8 regions");
  const int x = BID & 7, j = BID >> 3;
  constexpr int GP = NPT / RP;
  tq = (x / GP) * RQ + j % RQ;
  tp = (x % GP) * RP + j / RQ;
}

__device__ __forceinline__ void k4_map(unsigned BID, int& kh, int& tp, int& tq) {
  const int x8 = BID & 7, j8 = BID >> 3;
  kh = x8 & 1; tp = ((x8 >> 1) & 1) * 4 + (j8 >> 3); tq = (x8 >> 2) * 8 + (j8 & 7);
}

constexpr int K1_NS = 3, K1_PSTG = 192 * 128, K1_QSTG = 144 * 128;
constexpr int K1_QOFF = L_B, K1_ST = L_D;

__device__ __forceinline__ float max2(float a, float b) { return __builtin_elementwise_maximum(a, b); }
__device__ __forceinline__ float max3(float a, float b, float c) { return max2(max2(a, b), c); }
__device__ __forceinline__ float rows_sum(float v) {
  auto a = __builtin_amdgcn_permlane16_swap(__float_as_uint(v), __float_as_uint(v), false, false);
  v = __uint_as_float(a[0]) + __uint_as_float(a[1]);
  auto b = __builtin_amdgcn_permlane32_swap(__float_as_uint(v), __float_as_uint(v), false, false);
  return __uint_as_float(b[0]) + __uint_as_float(b[1]);
}

__device__ __forceinline__ float half_sum(float v) {
  auto b = __builtin_amdgcn_permlane32_swap(__float_as_uint(v), __float_as_uint(v), false, false);
  return __uint_as_float(b[0]) + __uint_as_float(b[1]);
}
__device__ __forceinline__ f32x2 ln_coef(float sm, float sq) {
  const float mean = sm * (1.0f / D_);
  const float var = fmaxf(__builtin_fmaf(-mean, mean, sq * (1.0f / D_)), 0.f);
  const float rs = __builtin_amdgcn_rcpf(rbf(__builtin_amdgcn_sqrtf(rbf(rbf(var) + 1e-5f))));
  return f32x2{rs, -rbf(mean) * rs};
}

constexpr u64 win_mask(int r, int lo, int hi) {
  u64 m = 0;
  for (int l = 0; l < 64; ++l) { const int d = 4 * (l >> 4) + r - (l & 15); if (d >= lo && d <= hi) m |= 1ull << l; }
  return m;
}
template <int R> __device__ __forceinline__ void win_bias(unsigned ninf, float& a, float& b) {
  constexpr u64 ma = ~win_mask(R, 9, 64), mb = ~win_mask(R, -7, 0);
  static_assert((unsigned)ma == ~0u, "mask");
  asm volatile("s_mov_b32 vcc_lo, %3\n\ts_mov_b32 vcc_hi, %4\n\tv_cndmask_b32 %1, 0, %2, vcc\n\t"
               "s_mov_b32 vcc_lo, -1\n\ts_mov_b32 vcc_hi, %5\n\tv_cndmask_b32 %0, 0, %2, vcc"
               : "=&v"(a), "=&v"(b) : "v"(ninf), "i"((int)mb), "i"((int)(mb >> 32)), "i"((int)(ma >> 32)) : "vcc");
}

template <class Pre>
__device__ __forceinline__ void k1_consumer(LAS char* lds, int lane, int wv, bool l0, int tq, const char* ob, Gate& gate, Pre&& pre) {
  u64 qm = l0 ? 0x0001000100010001ull : 0ull;
  asm volatile("" : "+v"(lane), "+s"(ob), "+s"(qm));
  const int p = wv & 1;
  f32x4 acc[6][4], hk, hv, hq;
  FragAddr<KM, 16, 3, false, 4> pa[2];
  pa[0].init(lds + L_A, wv, lane); pa[1].init(lds + L_A, wv ^ 1, lane);
  FragAddr<KC, 16, 4> qa, ha;
  qa.init(lds + K1_QOFF, 1 + 4 * p, lane);
#pragma unroll
  for (int t = 0; t < 2; ++t) { ha.a[t] = (qa.a[t] - (1 + 4 * p) * 2048) ^ ((1 - p) * 1024); asm volatile("" : "+v"(ha.a[t])); }
  struct Fr { bf16x8 p[6], q[4], h; };
  auto rd = [&](Fr& f, int slot, int t) {
    f.q[0] = qa.read(slot * K1_QSTG, 0, t);
#pragma unroll
    for (int i = 0; i < 6; ++i) f.p[i] = pa[i & 1].read(slot * K1_PSTG, i >> 1, t);
#pragma unroll
    for (int j = 1; j < 4; ++j) f.q[j] = qa.read(slot * K1_QSTG, j, t);
    f.h = ha.read(slot * K1_QSTG, 0, t);
  };
  auto m1 = [&](const Fr& f, int n) {
    const int j = n / 6, i = (j & 1) ? 5 - n % 6 : n % 6;
    acc[i][j] = i < 4 ? __builtin_amdgcn_mfma_f32_16x16x32_bf16(f.p[i], f.q[j], acc[i][j], 0, 0, 0) : __builtin_amdgcn_mfma_f32_16x16x32_bf16(f.q[j], f.p[i], acc[i][j], 0, 0, 0);
  };
  auto mh = [&](const Fr& f) {
    hk = __builtin_amdgcn_mfma_f32_16x16x32_bf16(f.p[2], f.h, hk, 0, 0, 0);
    hv = __builtin_amdgcn_mfma_f32_16x16x32_bf16(f.h, f.p[4], hv, 0, 0, 0);
  };
  auto mm = [&](const Fr& f) {
#pragma unroll
    for (int n = 0; n < HQP; ++n) m1(f, n);
    interleave<HQP, 1>();
    SCHED0;
    if (l0) hq = __builtin_amdgcn_mfma_f32_16x16x32_bf16(f.p[0], f.h, hq, 0, 0, 0);
    SCHED0;
#pragma unroll
    for (int n = HQP; n < 24; ++n) m1(f, n);
    mh(f);
    SCHED0;
  };
  const f32x4 z4 = {0.f, 0.f, 0.f, 0.f};
#pragma unroll
  for (int i = 0; i < 6; ++i)
#pragma unroll
    for (int j = 0; j < 4; ++j) { acc[i][j] = z4; asm volatile("" : "+v"(acc[i][j])); }
  hk = z4; hv = z4; hq = z4;
  asm volatile("" : "+v"(hk), "+v"(hv), "+v"(hq));
  Fr f0, f1;
  gate.pass();
  pre();
  BARRIER;
  rd(f0, 0, 0);
  SCHED0;
  gate.done(); WAITL0; SCHED0;
#pragma unroll 1
  for (int g = 0; g < 5; ++g)
    static_for<3>([&](auto uc) {
      constexpr int u = decltype(uc)::value;
      rd(f1, u, 1);
      mm(f0);
      if constexpr (u == 1) WAITV(0);
      WAITL0; BARRIER; SCHED0;
      rd(f0, (u + 1) % 3, 0);
      mm(f1);
      WAITL0;
      SCHED0;
    });
  constexpr int L_HX = L_B + 57344;
  asm volatile("" : "+v"(lane));
  const int fr = lane & 15, fq16 = lane & 48;
  unsigned dw = L_HX + wv * 2048 + (lane & 63) * 16, cq0 = L_CSQ + wv * 64 + fq16;
  f1.h = ha.read(0, 0, 1);
#pragma unroll
  for (int i = 0; i < 5; ++i) { const int k = (2 * i + 2) % 6 + (i >= 3); f1.p[k] = pa[k & 1].read(0, k >> 1, 1); }
  mh(f0);
#pragma unroll
  for (int n = 0; n < 9; ++n) m1(f0, n);
#pragma unroll
  for (int n = 0; n < 11; ++n) { __builtin_amdgcn_sched_group_barrier(0x8, 1, 0); __builtin_amdgcn_sched_group_barrier(0x100, 1, 0); __builtin_amdgcn_sched_group_barrier(0x2, 1, 0); }
  asm volatile("" : "+v"(dw), "+v"(cq0));
  SCHED0;
  mh(f1);
  if (l0) hq = __builtin_amdgcn_mfma_f32_16x16x32_bf16(f1.p[0], f1.h, __builtin_amdgcn_mfma_f32_16x16x32_bf16(f0.p[0], f0.h, hq, 0, 0, 0), 0, 0, 0);
  SCHED0;
  f1.p[1] = pa[1].read(0, 0, 1);
  f1.q[0] = qa.read(0, 0, 1);
#pragma unroll
  for (int n = 9; n < 12; ++n) m1(f0, n);
  interleave<3, 1>();
  SCHED0;
  *(LAS f32x4*)(size_t)dw = hk;
  m1(f0, 12); m1(f0, 13);
  SCHED0;
  *(LAS f32x4*)((LAS char*)(size_t)dw + 1024) = hv;
  m1(f0, 14); m1(f0, 15);
  SCHED0;
  asm volatile("s_mov_b64 exec, %2\n\tds_write_b128 %0, %1\n\ts_mov_b64 exec, -1" ::"v"(cq0), "v"(hq), "s"(qm) : "memory");
  SCHED0;
  unsigned a2 = K1_ST + (16 + 64 * p) * 8 + fr * 8, a4 = L_RS + (16 + 64 * p) * 4 + fq16;
  unsigned h2 = K1_ST + ((fr ^ ((1 - p) * 8)) * 8), h4 = L_RS + (fq16 ^ ((1 - p) * 32));
  unsigned ma = L_MSK + (lane & 63) * 16, ck0 = L_HX + wv * 2048 + fq16 * 16, cv0 = L_HX + 1024 + wv * 2048 + fr * 16;
#pragma unroll
  for (int j = 1; j < 4; ++j) f1.q[j] = qa.read(0, j, 1);
#pragma unroll
  for (int n = 16; n < 24; ++n) m1(f0, n);
#pragma unroll
  for (int n = 0; n < 8; ++n) { __builtin_amdgcn_sched_group_barrier(0x8, 1, 0); __builtin_amdgcn_sched_group_barrier(0x100, 1, 0); __builtin_amdgcn_sched_group_barrier(0x2, 2, 0); }
  asm volatile("" : "+v"(a2), "+v"(a4), "+v"(h2), "+v"(h4), "+v"(ma), "+v"(ck0), "+v"(cv0));
  SCHED0; WAITL0; BARRIER; SCHED0;
  unsigned oo[4];
  {
    const bool hole = ((p + (fr & 14)) & 7) == 3;
    oo[0] = phys<64>(p + (fr & 14)) * 128 + (fr & 1) * 64 + (((lane & 16) * 2) ^ (p * 32)) + ((lane & 32) >> 1);
    const unsigned e1 = hole ? 8 : 11, e2 = hole ? 10 : 12;
    oo[1] = oo[0] + (1u << e1); oo[2] = oo[0] + (1u << e2); oo[3] = oo[2] + (1u << e1);
  }
  f32x2 s2[5];
  f32x4 rs[5], mr[5], cs[4], pk_, pv_, bA, bB, b0;
  float cv[2];
  bA = *(const LAS f32x4*)(size_t)ma;
  cs[0] = *(const LAS f32x4*)(size_t)cq0; cs[1] = *(const LAS f32x4*)(size_t)(cq0 ^ 64);
  cs[2] = *(const LAS f32x4*)(size_t)ck0; cs[3] = *(const LAS f32x4*)(size_t)(ck0 ^ 2048);
#pragma unroll
  for (int j = 0; j < 5; ++j) s2[j] = j < 4 ? *(const LAS f32x2*)((LAS char*)(size_t)a2 + j * 128) : *(const LAS f32x2*)(size_t)h2;
  pk_ = *(const LAS f32x4*)(size_t)(dw ^ (2048 + 128)); pv_ = *(const LAS f32x4*)(size_t)(dw ^ (2048 + 1024 + 512));
  bB = *(const LAS f32x4*)((LAS char*)(size_t)ma + 1024);
  cv[0] = *(const LAS float*)(size_t)cv0; cv[1] = *(const LAS float*)(size_t)(cv0 ^ 2048);
#pragma unroll
  for (int j = 0; j < 5; ++j) {
    rs[j] = j < 4 ? *(const LAS f32x4*)((LAS char*)(size_t)a4 + j * 64) : *(const LAS f32x4*)(size_t)h4;
    mr[j] = j < 4 ? *(const LAS f32x4*)((LAS char*)(size_t)a4 + j * 64 + 576) : *(const LAS f32x4*)((LAS char*)(size_t)h4 + 576);
  }
  const unsigned nm = (unsigned)((tq - 1) >> 31) & 0xFF61B1E6u;
  int o1 = OPQ(0x3F803F80);
  bf16x8 ones = __builtin_bit_cast(bf16x8, u32x4{(unsigned)o1, (unsigned)o1, (unsigned)o1, (unsigned)o1});
#pragma unroll
  for (int n = 0; n < 24; ++n) m1(f1, n);
#pragma unroll
  for (int n = 0; n < 24; ++n) { __builtin_amdgcn_sched_group_barrier(0x8, 1, 0); __builtin_amdgcn_sched_group_barrier(0x2, 1, 0); __builtin_amdgcn_sched_group_barrier(0x100, 1, 0); __builtin_amdgcn_sched_group_barrier(0x2, 1, 0); }
  asm volatile("" : "+v"(oo[0]), "+v"(oo[1]), "+v"(oo[2]), "+v"(oo[3]), "+v"(ones));
  SCHED0;
  __builtin_amdgcn_s_waitcnt(0xCA7F);
  ALIGN8;
  auto qk = [&](const f32x4& a, const f32x4& c, const f32x2& s) { const f32x4 v = a * s[0] + c * s[1]; return u32x2{pk2(v[0], v[1]), pk2(v[2], v[3])}; };
  auto cat = [](const u32x2& a, const u32x2& b) { return __builtin_bit_cast(bf16x8, u32x4{a[0], a[1], b[0], b[1]}); };
  bf16x8 kf[5], qf[4];
#pragma unroll
  for (int j = 0; j < 4; ++j) {
    kf[j + 1] = cat(qk(acc[2][j], cs[2], s2[j]), qk(acc[3][j], cs[3], s2[j]));
    qf[j] = cat(qk(acc[0][j], cs[0], s2[j]), qk(acc[1][j], cs[1], s2[j]));
  }
  kf[0] = cat(qk(hk, cs[2], s2[4]), qk(pk_, cs[3], s2[4]));
  SCHED0;
  const float scale = 0.17677669529663687f, l2e = 1.4426950408889634f;
  f32x2 l[4][4];
  float mx[4];
#pragma unroll
  for (int r = 0; r < 4; ++r) b0[r] = __uint_as_float(__float_as_uint(bA[r]) | nm);
#pragma unroll
  for (int jj = 0; jj < 4; ++jj) {
    const int j = (jj + 1) & 3;
    const f32x4 sA = __builtin_amdgcn_mfma_f32_16x16x32_bf16(kf[j], qf[j], j == 0 ? b0 : bA, 0, 0, 0);
    const f32x4 sB = __builtin_amdgcn_mfma_f32_16x16x32_bf16(kf[j + 1], qf[j], bB, 0, 0, 0);
    l[j][0] = f32x2{sA[0], sA[1]}; l[j][1] = f32x2{sA[2], sA[3]}; l[j][2] = f32x2{sB[0], sB[1]}; l[j][3] = f32x2{sB[2], sB[3]};
    mx[j] = max3(max3(l[j][0][0], l[j][0][1], l[j][1][0]), max3(l[j][1][1], l[j][2][0], l[j][2][1]), max2(l[j][3][0], l[j][3][1]));
  }
  __builtin_amdgcn_sched_group_barrier(0x8, 2, 0);
#pragma unroll
  for (int n = 0; n < 6; ++n) { __builtin_amdgcn_sched_group_barrier(0x2, 2, 0); __builtin_amdgcn_sched_group_barrier(0x8, 1, 0); }
  SCHED0;
  WAITL0;
  ALIGN8;
  u32x12 vp[2];
  auto vv = [&](int e, int k, const f32x4& a, int j) { const f32x4 v = a * rs[j] + mr[j] * cv[e]; vp[e][2 * k] = pk2(v[0], v[1]); vp[e][2 * k + 1] = pk2(v[2], v[3]); };
  asm volatile("" : "=v"(vp[0]), "=v"(vp[1]));
  vv(0, 0, hv, 4); vv(1, 0, pv_, 4);
#pragma unroll
  for (int j = 0; j < 4; ++j) { vv(0, j + 1, acc[4][j], j); vv(1, j + 1, acc[5][j], j); }
  {
    auto mx2 = [](auto r) { return __float_as_uint(max2(__uint_as_float(r[0]), __uint_as_float(r[1]))); };
    const unsigned a = mx2(__builtin_amdgcn_permlane16_swap(__float_as_uint(mx[0]), __float_as_uint(mx[1]), false, false));
    const unsigned b = mx2(__builtin_amdgcn_permlane16_swap(__float_as_uint(mx[2]), __float_as_uint(mx[3]), false, false));
    const unsigned c = __float_as_uint(__uint_as_float(mx2(__builtin_amdgcn_permlane32_swap(a, b, false, false))) * (-scale * l2e));
    const auto d = __builtin_amdgcn_permlane32_swap(c, c, false, false);
    const auto e0 = __builtin_amdgcn_permlane16_swap(d[0], d[0], false, false), e1 = __builtin_amdgcn_permlane16_swap(d[1], d[1], false, false);
    mx[0] = __uint_as_float(e0[0]); mx[1] = __uint_as_float(e0[1]); mx[2] = __uint_as_float(e1[0]); mx[3] = __uint_as_float(e1[1]);
  }
#pragma unroll
  for (int j = 0; j < 4; ++j)
#pragma unroll
    for (int e = 0; e < 4; ++e) l[j][e] = __builtin_elementwise_fma(l[j][e], f32x2{scale * l2e, scale * l2e}, f32x2{mx[j], mx[j]});
  asm volatile("" : "+v"(vp[0]), "+v"(vp[1]));
  SCHED0;
  f32x4 oA[4], oB[4], zs[4];
  bf16x8 pf[4];
  auto ex = [&](int j) {
    u32x4 pp;
#pragma unroll
    for (int e = 0; e < 4; ++e) pp[e] = pk2(__builtin_amdgcn_exp2f(l[j][e][0]), __builtin_amdgcn_exp2f(l[j][e][1]));
    pf[j] = __builtin_bit_cast(bf16x8, pp);
  };
  auto pv = [&](auto jc) {
    constexpr int j = decltype(jc)::value;
    zs[j] = __builtin_amdgcn_mfma_f32_16x16x32_bf16(ones, pf[j], z4, 0, 0, 0);
    oA[j] = __builtin_amdgcn_mfma_f32_16x16x32_bf16(__builtin_bit_cast(bf16x8, __builtin_shufflevector(vp[0], vp[0], 2 * j, 2 * j + 1, 2 * j + 2, 2 * j + 3)), pf[j], z4, 0, 0, 0);
    oB[j] = __builtin_amdgcn_mfma_f32_16x16x32_bf16(__builtin_bit_cast(bf16x8, __builtin_shufflevector(vp[1], vp[1], 2 * j, 2 * j + 1, 2 * j + 2, 2 * j + 3)), pf[j], z4, 0, 0, 0);
  };
  auto out = [&](int j) {
    const float iz = __builtin_amdgcn_rcpf(zs[j][0]);
    asm volatile("" ::"v"(zs[j]));
    const f32x4 a = oA[j] * iz, b = oB[j] * iz;
    const auto x0 = __builtin_amdgcn_permlane16_swap(pk2(a[0], a[1]), pk2(b[0], b[1]), false, false);
    const auto x1 = __builtin_amdgcn_permlane16_swap(pk2(a[2], a[3]), pk2(b[2], b[3]), false, false);
    const u32x4 ov = {x0[0], x1[0], x0[1], x1[1]};
    asm volatile("global_store_dwordx4 %0, %1, %2 sc1\n\ts_nop 1" ::"v"(oo[j]), "v"(ov), "s"(ob) : "memory");
    SCHED0;
  };
  ex(0);
  SCHED0;
  static_for<3>([&](auto jc) {
    constexpr int j = decltype(jc)::value;
    pv(jc);
    ex(j + 1);
#pragma unroll
    for (int n = 0; n < 3; ++n) { __builtin_amdgcn_sched_group_barrier(0x8, 1, 0); __builtin_amdgcn_sched_group_barrier(0x400, 1, 0); }
    SCHED0;
    if constexpr (j == 2) { pv(std::integral_constant<int, 3>{}); SCHED0; }
    out(j);
  });
  out(3);
}

constexpr int L_FLG = L_A + 65536;
template <int OFF> __device__ __forceinline__ void fetch_flags(const void* p) {
  asm volatile("s_mov_b32 m0, %1\n\ts_nop 0\n\tglobal_load_lds_dwordx4 %0, off sc1" ::"v"(p), "i"(L_FLG + OFF) : "memory", "m0");
}
__device__ __forceinline__ bool flags_set(unsigned a, u64 tag) {
  u32x4 v;
  asm volatile("ds_read_b128 %0, %1\n\ts_waitcnt lgkmcnt(0)" : "=v"(v) : "v"(a) : "memory");
  const unsigned lo = (unsigned)tag, hi = (unsigned)(tag >> 32);
  return __builtin_amdgcn_ballot_w64((((v[0] ^ lo) | (v[1] ^ hi)) | ((v[2] ^ lo) | (v[3] ^ hi))) != 0) == 0;
}
template <int N>
__device__ __forceinline__ void wait_tags(const u64* p, u64 tag) {
#pragma unroll 1
  for (int spin = 0; spin < (1 << 16); ++spin) {
    bool ok = true;
#pragma unroll
    for (int i = 0; i < N; ++i) ok &= __hip_atomic_load(p + i, __ATOMIC_RELAXED, __HIP_MEMORY_SCOPE_AGENT) == tag;
    if (__all(ok)) break;
    __builtin_amdgcn_s_sleep(8);
  }
  CFENCE;
}
__device__ __forceinline__ const u64* first_flags(const u64* ctl0, int lane) {
  OPAQUE_BID;
  return ctl0 + C_OCNT + ((lane >> 2) * NR_O + (BID & 3) % NR_O) * RS1 + (lane & 3) * 4;
}
__device__ __forceinline__ void wait_first_phase(LAS char* lds, const u64* ctl0, int lane) { wait_tags<4>(first_flags(ctl0, lane), TAG); }
__device__ __forceinline__ const u64* x_flag(const u64* ctl, int tq, int tp, int lane) {
  return ctl + C_XCNT + (max(tq - ((lane >> 4) & 1), 0) * NR_X + ((tp + ((lane >> 4) & 1) * (NR_X / 2)) % NR_X)) * RS1 + (lane & 15);
}

__device__ __forceinline__ f32x2 uadd(f32x2 a, f32x2 b) {
  float x, y;
  asm("v_add_f32 %0, %2, %4\n\tv_add_f32 %1, %3, %5" : "=&v"(x), "=v"(y) : "v"(a[0]), "v"(a[1]), "v"(b[0]), "v"(b[1]));
  return f32x2{x, y};
}
constexpr size_t MB = 1 << 20;
constexpr size_t W_XA = 0, W_H = 4 * MB, W_O = 10 * MB, W_U = 16 * MB, W_PART = 26 * MB;
constexpr size_t W_XCH = 30 * MB, W_CTL = 31 * MB, W_PST = 33 * MB, W_EPOCH = 34 * MB, W_CW = 35 * MB;

#define SF1 5
#define SCW 13
#define SCO (SCW < 12 ? SCW + 3 : 99)
template <bool ISQ>
struct K1Hook : NoHook {
  static constexpr int AUX = ISQ ? 16 : 0;
  Stream<KM, 16, 64> so;
  static constexpr bool PREALL = true, PIN = ISQ, UNROLL = true, COLD = true;
  __device__ __forceinline__ bool cold() const { return l0; }
  static constexpr int SG = SF1 + 3;
  static constexpr int NG = ISQ ? 4 : 1;
  bool l0; int tq, tp, lw, lane; LAS char* lds;
  const u32x2* pstat; u64* ctl; u32x4* xch;
  const u16* wo; const u16* w1; const u16* w2; const u16* wq;
  const u32x2* sp; unsigned sa; const char* tw;
  f32x2 g[4 * NG];
  u64 fx; const u64* fp;
  __device__ __forceinline__ void ready() {
    if constexpr (ISQ) { if (!l0) { BARRIER; BARRIER; } }
  }
  __device__ __forceinline__ void first() {
    asm volatile("" : "+v"(lane));
    if constexpr (ISQ) {
      OPAQUE_BID;
      const int b = BID, l24 = lane % 24, r = l24 / 12, c = l24 - r * 12;
      tw = (const char*)wq + (256 + (((b >> 2) & 1) * 32 + (b >> 3)) * 4 + (lw - 2) * 2 + r) * 6144 + (c >> 2) * 2048 + ((b & 3) * 4 + (c & 3)) * 128;
      asm volatile("" : "+v"(tw));
      asm volatile("global_load_dwordx2 %0, %1, %2 sc1" : "=&v"(fx) : "v"(vconst<0>()), "s"((const char*)pstat + (W_EPOCH - W_PST)) : "memory");
    }
  }
  static constexpr int ng(int s) { return (s >= SG && s < SG + 4) ? NG : 0; }
  static constexpr int tstep(int i) { return i == 3 ? 3 : 0; }
  static constexpr int nt(int s) { int n = 0; for (int i = 0; i < 4; ++i) n += ISQ && tstep(i) == s; return n; }
  static constexpr int gs(int s) { return (!ISQ && s >= G0 && (s - G0) % GD == 0 && (s - G0) / GD < GN) ? (s - G0) / GD : -1; }
  int grp, duty; unsigned go;
  static constexpr int lx(int s) { return nt(s) + (!ISQ && s == FWS ? 1 : 0); }
  template <int S, int N> __device__ __forceinline__ void wait() {
    constexpr int A = N + (S < 13 ? ng(S - 1) + ng(S - 2) : 0), X = lx(S - 1) + lx(S - 2);
    if constexpr (S < 13 && (gs(S - 1) >= 0 || gs(S - 2) >= 0)) {
      if (grp == gs(S - 1) || grp == gs(S - 2)) waitv<A + X + 4>();
      else if (X > 0 && l0) waitv<A + X>();
      else waitv<A>();
    } else if constexpr (S == SCW + 1 || S == SCW + 2) {
      static_assert(X == 0, "copy step");
      if (duty >= 0) waitv<A + 4>(); else waitv<A>();
    } else {
      if (X > 0 && l0) waitv<A + X>(); else waitv<A>();
    }
  }
  template <int I> __device__ __forceinline__ f32x2 sum4() {
    asm volatile("" : "+v"(g[I]), "+v"(g[I + 1]), "+v"(g[I + 2]), "+v"(g[I + 3]));
    return uadd(uadd(g[I], g[I + 1]), uadd(g[I + 2], g[I + 3]));
  }
  template <int S> __device__ __forceinline__ void stepc() {
    if constexpr (nt(S) > 0) {
      if (l0) {
#pragma unroll
        for (int i = 0; i < 3; ++i) if (tstep(i) == S) touch_line(tw + i * 256 * 6144, (unsigned)(size_t)lds + L_SCR);
        if (tstep(3) == S) touch_line((const char*)wo + (size_t)((opq_bid() * 2 + lw - 2) * 32 + (lane & 31)) * 128, (unsigned)(size_t)lds + L_SCR);
      }
    }
    if constexpr (!ISQ && PF2 && S == 1) {
      OPAQUE_BID;
      int tq2, tp2;
      tile_map<16, 16, 2, 16>(BID, tq2, tp2);
      so.init(Operand{wo, D_, tp2 * 64, 64, D_ * D_ * 2, 0, false, nullptr, 0}, lw, lane);
      so.pin();
    }
    if constexpr (!ISQ && PF2 && S == WOT) {
      if (lw == 1) {
        OPAQUE_BID;
        const int cb0 = (lane >> 5) * 1024, cf = (3 - (int)(((size_t)wo + cb0) >> 7)) & 7;
        touch_line((const char*)wo + (size_t)((BID >> 3) * 32 + (lane & 31)) * (D_ * 2) + cb0 + cf * 128, (unsigned)(size_t)lds + L_SCR);
      }
    }
    if constexpr (S == (ISQ ? 4 : 2)) {
      asm volatile("" : "+v"(lane));
      const int trow = ISQ ? (lw - 2) * 64 + lane : 128 + (lane & 15);
      const int row = max(tq * 128 - 16 + trow, 0), line = (trow >> 1) + (trow < 16 ? (trow & 1) * 8 : 8 + (trow & 1) * 64);
      sp = pstat + (size_t)(row >> 5) * 512 + (row & 31) + (ISQ ? 0 : (lane >> 4) * 128);
      sa = (unsigned)(size_t)lds + K1_ST + line * 8;
      asm volatile("" : "+v"(sp), "+v"(sa));
    }
    if constexpr (!ISQ && S == ARMS) {
      if (lw == 0) { const unsigned m = vconst<~0u>(); st16(xch + ((tq * 4 + (lane >> 4)) * 16 + tp) * 16 + (lane & 15), u32x4{m, m, m, m}); }
    }
    if constexpr (!ISQ && S == FWS) {
      if (l0) {
        static_assert(NR_O == 8 && NR_H == 8 && NR_U == 8 && NR_X == 8 && (RS1 == RS2 || RS2 == 2 * RS1), "flag lines");
        const int i = opq_bid() * 7 + min(lane, 6);
        constexpr int N1 = 1024 + (C_END - C_UCNT) / RS1;
        const int wd = i < 1024 ? i * RSP : i < N1 ? C_UCNT + (i - 1024) * RS1 : C_HCNT + 16 + min(i - N1, 63) * RS2;
        touch_line(ctl + lw * C_WORDS + wd, L_SCR);
      }
    }
    if constexpr (S == 0) {
      const int c = (3 - (int)((size_t)wq >> 7)) & 7;
      duty = l0 && (tp & 7) != c ? tq * 14 + tp - (tp > c) - (tp > c + 8) : -1;
      asm volatile("" : "+s"(duty));
    }
    if constexpr (!ISQ && S == 0) {
      grp = duty >= 0 ? (int)((opq_bid() >> 3) % GN) : 99;
      const unsigned line = (duty * 2 + lw) * 64 + lane;
      go = ((((unsigned)((size_t)w1 >> 7) + line) & 7) == 3 ? line ^ 1 : line) * 128;
      asm volatile("" : "+v"(go));
    }
    if constexpr (ISQ && S == SF1) {
      OPAQUE_BID;
      int tq2, tp2;
      tile_map<16, 16, 2, 16>(BID, tq2, tp2);
      fp = x_flag(ctl, lane < 32 ? tq : tq2, tp, lane < 32 ? lane : lane & 15);
      asm volatile("global_load_dwordx2 %0, %1, off sc1" : "=&v"(fx) : "v"(fp) : "memory");
    }
    if constexpr (gs(S) >= 0) {
      if (grp == gs(S)) {
        const char* b = (const char*)w1;
        asm volatile("s_mov_b32 m0, %5\n\ts_nop 0\n\tglobal_load_lds_dword %0, %1\n\tglobal_load_lds_dword %0, %2\n\tglobal_load_lds_dword %0, %3\n\tglobal_load_lds_dword %0, %4"
                     ::"v"(go), "s"(b), "s"(b + 28672 * 128), "s"(b + 2 * 28672 * 128), "s"(b + 3 * 28672 * 128), "i"(L_SCR) : "memory", "m0");
      }
    }
    if constexpr (ISQ && S == 1) {
      asm volatile("" : "+v"(fx));
      if (l0 && lw == 2) {
        const u64 tag = fx + 1;
        if (lane < NR_X) flag_set(ctl + C_XCNT + (tq * NR_X + lane) * RS1 + tp, tag);
        asm volatile("ds_write_b64 %0, %1\n\ts_waitcnt lgkmcnt(0)" ::"v"(vconst<L_TAG>()), "v"(tag) : "memory");
      }
    }
    if constexpr (S == SCO) copy_out();
    if constexpr (S == SCW) {
      if (duty >= 0) {
        const int q = (duty * 4 + lw) * 2, r8 = lane >> 3, c16 = (lane & 7) * 16;
        const auto r1 = __builtin_amdgcn_make_buffer_rsrc((void*)w1, 0, S_ * F_ * 2, 0x00020000);
        const auto r2 = __builtin_amdgcn_make_buffer_rsrc((void*)w2, 0, S_ * F_ * 2, 0x00020000);
        const int s1 = ((q & 255) * 8 * F_ + (((3 - (int)((size_t)w1 >> 7)) & 7) + 8 * (q >> 8)) * 64) * 2;
        const int s2 = ((q >> 2) * 8 * S_ + (((3 - (int)((size_t)w2 >> 7)) & 7) + 8 * (q & 3)) * 64) * 2;
        const unsigned v1 = r8 * (F_ * 2) + c16, v2 = r8 * (S_ * 2) + c16;
        LAS char* d = lds + L_C + lw * 4096;
#if defined(__HIP_DEVICE_COMPILE__)
        __builtin_amdgcn_raw_ptr_buffer_load_lds(r1, (LAS void*)d, 16, v1, s1, 0, AC);
        __builtin_amdgcn_raw_ptr_buffer_load_lds(r1, (LAS void*)(d + 1024), 16, v1, s1 + 8 * F_ * 2, 0, AC);
        __builtin_amdgcn_raw_ptr_buffer_load_lds(r2, (LAS void*)(d + 2048), 16, v2, s2, 0, AC);
        __builtin_amdgcn_raw_ptr_buffer_load_lds(r2, (LAS void*)(d + 3072), 16, v2, s2 + 1024, 0, AC);
#endif
      }
    }
    if constexpr (ISQ && S == SF1 + 2) {
      asm volatile("" : "+v"(fx));
      if (lw == 3 && !__all(fx == TAG)) wait_tags<1>(fp, TAG);
    }
    if constexpr (ng(S) == 4) {
      constexpr int i = (S - SG) * 4;
      asm volatile(
          "global_load_dwordx2 %0, %4, off offset:%5 sc1\n\tglobal_load_dwordx2 %1, %4, off offset:%5+256 sc1\n\t"
          "global_load_dwordx2 %2, %4, off offset:%5+512 sc1\n\tglobal_load_dwordx2 %3, %4, off offset:%5+768 sc1"
          : "=&v"(g[i]), "=&v"(g[i + 1]), "=&v"(g[i + 2]), "=&v"(g[i + 3]) : "v"(sp), "i"(i * 256) : "memory");
    }
    if constexpr (ng(S) == 1) asm volatile("global_load_dwordx2 %0, %1, off offset:%2 sc1" : "=&v"(g[S - SG]) : "v"(sp), "i"((S - SG) * 256) : "memory");
    if constexpr (ISQ) {
      if constexpr (S == SG + 3) { g[0] = sum4<0>(); asm volatile("" : "+v"(g[0])); }
      if constexpr (S == SG + 4) { g[0] = uadd(g[0], sum4<4>()); asm volatile("" : "+v"(g[0])); }
      if constexpr (S == SG + 5) { g[0] = uadd(g[0], uadd(sum4<8>(), sum4<12>())); asm volatile("" : "+v"(g[0])); }
    } else {
      if constexpr (S == SG + 5) { g[0] = sum4<0>(); float a = rows_sum(g[0][0]); asm volatile("" : "+v"(a)); g[0] = f32x2{a, rows_sum(g[0][1])}; asm volatile("" : "+v"(g[0])); }
    }
    if constexpr (S == SG + 6) {
      asm volatile("" : "+v"(g[0]));
      const f32x2 c = ln_coef(g[0][0], g[0][1]);
      asm volatile("ds_write_b64 %0, %1\n\tds_write2_b32 %2, %3, %4 offset1:144\n\ts_waitcnt lgkmcnt(0)" ::"v"(sa), "v"(c), "v"((sa >> 1) + (L_RS - K1_ST / 2)), "v"(c[0]), "v"(c[1]) : "memory");
    }
  }
  template <int T> __device__ __forceinline__ void next() {
    stepc<13 + T>();
    SCHED0;
    if constexpr (!ISQ && PF2) {
      constexpr int n0 = PF2 == 5 ? 0 : T == 1 ? 0 : 2, n1 = T == 3 ? 4 : T == 1 && PF2 != 5 ? 2 : 0;
#pragma unroll
      for (int i = n0; i < n1; ++i) so.template stage<0>(lds + L_A + 32768 + i * 8192 + lw * 1024);
      SCHED0;
    }
  }
  __device__ __forceinline__ void finish() {
    CFENCE;
    __builtin_amdgcn_s_waitcnt(0x0F70);
    CFENCE;
    if (SCO == 99) copy_out();
  }
  u16* cw;
  __device__ __forceinline__ void copy_out() {
    if (duty >= 0) {
      const int q = (OPQ(duty) * 4 + lw) * 2, r8 = lane >> 3, c16 = (lane & 7) * 16;
      const int k1 = (q & 7) * 8 + r8, k2 = ((q >> 3) & 7) * 8 + r8;
      const unsigned cd1 = (unsigned)((q >> 3) * CW_BLK + phys<64>(k1) * 128 + c16);
      const unsigned cd2 = (unsigned)((224 + (q & 3) * 56 + (q >> 6) * 2 + ((q >> 2) & 1)) * CW_BLK + phys<64>(k2) * 128 + c16);
      const unsigned d1 = pow2sel(r8 == 3, 7, 10);
      u32x4 cv[4];
      asm volatile("ds_read_b128 %0, %4\n\tds_read_b128 %1, %4 offset:1024\n\tds_read_b128 %2, %4 offset:2048\n\tds_read_b128 %3, %4 offset:3072\n\ts_waitcnt lgkmcnt(0)"
                   : "=&v"(cv[0]), "=&v"(cv[1]), "=&v"(cv[2]), "=&v"(cv[3]) : "v"(L_C + lw * 4096 + lane * 16) : "memory");
      asm volatile("s_nop 4\n\tglobal_store_dwordx4 %0, %4, %8 sc1\n\tglobal_store_dwordx4 %1, %5, %8 sc1\n\tglobal_store_dwordx4 %2, %6, %8 sc1\n\tglobal_store_dwordx4 %3, %7, %8 sc1\n\ts_nop 1"
                   ::"v"(cd1), "v"(cd1 + d1), "v"(cd2), "v"(cd2 + 56 * CW_BLK), "v"(cv[0]), "v"(cv[1]), "v"(cv[2]), "v"(cv[3]), "s"(cw) : "memory");
    }
  }
};
static_assert(SF1 + 3 + 5 == 13 && SCW != SF1 && SCO != SF1 && SCW != 11 && SCO != 11, "the fetches end two steps before the tail; statistics in LDS before the last barrier");

constexpr int K2_NSQ = 4, K2_NSP = 4;
__device__ __forceinline__ void k1_body(LAS char* lds, int wid, bool l0, const u16* __restrict__ w, const u16* x, u16* o, u32x4* xch, u64* ctl,
                                        u32x2* pstat, const u16* __restrict__ wo, const u16* __restrict__ w1,
                                        const u16* __restrict__ w2, u16* __restrict__ cw) {
  LAS char* pbuf = lds + L_A;
  LAS char* qbuf = lds + K1_QOFF;
  OPAQUE_BID;
  const int lane = lane_id();
  int tq, tp;
  tile_map<16, 16, 8, 4>(BID, tq, tp);
  const int s0 = tq * 128;
  if (wid >= 4) {
    auto fill = [&](auto& h) {
      h.l0 = l0; h.tq = tq; h.tp = tp; h.lw = wid - 4; h.lane = lane; h.lds = lds; h.pstat = pstat; h.ctl = ctl; h.xch = xch;
      h.wo = wo; h.w1 = w1; h.w2 = w2; h.wq = w; h.cw = cw;
    };
    if (wid >= 6) {
      if (lane < 8 * K1_NS) {
        const unsigned o1 = vconst<0x3F803F80u>();
        *(LAS u32x4*)(qbuf + (wid - 6) * 1024 + (lane >> 3) * K1_QSTG + (lane & 7) * 16) = u32x4{o1, o1, o1, o1};
      }
      WAITL0;
      K1Hook<true> hk;
      fill(hk);
      Operand Q{x, D_, s0 - 16, 64, S_ * D_ * 2, 0, false, nullptr, 0};
      g_loader<KC, 16, 144, K1_NS, 16, -1, true>(qbuf, Q, wid - 6, lane, hk);
      hk.finish();
    } else {
      Operand P{w, 3 * D_, tp * 64, D_, 3 * D_ * D_ * 2, 0, false, nullptr, 0};
      SlowColHook<K1Hook<false>, 3, 2 * D_, 4, 32, 16> hs;
      fill(hs);
      hs.setup(w, 3 * D_, (tp & ~3) * 128, 0, BID >> 3, lane, wid == 4, (unsigned)(size_t)lds + L_SCR);
      g_loader<KM, 16, 192, K1_NS, 16, 0, false>(pbuf, P, wid - 4, lane, hs, !l0 && ((((int)((size_t)w >> 7)) + tp) & 7) != 3);
      hs.template next<3>();
      hs.finish();
    }
  } else {
    Gate gate;
    {
      int kh, tp4, tq4;
      k4_map(BID, kh, tp4, tq4);
      gate.prep(l0 ? 2 : wid, ctl + C_XCNT + (tq4 * NR_X + lane) * RS1 + tp4 * 2 + kh, (1 << NR_X) - 1, x_flag(ctl, tq, tp, lane), 0xFFFFFFFFull, l0 ? 0ull : 5ull);
      gate.m1 = 0x00030003ull;
      gate.seam();
    }
    k1_consumer(lds, lane, wid, l0, tq, (const char*)o + (size_t)(tq * 16 + tp) * TB + (wid >> 1) * (TB / 2), gate, [&] {
      if (l0) {
        int ln = lane;
        asm volatile("" : "+v"(ln));
        const int fr = ln & 15, fq = (ln & 63) >> 4;
        if (wid == 3) {
          f32x4 ta, tb;
          const unsigned ninf = vconst<0xFF61B1E6u>();
          static_for<4>([&](auto rc) { float a, b; win_bias<decltype(rc)::value>(ninf, a, b); ta[decltype(rc)::value] = a; tb[decltype(rc)::value] = b; });
          asm volatile("ds_write_b128 %2, %0\n\tds_write_b128 %2, %1 offset:1024" ::"v"(ta), "v"(tb), "v"(ln * 16 + L_MSK) : "memory");
        }
        const unsigned o1 = vconst<0x3F803F80u>();
        const bf16x8 ones = __builtin_bit_cast(bf16x8, u32x4{o1, o1, o1, o1});
        if (wid & 1) WAITV(2); else WAITV(1);
        bf16x8 f[2][2];
#pragma unroll
        for (int k = 0; k < 4; ++k) f[k >> 1][k & 1] = *(const LAS bf16x8*)(lds + L_C + wid * 4096 + k * 1024 + ln * 16);
        const f32x4 z4 = {0.f, 0.f, 0.f, 0.f};
        u64 ps[2];
#pragma unroll
        for (int j = 0; j < 2; ++j) {
          const f32x4 gm = __builtin_amdgcn_mfma_f32_16x16x32_bf16(f[j][1], f[j][1], __builtin_amdgcn_mfma_f32_16x16x32_bf16(f[j][0], f[j][0], z4, 0, 0, 0), 0, 0, 0);
          const f32x4 sm = __builtin_amdgcn_mfma_f32_16x16x32_bf16(ones, f[j][1], __builtin_amdgcn_mfma_f32_16x16x32_bf16(ones, f[j][0], z4, 0, 0, 0), 0, 0, 0);
          const float da = (fr & 1) ? gm[1] : gm[0], db = (fr & 1) ? gm[3] : gm[2], dg = (fr & 2) ? db : da;
          ps[j] = ((u64)__float_as_uint(dg) << 32) | __float_as_uint(sm[0]);
        }
        if (fq == (fr >> 2)) {
#pragma unroll
          for (int j = 0; j < 2; ++j)
            __hip_atomic_store((u64*)pstat + (size_t)(tq * 4 + wid) * 512 + tp * 32 + j * 16 + fr, ps[j], __ATOMIC_RELAXED, __HIP_MEMORY_SCOPE_AGENT);
        }
        SCHED0;
      }
    });
  }
}

struct ResTile {
  LAS char* a;
  int sw;
  LAS char* b;
  int c8, r8;
  __device__ __forceinline__ void init(LAS char* rt, int lane) {
    a = rt + (lane & 15) * 128; sw = (lane & 15) >> 1;
    r8 = lane >> 3; c8 = lane & 7; b = rt + r8 * 128;
  }
  __device__ __forceinline__ void load(u32x2 (&v)[8]) const {
#pragma unroll
    for (int t = 0; t < 8; ++t) v[t] = *(const LAS u32x2*)((LAS char*)(size_t)(wa[t & 3] - (L_C - L_A)) + (t >> 2) * 2048);
  }
  unsigned rb[2];
  __device__ __forceinline__ void stage_rows(const u32x2 (&v)[8], u32x4 (&y)[4]) const {
#pragma unroll
    for (int t = 0; t < 8; ++t) *(LAS u32x2*)((LAS char*)(size_t)wa[t & 3] + (t >> 2) * 2048) = v[t];
    WAITL0;
#pragma unroll
    for (int k = 0; k < 4; ++k) y[k] = *(const LAS u32x4*)((LAS char*)(size_t)rb[k & 1] + k * 1024);
  }
  unsigned wa[4], ra[8], go[4];
  __device__ __forceinline__ void prep_t(LAS char* rt, int d0, int s0, int lane) {
    const int g = lane >> 4, q4 = (lane & 15) >> 2, p4 = lane & 3, fr = lane & 15;
    const int rl = 8 * g + q4, rh = rl + 4;
    const unsigned al = (unsigned)(size_t)rt + rl * 128 + (p4 & 1) * 8;
#pragma unroll
    for (int t = 0; t < 4; ++t) wa[t] = (unsigned)(size_t)a + ((((t >> 1) * 4 + g) ^ sw) * 16) + ((t ^ (g >> 1)) & 1) * 8;
#pragma unroll
    for (int k = 0; k < 4; ++k) {
      const int ch = 2 * k + (p4 >> 1);
      ra[2 * k] = al + ((ch ^ ((rl >> 1) & 7)) * 16);
      ra[2 * k + 1] = al + 512 + ((ch ^ ((rh >> 1) & 7)) * 16);
    }
    {
      const unsigned s1 = pow2sel((fr >> 1) == 3, 7, 10);
      go[0] = phys<64>((d0 + fr) >> 1) * 128 + (fr & 1) * 64 + (s0 >> 5) * (TB / 2) + g * 16;
      go[1] = go[0] + s1; go[2] = go[1] + s1; go[3] = go[2] + s1;
    }
    rb[0] = (unsigned)(size_t)b + ((c8 ^ (r8 >> 1)) * 16); rb[1] = (unsigned)(size_t)b + ((c8 ^ (4 + (r8 >> 1))) * 16);
#pragma unroll
    for (int t = 0; t < 8; ++t) asm volatile("" : "+v"(wa[t & 3]), "+v"(ra[t]), "+v"(go[t & 3]), "+v"(rb[t & 1]));
  }
  __device__ __forceinline__ void load_t(f32x2 (&z)[16]) const {
#pragma unroll
    for (int k = 0; k < 8; ++k) {
      const u32x2 v = __builtin_bit_cast(u32x2, __builtin_amdgcn_ds_read_tr16_b64_v4i16((LAS s16x4*)(size_t)ra[k]));
      z[2 * k] = f32x2{lo2f(v[0]), hi2f(v[0])}; z[2 * k + 1] = f32x2{lo2f(v[1]), hi2f(v[1])};
    }
#pragma unroll
    for (int e = 0; e < 16; ++e) asm volatile("" : "+v"(z[e]));
  }
};

__device__ __forceinline__ u32x2 row_stats(const f32x2 (&z)[16]) {
  unsigned s1[2], q1[2];
#pragma unroll
  for (int j = 0; j < 2; ++j) {
    f32x2 sm[2], sq[2];
#pragma unroll
    for (int e = 0; e < 8; ++e) {
      const f32x2 v = z[8 * j + e];
      if (e < 2) { sm[e] = v; sq[e] = v * v; }
      else { sm[e & 1] += v; sq[e & 1] = __builtin_elementwise_fma(v, v, sq[e & 1]); }
    }
    const f32x2 s2 = sm[0] + sm[1], q2 = sq[0] + sq[1];
    s1[j] = __float_as_uint(s2[0] + s2[1]); q1[j] = __float_as_uint(q2[0] + q2[1]);
  }
  auto add2 = [](auto r) { return __float_as_uint(__uint_as_float(r[0]) + __uint_as_float(r[1])); };
  const unsigned sa = add2(__builtin_amdgcn_permlane16_swap(s1[0], s1[1], false, false)), qa = add2(__builtin_amdgcn_permlane16_swap(q1[0], q1[1], false, false));
  const unsigned t = add2(__builtin_amdgcn_permlane32_swap(sa, qa, false, false));
  const auto c = __builtin_amdgcn_permlane32_swap(t, t, false, false);
  return u32x2{c[0], c[1]};
}
#define XLD \
  "global_load_dwordx4 v[192:195], %[gx], off sc1\n\tglobal_load_dwordx4 v[196:199], %[gx], off offset:256 sc1\n\t" \
  "global_load_dwordx4 v[200:203], %[gx], off offset:512 sc1\n\tglobal_load_dwordx4 v[204:207], %[gx], off offset:768 sc1\n\t"
using u32x16 = __attribute__((ext_vector_type(16))) unsigned;
__device__ __forceinline__ void x_issue(u32x16& g, const char* gx, const u32x4 (&y)[4], unsigned vo, const u16* ob) {
  asm volatile(XLD
               "global_store_dwordx4 %[vo], %[y0], %[ob] sc1\n\tv_add_u32 %[vo], 0x4000, %[vo]\n\t"
               "global_store_dwordx4 %[vo], %[y1], %[ob] sc1\n\tv_add_u32 %[vo], 0x4000, %[vo]\n\t"
               "global_store_dwordx4 %[vo], %[y2], %[ob] sc1\n\tv_add_u32 %[vo], 0x4000, %[vo]\n\t"
               "global_store_dwordx4 %[vo], %[y3], %[ob] sc1\n\ts_nop 1"
               : "=&{v[192:207]}"(g), [vo] "+v"(vo)
               : [gx] "v"(gx), [y0] "v"(y[0]), [y1] "v"(y[1]), [y2] "v"(y[2]), [y3] "v"(y[3]), [ob] "s"(ob) : "memory");
}
template <int SL>
__device__ __forceinline__ void x_gather(u32x16& g, const char* gx, f32x2& s0, f32x2& s1) {
  unsigned n;
  asm volatile("s_mov_b32 %[n], 0x8000\n\ts_waitcnt vmcnt(4)\n"
               "1:\n\tv_max3_u32 v208, v193, v195, v197\n\tv_max3_u32 v209, v199, v201, v203\n\tv_max3_u32 v208, v208, v205, v207\n\t"
               "v_pk_add_f32 v[192:193], v[192:193], v[196:197]\n\tv_max_u32 v208, v208, v209\n\tv_cmp_eq_u32 vcc, -1, v208\n\t"
               "v_pk_add_f32 v[194:195], v[194:195], v[198:199]\n\tv_pk_add_f32 v[200:201], v[200:201], v[204:205]\n\t"
               "v_pk_add_f32 v[202:203], v[202:203], v[206:207]\n\ts_cbranch_vccz 3f\n\t"
               "s_sleep %[sl]\n\t" XLD "s_waitcnt vmcnt(0)\n\t"
               "s_sub_u32 %[n], %[n], 1\n\ts_cbranch_scc0 1b\n"
               "3:\n\tv_pk_add_f32 %[s0], v[192:193], v[200:201]\n\tv_pk_add_f32 %[s1], v[194:195], v[202:203]\n\ts_nop 1"
               : "+{v[192:207]}"(g), [s0] "=&v"(s0), [s1] "=&v"(s1), [n] "=&s"(n)
               : [gx] "v"(gx), [sl] "i"(SL) : "memory", "vcc", "scc", "v208", "v209");
}

#define XSLEEP0 2
#define XSLEEP1 1
constexpr int K2_RES = L_C;
template <class Base>
struct K3NextHook : Base {
  static constexpr int NEXT = 8;
  Stream<KM, 16, 128> sn;
  bool on; int nev; const u16* w1; const u16* cw1; const u64* ctl; LAS char* lds; int gwn, lanen;
  __device__ __forceinline__ void afterr() {
    OPAQUE_BID;
    int tq, tp;
    tile_map<8, 28, 4, 7>(BID, tq, tp);
    sn.init(Operand{w1, F_, min(tp, 27) * 128, 64, S_ * F_ * 2, 0, false, cw1, 32}, gwn, lanen);
    sn.pin();
  }
  __device__ __forceinline__ void step(int s) {
    if (__builtin_expect(s == nev, 0)) {
      if (s == 6) { if (gwn == 0) fetch_flags<0>(first_flags(ctl, lanen)); else fetch_flags<1024>(first_flags(ctl, lanen) + 2); }
      else if (!flags_set(L_FLG + gwn * 1024 + lanen * 16, TAG)) wait_first_phase(lds, ctl, lanen);
      nev += 4;
      asm volatile("" : "+s"(nev));
    }
    Base::step(s);
  }
  template <int S, int N> __device__ __forceinline__ void wait() {
    if (on) waitv<N>(); else waitv<N - (S - 12) * NEXT>();
  }
  template <int T> __device__ __forceinline__ void next() { if (on) sn.template stage<A1>(lds + L_A + gwn * 1024 + T * 16384); }
};
__device__ __forceinline__ void k2_body(LAS char* lds, int wid, const u16* __restrict__ wo, const u16* o, const u16* res, u16* out, u16* hout,
                                        unsigned long long* xch, u64* ctl, bool l0, const u16* w1, const u16* cw1) {
  LAS char* qbuf = lds + L_B;
  LAS char* pbuf = lds + L_A + 32768;
  OPAQUE_BID;
  const int lane = lane_id();
  int tq, tp;
  tile_map<16, 16, 2, 16>(BID, tq, tp);
  const int p0 = tp * 64, q0 = tq * 128;
  if (wid >= 6) {
    Operand P{wo, D_, p0, 64, D_ * D_ * 2, 0, false, nullptr, 0};
    SyncHook<K3NextHook<ResHook<4, 32>>, 1> hk;
    hk.on = BID < 224; hk.nev = l0 ? 6 : 99; hk.w1 = w1; hk.cw1 = cw1; hk.ctl = ctl; hk.lds = lds; hk.gwn = wid - 6; hk.lanen = lane;
    static_cast<ResHook<4, 32>&>(hk) = ResHook<4, 32>{{}, res, q0, p0, lds + L_A, wid - 6, lane};
    g_loader<KM, 16, 64, K2_NSP, 16, -1, false>(pbuf, P, wid - 6, lane, hk, PF2);
    __builtin_amdgcn_s_waitcnt(0x0F70);
    return;
  }
  if (wid >= 4) {
    Operand Q{o, 16, q0, 64, 16 * 16 * TB, 0, true, nullptr, 0};
    WaitHookX hk;
    g_loader<KC, 16, 128, K2_NSQ, 16, 1, false>(qbuf, Q, wid - 4, lane, hk);
    __builtin_amdgcn_s_waitcnt(0x0F70);
    return;
  }
  Gate gate;
  {
    int tq1, tp1;
    tile_map<16, 16, 8, 4>(BID, tq1, tp1);
    gate.prep(wid, ctl + C_OCNT + (tq1 * NR_O + lane) * RS1 + tp1, (1 << NR_O) - 1, ctl + C_OCNT + (tq * NR_O + tp % NR_O) * RS1 + (lane & 15), 0xFFFFull, 1);
    gate.m1 = (1 << NP2) - 1;
    gate.seam();
  }
  f32x4 acc[4][2];
  const int row0 = q0 + wid * 32;
  const size_t obase = (size_t)row0 * D_ + p0;
  ResTile rt;
  rt.init(lds + K2_RES + wid * 4096, lane);
  u32x2 x[8];
  unsigned long long* xw = xch + (size_t)(row0 >> 5) * 512 + tp * 32 + (lane & 15) * 2 + ((lane >> 4) & 1);
  const char* gx = (const char*)(xch + (size_t)(row0 >> 5) * 512) + (lane >> 4) * 1024 + (lane & 15) * 16;
  asm volatile("" : "+v"(xw), "+v"(gx));
  unsigned xo = (unsigned)((lane >> 3) * D_ * 2 + (lane & 7) * 16);
  const u16* xb = out + obase;
  asm volatile("" : "+v"(xo), "+s"(xb));
  rt.prep_t(lds + K2_RES + wid * 4096, p0 & 127, row0 & 63, lane);
  const char* hb = (const char*)hout + (size_t)((p0 >> 7) * 32 + (row0 >> 6)) * TB;
  asm volatile("" : "+s"(hb));
  unsigned cwa = K2_RES + wid * 4096 + lane * 4, cra = K2_RES + wid * 4096 + (lane >> 4) * 32;
  asm volatile("" : "+v"(cwa), "+v"(cra));
  const bool hi = lane >= 32;
  consumer16w<KM, KC, 4, 2, K2_NSP, 64 * 128, 128 * 128, 16, false>(pbuf, qbuf, acc, 0, wid * 2, lane, gate, [&] {
    rt.load(x);
#pragma unroll
    for (int t = 0; t < 8; ++t) acc[t & 3][t >> 2] = f32x4{lo2f(x[t][0]), hi2f(x[t][0]), lo2f(x[t][1]), hi2f(x[t][1])};
  }, [](auto&&) {}, [](int) {});
  f32x2 z[16];
#pragma unroll
  for (int e = 0; e < 16; ++e) z[e] = f32x2{acc[(e >> 1) & 3][e >> 3][2 * (e & 1)], acc[(e >> 1) & 3][e >> 3][2 * (e & 1) + 1]};
  ALIGN4;
  u32x2 ps = row_stats(z);
  ps[1] = min(ps[1], 0xFFFFFFFEu);
  asm volatile("s_mov_b32 exec_hi, 0\n\tglobal_store_dwordx2 %0, %1, off sc1\n\ts_mov_b32 exec_hi, -1" ::"v"(xw), "v"(ps) : "memory");
  SCHED0;
#pragma unroll
  for (int e = 0; e < 16; ++e) x[e >> 1][e & 1] = pk2(z[e][0], z[e][1]);
  u32x4 yr[4];
  rt.stage_rows(x, yr);
  WAITL0;
  if (XSLEEP0) __builtin_amdgcn_s_sleep(XSLEEP0);
  f32x2 s0, s1;
  u32x16 xg;
  x_issue(xg, gx, yr, xo, xb);
  rt.load_t(z);
  x_gather<XSLEEP1>(xg, gx, s0, s1);
  auto add2 = [](auto r) { return __uint_as_float(r[0]) + __uint_as_float(r[1]); };
  const float sm = half_sum(add2(__builtin_amdgcn_permlane16_swap(__float_as_uint(s0[0]), __float_as_uint(s1[0]), false, false)));
  const float sq = half_sum(add2(__builtin_amdgcn_permlane16_swap(__float_as_uint(s0[1]), __float_as_uint(s1[1]), false, false)));
  const float mean = sm * (1.0f / D_);
  const float var = fmaxf(__builtin_fmaf(-mean, mean, sq * (1.0f / D_)), 0.f);
  const float m1 = rbf(mean), i1 = __builtin_amdgcn_rcpf(rbf(__builtin_amdgcn_sqrtf(rbf(rbf(var) + 1e-5f))));
  const float m2 = rbf((mean - m1) * i1), i2 = __builtin_amdgcn_rcpf(rbf(__builtin_amdgcn_sqrtf(rbf(rbf(var * i1 * i1) + 1e-5f))));
  *(LAS float*)(size_t)cwa = (hi ? -(m1 * i1 + m2) : i1) * i2;
  CFENCE;
  f32x4 cf[4];
#pragma unroll
  for (int k = 0; k < 4; ++k) cf[k] = *(const LAS f32x4*)((LAS char*)(size_t)cra + (k & 1) * 16 + (k >> 1) * 128);
#pragma unroll
  for (int k = 0; k < 4; ++k) {
    u32x4 y;
#pragma unroll
    for (int e = 0; e < 4; ++e) {
      const f32x2 v = z[4 * k + e] * f32x2{cf[e >> 1][2 * (e & 1)], cf[e >> 1][2 * (e & 1) + 1]} + f32x2{cf[2 + (e >> 1)][2 * (e & 1)], cf[2 + (e >> 1)][2 * (e & 1) + 1]};
      y[e] = pk2(v[0], v[1]);
    }
    asm volatile("global_store_dwordx4 %0, %1, %2 sc1\n\ts_nop 1" ::"v"(rt.go[k]), "v"(y), "s"(hb) : "memory");
    SCHED0;
  }
}

constexpr int K4_NS = 4;
constexpr int K4_RES = L_C;
__device__ __forceinline__ void w2_init(Stream<KM, 16, 128>& st, const u16* w2, const u16* cw2, int gw, int lane) {
  OPAQUE_BID;
  int kh, tp, tq;
  k4_map(BID, kh, tp, tq);
  st.init(Operand{w2, S_, tq * 128 + kh * 64, 64, F_ * S_ * 2, kh * 28, false, cw2, 56}, gw, lane);
  st.twice();
  st.pin();
}
__device__ __forceinline__ void u_init(Stream<KC, 16, 128, true>& st, const u16* u, int gw, int lane) {
  OPAQUE_BID;
  int kh, tp, tq;
  k4_map(BID, kh, tp, tq);
  st.init(Operand{u, 56, tp * 128, 64, 8 * 56 * TB, kh * 28, true, nullptr, 0}, gw, lane);
  st.twice();
  st.pin();
}
template <class T> __device__ __forceinline__ T* OPV(T* p) { return (T*)(__attribute__((address_space(1))) T*)(size_t)p; }
struct UFlags {
  u64* set; const u64* get; u64 tag;
  __device__ __forceinline__ void init(LAS char* lds, u64* flags, int gw, int lane) {
    OPAQUE_BID;
    int kh, tp, tq, tq3, tp3;
    k4_map(BID, kh, tp, tq);
    tile_map<8, 28, 4, 7>(BID, tq3, tp3);
    u64* b = flags + (gw ? C_UCN2 : C_UCNT);
    set = b + ((tq3 * 2 + tp3 / 14) * NR_U + lane) * RS1 + tp3 % 14;
    get = b + ((tp * 2 + (gw ? (lane >> 3) & 1 : kh)) * NR_U + tq % NR_U) * RS1 + (gw ? min((lane & 7) * 2, 12) : min(lane, 13));
    tag = TAG;
    asm volatile("" : "+v"(set), "+v"(get), "+v"(tag));
  }
  __device__ __forceinline__ void publish() { asm volatile("s_mov_b64 exec, %2\n\tglobal_atomic_swap_x2 %0, %1, off sc1\n\ts_mov_b64 exec, -1" ::"v"(set), "v"(tag), "i"((1 << NR_U) - 1) : "memory"); }
};
struct K4NextHook : NoHook {
  static constexpr int AUX = A1;
  static constexpr int NEXT = 8;
  Stream<KM, 16, 128>* st; LAS char* lds; int gw;
  template <int T> __device__ __forceinline__ void next() { st->stage<A2>(lds + L_A + gw * 1024 + T * 16384); }
};
struct UHook : WaitHookP { static constexpr bool EVOD = true; int kh; };
template <class Base>
struct W2Hook : Base {
  static constexpr bool PREALL = true, EVOD = true, OWNP = true;
  int kh;
  UFlags* uf; bool own; int gww;
  __device__ __forceinline__ void setup() { Base::first(); Base::afterr(); }
  __device__ __forceinline__ void first() {
    if (gww == 0) {
      u64 a, b, c, d, t, z = vz(), m1 = (1 << NP4) - 1;
      unsigned n;
      asm volatile("s_mov_b64 exec, 0x3FFF\n\ts_mov_b32 %[n], 0\n\t" POLL1(a) POLL1(b) POLL1(c) POLL0(d)
                   "1:\n\t" POLL3(a, 4) POLL3(b, 5) POLL3(c, 6) POLL3(d, 7) PEND
                   : [a] "=&v"(a), [b] "=&v"(b), [c] "=&v"(c), [d] "=&v"(d), [n] "=&s"(n), [t] "=&s"(t), [m1] "+s"(m1)
                   : [q] "v"(uf->get), [z] "v"(z), [tag] "v"(uf->tag), [sl] "i"(PSLP) : "memory", "vcc", "scc");
      asm volatile("s_barrier\n\ts_waitcnt vmcnt(0)" ::"v"(a), "v"(b), "v"(c), "v"(d) : "memory");
    } else { BARRIER; WAITV(0); BARRIER; }
  }
  int nev;
  __device__ __forceinline__ void step(int s) {
    if (__builtin_expect(s == nev, 0)) {
      if (s == 0) { if (own) uf->publish(); }
      else if (s == 4) fetch_flags<0>(uf->get);
      else if (!flags_set(L_FLG + Base::lanen * 16, uf->tag)) wait_tags<2>(OPV(uf->get), uf->tag);
      nev = s >= 8 ? 99 : s + 4;
      asm volatile("" : "+s"(nev));
    }
  }
};
template <class Base>
struct K1NextHook : Base {
  static constexpr int NEXT = 8;
  Stream<KM, 16, 192> sn;
  bool on; const u16* wq; LAS char* dstn; int gwn, lanen;
  __device__ __forceinline__ void afterr() {
    OPAQUE_BID;
    int tq, tp;
    tile_map<16, 16, 8, 4>(BID, tq, tp);
    sn.init(Operand{wq, 3 * D_, tp * 64, D_, 3 * D_ * D_ * 2, 0, false, nullptr, 0}, gwn, lanen);
#pragma unroll
    for (int i = 0; i < 4; ++i) asm volatile("" : "+v"(sn.voff[i]));
  }
  template <int U> __device__ __forceinline__ void unit() {
    const auto rsrc = __builtin_amdgcn_make_buffer_rsrc((void*)sn.gbase[0], 0, sn.gbytes[0], 0x00020000);
#pragma unroll
    for (int i = 0; i < 4; ++i) {
#if defined(__HIP_DEVICE_COMPILE__)
      __builtin_amdgcn_raw_ptr_buffer_load_lds(rsrc, (LAS void*)(dstn + U * 8192 + i * 2048), 16, sn.voff[i], sn.so[0] + (U / 3) * sn.inc[0] + (U % 3) * D_ * 2, 0, 0);
#endif
    }
  }
  __device__ __forceinline__ void last() { Base::last(); if (on) unit<8>(); }
  template <int S, int N> __device__ __forceinline__ void wait() {
    if (on) waitv<N + (S < 26 + KD ? 4 : 0)>(); else waitv<N - (S - 24 - KD) * NEXT>();
  }
  template <int T> __device__ __forceinline__ void next() { if (on) { unit<2 * ((KD + T) % 4)>(); unit<2 * ((KD + T) % 4) + 1>(); } }
};
__device__ __forceinline__ void k4_consumer(LAS char* lds, u16* out, u32x2* pstat, float* part, u64* flags, int wid) {
  LAS char* pbuf = lds + L_B;
  LAS char* qbuf = lds + L_A;
  OPAQUE_BID;
  const int lane = lane_id();
  int kh, tp, tq;
  k4_map(BID, kh, tp, tq);
  const int p0 = tp * 128, q0 = tq * 128;
  DrainGate gate;
  const int wp = wid >> 1, wq = wid & 1;
  f32x4 acc[4][4];
  const int row0 = q0 + kh * 64 + wq * 32;
  const size_t obase = (size_t)row0 * D_ + p0 + wp * 64;
#pragma unroll
  for (int j = 0; j < 4; ++j) {
    const float bq = j < 2 ? bf2f(*(const LAS u16*)(lds + L_BIA + 256 + (kh * 64 + wq * 32 + j * 16 + (lane & 15)) * 2)) : 0.f;
#pragma unroll
    for (int i = 0; i < 4; ++i) acc[i][j] = f32x4{bq, bq, bq, bq};
  }
  u32x4 x[2][2];
#define GLD "global_load_dwordx4 %[g0], %[go], %[gp] sc1\n\tglobal_load_dwordx4 %[g1], %[go], %[gp] offset:1024 sc1\n\t" \
            "global_load_dwordx4 %[g2], %[go], %[gp] offset:2048 sc1\n\tglobal_load_dwordx4 %[g3], %[go], %[gp] offset:3072 sc1"
  u64 ptag, a, a2, miss;
  u32x4 g[4];
  const char* gp = (const char*)part + (size_t)((BID ^ 1) * 4 + wid) * 4096;
  unsigned v0;
  const u64* myf = flags + (BID * 4 + wid) * RSP;
  const u64* paf = flags + ((BID ^ 1) * 4 + wid) * RSP;
  u64 zz;
  auto poll1 = [&](u64& r) {
    asm volatile("s_mov_b64 exec, 1\n\tglobal_atomic_or_x2 %0, %1, %2, %3 sc0 sc1\n\ts_mov_b64 exec, -1" : "=&v"(r) : "v"(v0), "v"(zz), "s"(paf) : "memory");
  };
  consumer16w<KC, KM, 4, 4, K4_NS, 128 * 128, 128 * 128, 28 - KD, true>(pbuf, qbuf, acc, wp * 4, wq * 2, lane, gate, [&] {
    const int fr = lane & 15, fq = lane >> 4;
#pragma unroll
    for (int t = 0; t < 4; ++t) x[t >> 1][t & 1] = *(const LAS u32x4*)(lds + K4_RES + wid * 4096 + (t >> 1) * 2048 + fr * 128 + ((((t & 1) * 4 + fq) ^ (fr >> 1)) * 16));
  }, [&](int t) {
    const auto rs = __builtin_amdgcn_make_buffer_rsrc((void*)part, 0, 256 * 16384, 0x00020000);
    const f32x4 a = acc[t][2], b = acc[t][3];
    __builtin_amdgcn_raw_buffer_store_b128(u32x4{pk2(a[0], a[1]), pk2(a[2], a[3]), pk2(b[0], b[1]), pk2(b[2], b[3])}, rs, lane * 16 + t * 1024, (BID * 4 + wid) * 4096, 16);
  }, [&](int k) {
    if (k == 0) asm volatile("s_nop 3\n\tv_mov_b32 %0, %3\n\tv_mov_b64 %2, 0\n\tds_read_b64 %1, %0" : "=&v"(v0), "=&v"(ptag), "=&v"(zz) : "i"(L_TAG) : "memory");
    if (k == FBK) {
      asm volatile("v_mov_b32 %0, 0\n\ts_waitcnt vmcnt(0)\n\ts_mov_b64 exec, 1\n\tglobal_atomic_swap_x2 %0, %1, %2 sc1\n\ts_mov_b64 exec, -1" : "+v"(v0), "+v"(ptag) : "s"(myf) : "memory");
    }
    if (k == CKK - 2) poll1(a);
    if (k == CKK - 1) poll1(a2);
    if (k == CKK)
      asm volatile("s_mov_b64 exec, 1\n\ts_waitcnt vmcnt(1)\n\tv_cmp_ne_u64 vcc, %[tag], %[a]\n\ts_mov_b64 exec, -1\n\ts_mov_b64 %[m], vcc\n\ts_cbranch_vccnz 1f\n\t" GLD "\n1:"
                   : [g0] "=&v"(g[0]), [g1] "=&v"(g[1]), [g2] "=&v"(g[2]), [g3] "=&v"(g[3]), [m] "=&s"(miss) : [tag] "v"(ptag), [a] "v"(a), [go] "v"(lane * 16), [gp] "s"(gp) : "memory", "vcc");
    if (k == CKK + 1)
      asm volatile("s_cmp_eq_u64 %[m], 0\n\ts_cbranch_scc1 1f\n\ts_mov_b64 exec, 1\n\ts_waitcnt vmcnt(0)\n\tv_cmp_ne_u64 vcc, %[tag], %[a]\n\ts_mov_b64 exec, -1\n\ts_mov_b64 %[m], vcc\n\ts_cbranch_vccnz 1f\n\t" GLD "\n1:"
                   : [g0] "+v"(g[0]), [g1] "+v"(g[1]), [g2] "+v"(g[2]), [g3] "+v"(g[3]), [m] "+s"(miss), [a] "+v"(a2) : [tag] "v"(ptag), [go] "v"(lane * 16), [gp] "s"(gp) : "memory", "vcc", "scc");
  });
  SCHED0;
  f32x2 z[16];
#pragma unroll
  for (int e = 0; e < 16; ++e) {
    const unsigned r = x[e >> 3][(e >> 2) & 1][e & 3];
    const f32x4 v = acc[(e >> 1) & 3][e >> 3];
    z[e] = f32x2{lo2f(r), hi2f(r)} + f32x2{v[2 * (e & 1)], v[2 * (e & 1) + 1]};
    asm volatile("" : "+v"(z[e]));
  }
  unsigned vo[4];
#pragma unroll
  for (int k = 0; k < 4; ++k) { vo[k] = (unsigned)(((lane & 7) + 8 * k) * D_ * 2 + (lane & 8) * 8 + (lane >> 4) * 16); asm volatile("" : "+v"(vo[k])); }
  const u16* ob = out + obase;
  asm volatile("" : "+s"(ob));
  {
    u64 c, d, sv;
    unsigned n;
    asm volatile("s_cmp_eq_u64 %[m], 0\n\ts_cbranch_scc1 9f\n\t" POLL_LOOP GLD "\n9:\n\ts_waitcnt vmcnt(0)"
        : [a] "=&v"(a), [b] "+v"(a2), [c] "=&v"(c), [d] "=&v"(d), [sv] "=&s"(sv), [n] "=&s"(n), [g0] "+v"(g[0]), [g1] "+v"(g[1]), [g2] "+v"(g[2]), [g3] "+v"(g[3])
        : [q] "v"(paf), [z] "v"(zz), [tag] "v"(ptag), [em] "s"(1ull), [sl] "i"(2), [m] "s"(miss), [go] "v"(lane * 16), [gp] "s"(gp) : "memory", "vcc", "scc");
  }
  {
#pragma unroll
    for (int e = 0; e < 16; ++e) {
      const unsigned r = g[(e >> 1) & 3][2 * (e >> 3) + (e & 1)];
      z[e] += f32x2{lo2f(r), hi2f(r)};
      x[e >> 3][(e >> 2) & 1][e & 3] = pk2(z[e][0], z[e][1]);
      if ((e & 7) == 7) {
        u32x4 n0, n1;
#pragma unroll
        for (int k = 0; k < 4; ++k) {
          n0[k] = __builtin_amdgcn_update_dpp(x[e >> 3][0][k], x[e >> 3][1][k], 0x128, 0xF, 0xC, false);
          n1[k] = __builtin_amdgcn_update_dpp(x[e >> 3][1][k], x[e >> 3][0][k], 0x128, 0xF, 0x3, false);
        }
        asm volatile("global_store_dwordx4 %0, %2, %4 sc1\n\tglobal_store_dwordx4 %1, %3, %4 sc1\n\ts_nop 1" ::"v"(vo[(e >> 3) * 2]), "v"(vo[(e >> 3) * 2 + 1]), "v"(n0), "v"(n1), "s"(ob) : "memory");
      }
    }
    {
      if (pstat != nullptr) {
        const u32x2 c = row_stats(z);
        if (lane < 32)
          __hip_atomic_store((unsigned long long*)pstat + (size_t)(row0 >> 5) * 512 + (tp * 2 + wp) * 32 + lane, ((unsigned long long)c[1] << 32) | c[0],
                             __ATOMIC_RELAXED, __HIP_MEMORY_SCOPE_AGENT);
      }
    }
  }
}

constexpr int K3_NSP = 4, K3_NSQ = 4;
__device__ __forceinline__ u64* h_flag(u64* ctl, unsigned BID, int lane) {
  int tq, tp;
  tile_map<16, 16, 2, 16>(BID, tq, tp);
  return ctl + C_HCNT + ((tp >> 1) * NR_H + lane) * RS2 + (tp & 1) * 16 + tq;
}
template <class T> __device__ __forceinline__ T* OPQ(T* p) {
  auto q = (__attribute__((address_space(1))) T*)p;
  asm volatile("" : "+s"(q));
  return (T*)q;
}
#define APOS 2
__device__ __forceinline__ void k34_body(LAS char* lds, int wid, bool l0, const u16* __restrict__ w1, const u16* h, u16* u, const u16* __restrict__ cw1, u64* flags,
                                         const u16* __restrict__ w2, const u16* res, u16* out, u32x2* pstat, float* part, const u16* __restrict__ wq_) {
  LAS char* pbuf = lds + L_A;
  LAS char* qbuf = lds + L_B;
  OPAQUE_BID;
  const int lane = lane_id();
  const bool own = BID < 224;
  if (!own) {
    WAITV(0);
    BARRIER;
    if (wid == 0 && lane < NR_H) flag_set(h_flag(flags, BID, lane), TAG);
    if (l0) {
      if (wid == 1) {
        u64 a, b, c, d, sv, z = vz(), tag = TAG;
        unsigned n;
        const u64* q = flags + C_HCNT + ((BID & 7) * NR_H + ((BID >> 3) & 3) * 2) * RS2 + (lane & 31);
        asm volatile(POLL_LOOP "s_waitcnt vmcnt(0)" : [a] "=&v"(a), [b] "=&v"(b), [c] "=&v"(c), [d] "=&v"(d), [n] "=&s"(n), [sv] "=&s"(sv)
                     : [q] "v"(q), [z] "v"(z), [tag] "v"(tag), [em] "s"(0xFFFFFFFFull), [sl] "i"(8) : "memory", "vcc", "scc");
      }
      BARRIER;
      for (int k = 0; k < WT0; ++k) __builtin_amdgcn_s_sleep(8);
      for (int k = 0; k < wid; ++k) __builtin_amdgcn_s_sleep(WSTG);
#pragma unroll 1
      for (int i = 0; i < 7; ++i) {
        const int g = 2 * i + (lane >> 5), par = g >= 7;
        touch_line(noslow(w2, (((BID >> 4) & 1) * 1792 + (g - 7 * par) * 256 + (BID & 15) * 16 + par * 8 + wid) * 32 + (lane & 31)), L_SCR);
        __builtin_amdgcn_s_sleep(WPER);
      }
    } else if (wid == 4) {
      __builtin_amdgcn_s_sleep(127); __builtin_amdgcn_s_sleep(127); __builtin_amdgcn_s_sleep(70);
    }
  }
  int tq, tp;
  tile_map<8, 28, 4, 7>(BID, tq, tp);
  const int p0 = tp * 128, q0 = tq * 128;
  if (wid >= 6) {
    Stream<KC, 16, 128, true> st;
    u_init(st, u, wid - 6, lane);
    if (own) {
      Operand Q{h, 32, q0, 64, 8 * 32 * TB, 0, true, nullptr, 0};
      WaitHookX hk;
      g_loader<KC, 16, 128, K3_NSQ, 32, -1, false>(qbuf, Q, wid - 6, lane, hk);
      BARRIER;
    }
    UHook hk;
    hk.kh = BID & 1;
    g_run<KC, 16, 128, K4_NS, 28 + KD, 3, false>(qbuf, st, wid - 6, lane, hk, false);
    __builtin_amdgcn_s_waitcnt(0x0F70);
    return;
  }
  if (wid >= 4) {
    Stream<KM, 16, 128> st;
    UFlags uf;
    w2_init(st, w2, cw1 + 224 * CW_BLK / 2, wid - 4, lane);
    uf.init(lds, flags, wid - 4, lane);
    int kh, tp4, tq4;
    k4_map(OPQ((int)BID), kh, tp4, tq4);
    W2Hook<K1NextHook<ResHook<2, 32>>> h4;
    static_cast<ResHook<2, 32>&>(h4) = ResHook<2, 32>{{}, res, tq4 * 128 + kh * 64, tp4 * 128, lds + K4_RES, wid - 4, lane};
    h4.on = l0 && ((((int)((size_t)wq_ >> 7)) + (BID & 1) * 4 + (BID >> 6)) & 7) != 3; h4.wq = wq_; h4.dstn = lds + L_A + (wid - 4) * 1024; h4.gwn = wid - 4; h4.lanen = lane;
    h4.kh = kh; h4.uf = &uf; h4.own = own; h4.gww = wid - 4; h4.nev = wid == 5 ? 0 : 99;
    h4.setup();
    if (own) {
      Operand P{w1, F_, p0, 64, S_ * F_ * 2, 0, false, cw1, 32};
      SyncHook<K4NextHook, 2> hk;
      hk.st = &st; hk.lds = lds; hk.gw = wid - 4;
      g_loader<KM, 16, 128, K3_NSP, 32, 2, false>(pbuf, P, wid - 4, lane, hk, true);
      BARRIER;
      if (wid == 4) {
        uf.publish();
      }
    }
    g_run<KM, 16, 128, K4_NS, 28 + KD, -1, false>(pbuf, st, wid - 4, lane, h4, own);
    __builtin_amdgcn_s_waitcnt(0x0F70);
    return;
  }
  if (own) {
    Gate gate;
    gate.prep(wid, h_flag(flags, BID, lane), (1 << NR_H) - 1, flags + C_HCNT + (tq * NR_H + tp % NR_H) * RS2 + (lane & 31), 0xFFFFFFFFull, 1);
    gate.m1 = NP3 == 2 ? 0x00010001ull : 0x00030003ull;
    gate.seam();
    const int wp = wid >> 1, wq = wid & 1;
    f32x4 acc[4][4];
#pragma unroll
    for (int i = 0; i < 4; ++i) {
      const u32x2 bv = *(const LAS u32x2*)(lds + L_BIA + (wp * 64 + (i >> 1) * 8 + (lane >> 4) * 16 + ((i ^ (lane >> 5)) & 1) * 4) * 2);
#pragma unroll
      for (int j = 0; j < 4; ++j) acc[i][j] = f32x4{lo2f(bv[0]), hi2f(bv[0]), lo2f(bv[1]), hi2f(bv[1])};
    }
    consumer16w<KM, KC, 4, 4, K3_NSP, 128 * 128, 128 * 128, 32, false>(pbuf, qbuf, acc, wp * 4, wq * 4, lane, gate, [&] { }, [&](auto&& mm) {
      int ln = lane;
      asm volatile("" : "+v"(ln));
      const int fr = ln & 15, fq = (ln & 63) >> 4;
      const char* ub = (const char*)u + (size_t)(tq * 56 + tp * 2) * TB + wp * (TB / 2);
      unsigned uo[4];
      {
        const unsigned s1 = pow2sel((fr >> 1) == 3, 7, 10);
        uo[0] = phys<64>(wq * 32 + (fr >> 1)) * 128 + (fr & 1) * 64 + fq * 16; uo[1] = uo[0] + s1; uo[2] = uo[1] + s1; uo[3] = uo[2] + s1;
      }
      mm(0, 8);
#pragma unroll
      for (int n = 0; n < 8; ++n) { __builtin_amdgcn_sched_group_barrier(0x8, 1, 0); __builtin_amdgcn_sched_group_barrier(0x2, 2, 0); }
      asm volatile("" : "+v"(uo[0]), "+v"(uo[1]), "+v"(uo[2]), "+v"(uo[3]));
      const float c0 = -2.0f * 0.7978845608028654f * 1.4426950408889634f, c1 = c0 * 0.044715f;
      auto shade = [&](int n0) {
        if (n0 < 32) {
          mm(n0, n0 + GM);
#pragma unroll
          for (int n = 0; n < GM; ++n) { __builtin_amdgcn_sched_group_barrier(0x8, 1, 0); __builtin_amdgcn_sched_group_barrier(0x400, 1, 0); }
          __builtin_amdgcn_sched_group_barrier(0x400, 16 - GM, 0);
        }
      };
#pragma unroll
      for (int g = 0; g < 4; ++g) {
        f32x2 x[8], z[8];
        auto abar = [&](int pos) {
          if (g == 3 && pos == APOS) {
            if (pos > 0) asm volatile("" : "+v"(z[0]), "+v"(z[1]), "+v"(z[2]), "+v"(z[3]), "+v"(z[4]), "+v"(z[5]), "+v"(z[6]), "+v"(z[7]));
            SCHED0; WAITV(2); BARRIER; SCHED0;
          }
        };
        abar(0);
        ALIGN8;
#pragma unroll
        for (int e = 0; e < 8; ++e) {
          const f32x4 v = acc[(g >> 1) * 2 + ((e >> 1) & 1)][(g & 1) * 2 + (e >> 2)];
          x[e] = f32x2{v[2 * (e & 1)], v[2 * (e & 1) + 1]};
          z[e] = x[e] * x[e];
        }
#pragma unroll
        for (int e = 0; e < 8; ++e) z[e] = z[e] * c1 + c0;
#pragma unroll
        for (int e = 0; e < 8; ++e) z[e] *= x[e];
        SCHED0;
        abar(1);
#pragma unroll
        for (int e = 0; e < 8; ++e) z[e] = f32x2{__builtin_amdgcn_exp2f(z[e][0]), __builtin_amdgcn_exp2f(z[e][1])};
        shade(8 + g * GM);
        abar(2);
        ALIGN8;
#pragma unroll
        for (int e = 0; e < 8; ++e) z[e] += 1.0f;
        SCHED0;
#pragma unroll
        for (int e = 0; e < 8; ++e) z[e] = f32x2{__builtin_amdgcn_rcpf(z[e][0]), __builtin_amdgcn_rcpf(z[e][1])};
        abar(3);
        ALIGN8;
        u32x4 o[2];
#pragma unroll
        for (int e = 0; e < 8; ++e) z[e] *= x[e];
#pragma unroll
        for (int e = 0; e < 8; ++e) o[e >> 2][e & 3] = pk2(z[e][0], z[e][1]);
#pragma unroll
        for (int a = 0; a < 2; ++a)
          asm volatile("global_store_dwordx4 %0, %1, %2 sc1\n\ts_nop 1" ::"v"(uo[(g & 1) * 2 + a]), "v"(o[a]), "s"(ub + (g >> 1) * TB) : "memory");
      }
    }, [](int) {});
  }
  k4_consumer(lds, OPQ(out), pstat, OPQ(part), OPQ(flags), wid);
}

extern "C" __global__ __launch_bounds__(512, 2) void k_all(const u16* x0_, const u16* wqkv_, const u16* wo_, const u16* w1_, const u16* b1_, const u16* w2_,
                                                           const u16* b2_, u16* out_, char* ws_) {
  LAS char* lds = (LAS char*)(size_t)0;
  {
    unsigned long long pc;
    unsigned off;
    asm volatile("s_cmp_lt_u32 %3, %5\n\ts_cbranch_scc0 1f\n\ts_getpc_b64 %0\n\tv_lshlrev_b32 %1, 7, %2\n\ts_mov_b32 m0, %4\n\t"
                 "v_min_u32 %1, k9_end-k_all-256, %1\n\tglobal_load_lds_dword %1, %0\n1:"
                 : "=&s"(pc), "=&v"(off) : "v"(threadIdx.x), "s"(blockIdx.x), "i"(L_SCR), "i"(CWN) : "memory", "scc", "m0");
  }
  auto ka = (const __attribute__((address_space(4))) u64*)__builtin_amdgcn_kernarg_segment_ptr();
  asm volatile("" : "+s"(ka));
  typedef __attribute__((address_space(1))) u16 gu16;
#define ARGS auto kb = ka; asm volatile("" : "+s"(kb)); \
  const u16 *x0 = (u16*)(gu16*)kb[0], *wqkv = (u16*)(gu16*)kb[1], *wo = (u16*)(gu16*)kb[2], *w1 = (u16*)(gu16*)kb[3], *b1 = (u16*)(gu16*)kb[4], *w2 = (u16*)(gu16*)kb[5], *b2 = (u16*)(gu16*)kb[6]; \
  u16* out = (u16*)(gu16*)kb[7]; char* w = OPQ(ws)
  char* ws = (char*)(u16*)(gu16*)ka[8];
  const int wid = __builtin_amdgcn_readfirstlane(threadIdx.x >> 6);
  {
    const u16 *x0 = (u16*)(gu16*)ka[0], *wqkv = (u16*)(gu16*)ka[1];
    const int lane = threadIdx.x & 63;
    int tq, tp;
    tile_map<16, 16, 8, 4>(blockIdx.x, tq, tp);
    if (wid >= 6) {
      Stream<KC, 16, 144> st;
      st.init(Operand{x0, D_, tq * 128 - 16, 64, S_ * D_ * 2, 0, false, nullptr, 0}, wid - 6, lane);
      st.stage<16>(lds + K1_QOFF + (wid - 6) * 1024, lane >= 8);
    } else if (wid >= 4) {
      Stream<KM, 16, 192> st;
      st.init(Operand{wqkv, 3 * D_, tp * 64, D_, 3 * D_ * D_ * 2, 0, false, nullptr, 0}, wid - 4, lane);
      st.stage<0>(lds + L_A + (wid - 4) * 1024);
    } else {
      const u16* xb = x0 + (size_t)(tq * 128 + wid * 32) * D_ + tp * 64;
      int t3q, t3p, kh4, t4p, t4q;
      tile_map<8, 28, 4, 7>(blockIdx.x, t3q, t3p);
      k4_map(blockIdx.x, kh4, t4p, t4q);
      const char* bp = wid >= 2 ? ws + W_EPOCH : (const char*)(u16*)(gu16*)ka[4 + 2 * (wid & 1)] + (wid ? t4q : min(t3p, 27)) * 256;
      asm volatile("s_mov_b32 m0, %[lx]\n\ts_nop 0\n\tglobal_load_lds_dwordx4 %[xo], %[xb]\n\ts_add_u32 m0, %[lx], 1024 - 64\n\ts_nop 0\n\tglobal_load_lds_dwordx4 %[xo], %[xb] offset:64\n\t"
                   "s_add_u32 m0, %[lx], 2048\n\ts_nop 0\n\tglobal_load_lds_dwordx4 %[xo], %[xc]\n\ts_add_u32 m0, %[lx], 3072 - 64\n\ts_nop 0\n\tglobal_load_lds_dwordx4 %[xo], %[xc] offset:64\n\t"
                   "s_mov_b32 m0, %[lb]\n\ts_nop 0\n\tglobal_load_lds_dword %[bo], %[bp] sc1"
                   :: [xo] "v"((lane & 15) * (D_ * 2) + (lane & 48)), [xb] "s"(xb), [xc] "s"(xb + 16 * D_), [bo] "v"(lane * 4), [bp] "s"(bp),
                      [lx] "s"(L_C + wid * 4096), [lb] "s"(wid >= 2 ? L_RS + wid * 256 : L_BIA + wid * 256) : "memory", "m0", "scc");
      if (wid == 1) {
        const int b = blockIdx.x, l = min(lane, 35), r = l / 12, c = l - r * 12;
        touch_line((const char*)wqkv + (64 + 64 * r + ((b >> 2) & 1) * 32 + (b >> 3)) * 6144 + (c >> 2) * 2048 + ((b & 3) * 4 + (c & 3)) * 128, L_SCR);
      }
      if (wid == 3) {
        SlowColHook<NoHook, 3, 2 * D_, 4, 32, 16> hs;
        hs.setup2(wqkv, 3 * D_, (tp & ~3) * 128, 0, blockIdx.x >> 3, lane, true, L_SCR);
        touch_line(hs.p != nullptr ? hs.p : (const char*)wqkv, L_SCR);
      }
    }
    CFENCE;
  }
#pragma unroll 1
  for (int layer = 0; layer < 2; ++layer) {
    {
      ARGS;
      const int l = OPQ(layer);
      k1_body(lds, OPQ(wid), l == 0, wqkv, (u16*)(gu16*)kb[l * 7], (u16*)(w + W_O), (u32x4*)(w + W_XCH),
              (u64*)(w + W_CTL) + l * C_WORDS, (u32x2*)(w + W_PST), wo, w1, w2, (u16*)(w + W_CW));
    }
    {
      ARGS;
      const int l = OPQ(layer);
      k2_body(lds, OPQ(wid), wo, (const u16*)(w + W_O), (u16*)(gu16*)kb[l * 7], (u16*)(w + W_XA), (u16*)(w + W_H),
              (unsigned long long*)(w + W_XCH), (u64*)(w + W_CTL) + l * C_WORDS, l == 0, w1, (const u16*)(w + W_CW));
    }
    {
      ARGS;
      const int l = OPQ(layer);
      k34_body(lds, OPQ(wid), l == 0, w1, (const u16*)(w + W_H), (u16*)(w + W_U), (const u16*)(w + W_CW), (u64*)(w + W_CTL) + l * C_WORDS,
               w2, (const u16*)(w + W_XA), out, l == 0 ? (u32x2*)(w + W_PST) : nullptr, (float*)(w + W_PART), wqkv);
    }
  }
  ARGS;
  if (blockIdx.x == 0 && wid == 7 && lane_id() == 0) __hip_atomic_store((u64*)(w + W_EPOCH), TAG, __ATOMIC_RELAXED, __HIP_MEMORY_SCOPE_AGENT);
}

extern "C" __global__ void k9_end() {}

extern "C" void kernel_launch(void* const* d_in, const int* in_sizes, int n_in, void* d_out, int out_size, void* d_ws,
                              size_t ws_size, hipStream_t stream) {
  (void)hipFuncSetAttribute((const void*)k_all, hipFuncAttributeMaxDynamicSharedMemorySize, L_SHM);
  k_all<<<256, 512, L_SHM, stream>>>((const u16*)d_in[0], (const u16*)d_in[1], (const u16*)d_in[2], (const u16*)d_in[4], (const u16*)d_in[5],
                                     (const u16*)d_in[6], (const u16*)d_in[7], (u16*)d_out, (char*)d_ws);
}
```

```cpp
#include <hip/hip_runtime.h>
#include <hip/hip_bf16.h>
#include <utility>
#include <type_traits>

typedef unsigned short u16;
using bf16x8 = __attribute__((ext_vector_type(8))) short;
using s16x4 = __attribute__((ext_vector_type(4))) short;
using f32x2 = __attribute__((ext_vector_type(2))) float;
using f32x4 = __attribute__((ext_vector_type(4))) float;
using f32x16 = __attribute__((ext_vector_type(16))) float;
using u32x2 = __attribute__((ext_vector_type(2))) unsigned;
using u32x4 = __attribute__((ext_vector_type(4))) unsigned;
using u32x12 = __attribute__((ext_vector_type(12))) unsigned;
typedef __bf16 bf16x2_t __attribute__((ext_vector_type(2)));
#define LAS __attribute__((address_space(3)))

constexpr int S_ = 2048, D_ = 1024, F_ = 3584;
template <int N> __device__ __forceinline__ int phys(int r) { return (r & 7) != 3 ? r : N + 4 + ((r >> 5) << 3) + ((r >> 3) & 3); }
constexpr int TB = 160 * 128;

__device__ __forceinline__ unsigned pk2(float a, float b) {
  f32x2 v = {a, b};
  return __builtin_bit_cast(unsigned, __builtin_convertvector(v, bf16x2_t));
}
__device__ __forceinline__ float lo2f(unsigned v) { return __uint_as_float(v << 16); }
__device__ __forceinline__ float hi2f(unsigned v) { return __uint_as_float(v & 0xFFFF0000u); }
__device__ __forceinline__ float bf2f(u16 v) { return __uint_as_float((unsigned)v << 16); }
__device__ __forceinline__ float rbf(float f) { return lo2f(pk2(f, f)); }
__device__ __forceinline__ u16 f2bf(float f) { return (u16)(pk2(f, f) & 0xFFFFu); }

enum { KC = 0, KM = 1 };
#define OPAQUE_BID unsigned BID = blockIdx.x; asm volatile("" : "+s"(BID))
__device__ __forceinline__ int OPQ(int v) { asm volatile("" : "+s"(v)); return v; }
__device__ __forceinline__ unsigned opq_bid() { OPAQUE_BID; return BID; }
__device__ __forceinline__ int lane_id() {
  const unsigned l = __builtin_amdgcn_mbcnt_hi(~0u, __builtin_amdgcn_mbcnt_lo(~0u, OPQ(0)));
  __builtin_assume(l < 64);
  return l;
}
constexpr int L_A = 0, L_B = 73728, L_C = L_B + 65536, L_D = L_C + 16384, L_TAG = L_D + 1280, L_CSQ = L_D + 1408, L_SCR = L_D + 1792, L_BIA = L_D + 2048, L_RS = L_D + 2560, L_MSK = L_RS + 1152, L_COF = L_MSK + 2048, L_SHM = L_COF + 1024;
typedef unsigned long long u64;
#define NR_O 8
#define NR_H 8
#define NR_U 8
#define NR_X 8
#define RS1 32
#define RS2 64
#define RSP 32
constexpr int C_FLAG = 0, C_UCNT = 1024 * RSP, C_HCNT = C_UCNT + 16 * NR_U * RS1, C_OCNT = C_HCNT + 8 * NR_H * RS2, C_XCNT = C_OCNT + 16 * NR_O * RS1,
              C_UCN2 = C_XCNT + 16 * NR_X * RS1, C_END = C_UCN2 + 16 * NR_U * RS1, C_WORDS = C_END <= 32768 ? 32768 : 131072;
static_assert(C_END <= C_WORDS, "control words");
#define TAG (*(volatile LAS u64*)(size_t)vconst<L_TAG>())

#define A1 16
#define A2 16
#define AC 2
#define CWN 8
#define FWS 1
#define KD 3
#define CKK 5
#define FBK 1
#define NP2 3
#define NP3 4
#define NP4 2
#define HQP 21
#define WT0 13
#define WSTG 2
#define WPER 16
#define ARMS 13
#define WC0 1
#define GM 8
#define HSW 1
#define GN 8
#define G0 3
#define GD 1
#define CFENCE asm volatile("" ::: "memory")
#define WAITV(n) asm volatile("s_waitcnt vmcnt(%0)" ::"i"(n) : "memory")
#define WAITL0 do { CFENCE; __builtin_amdgcn_s_waitcnt(0xC07F); CFENCE; } while (0)
#define BARRIER do { CFENCE; __builtin_amdgcn_s_barrier(); CFENCE; } while (0)
#define SCHED0 __builtin_amdgcn_sched_barrier(0)
#define ALIGN4 do { SCHED0; asm volatile(".fill (((. - k_all) >> 2) & 1) ^ 1, 4, 0xBF800000"); SCHED0; } while (0)
#define ALIGN8 do { SCHED0; asm volatile(".p2align 3"); SCHED0; } while (0)

struct Operand {
  const u16* base;
  int ld;
  int off0;
  int cstride;
  int bytes;
  int kstep0;
  bool holes;
  const u16* alt;
  int alt_nt;
};
constexpr int CW_BLK = 80 * 128;

template <int L, int MS>
__device__ __forceinline__ int swz(int line) {
  if constexpr (L == KC) {
    return MS == 16 ? (line & 7) : ((line >> 1) & 7);
  } else {
    return MS == 16 ? ((((line >> 1) & 1) | (((line >> 3) & 1) << 1)) << 1) : (((line >> 1) & 1) << 2);
  }
}
template <int N> __device__ __forceinline__ void waitv() { WAITV(N); }
__device__ __forceinline__ unsigned pow2sel(bool c, int a, int b) { unsigned e = c ? a : b; asm volatile("" : "+v"(e)); return 1u << e; }
template <unsigned C> __device__ __forceinline__ unsigned vconst() { unsigned v; asm volatile("v_mov_b32 %0, %1" : "=v"(v) : "i"(C)); return v; }

__device__ __forceinline__ u64 vz() { u64 v; asm volatile("v_mov_b64 %0, 0" : "=v"(v)); return v; }

__device__ __forceinline__ u64 tag_ld() {
  u64 t;
  asm volatile("ds_read_b64 %0, %1\n\ts_waitcnt lgkmcnt(0)" : "=v"(t) : "v"(vconst<L_TAG>()) : "memory");
  return t;
}
__device__ __forceinline__ void st16(void* p, u32x4 v) {
  asm volatile("global_store_dwordx4 %0, %1, off sc1\n\ts_nop 1" ::"v"(p), "v"(v) : "memory");
}

__device__ __forceinline__ const char* noslow(const void* base, unsigned line) {
  return (const char*)base + (size_t)((((unsigned)((size_t)base >> 7) + line) & 7) == 3 ? line ^ 1 : line) * 128;
}
__device__ __forceinline__ void touch_line(const void* p, unsigned lds_scratch) {
  unsigned keep;
  asm volatile("s_mov_b32 %0, m0\n\ts_mov_b32 m0, %2\n\ts_nop 0\n\tglobal_load_lds_dword %1, off\n\ts_mov_b32 m0, %0"
               : "=&s"(keep) : "v"(p), "s"(lds_scratch) : "memory");
}

struct NoHook {
  static constexpr int EXTRA = 0, NFIRST = 0, AUX = 0, NEXT = 0;
  static constexpr bool EVOD = false;
  static constexpr bool OWNP = false;
  template <int T> __device__ __forceinline__ void next() {}
  static constexpr bool PREALL = false;
  static constexpr bool PIN = false, UNROLL = false, COLD = false, PSW = false;
  __device__ __forceinline__ bool cold() const { return false; }
  template <int S> __device__ __forceinline__ void stepc() {}
  template <int S, int N> __device__ __forceinline__ void wait() { waitv<N>(); }
  template <int T> __device__ __forceinline__ void tail() {}
  __device__ __forceinline__ void ready() {}
  __device__ __forceinline__ void first() {}
  __device__ __forceinline__ void afterr() {}
  __device__ __forceinline__ void last() {}
  __device__ __forceinline__ void mid() {}
  __device__ __forceinline__ void step(int) {}
};

template <int L, int MS, int B, bool PSW = false>
struct Stream {
  static constexpr int LW = B / 16, STG = B * 128;
  static constexpr int NSUB = (L == KM && B == 128 && !PSW) ? 2 : 1;
  unsigned voff[LW];
  const void* gbase[NSUB];
  int gbytes[NSUB], so[NSUB], inc[NSUB], jmp[NSUB];
  __device__ __forceinline__ void init(const Operand& X, int gw, int lane) {
    const int r8 = lane >> 3, c8 = lane & 7, l0 = gw * 8 + r8;
    const unsigned sw = (unsigned)((c8 ^ (PSW && L == KC ? (r8 & 3) | (gw << 2) : swz<L, MS>(l0))) * 16);
    if constexpr (L == KC) {
      gbase[0] = X.base; gbytes[0] = X.bytes;
      if (X.holes) {
        jmp[0] = 27 * TB;
        inc[0] = TB; so[0] = ((X.off0 >> 7) * X.ld + X.kstep0) * TB;
        {
          const bool iso = X.ld == 16;
          const int ln = !iso ? l0 >> 1 : ((l0 >> 1) & ~1) | (l0 & 1), ps = !iso ? l0 & 1 : (l0 >> 1) & 1;
          const bool h = ln == 3;
          const unsigned v0 = (h ? 68 : ln) * 128 + ps * 64 + (sw & 48) + (sw >> 6) * (TB / 2), s1 = pow2sel(h, 7, 10), s4 = pow2sel(h, 10, 12);
#pragma unroll
          for (int i = 0; i < LW; ++i) voff[i] = i == 0 ? v0 + ((X.off0 & 64) ? s4 : 0) : (i == 4 ? v0 + s4 : voff[i - 1] + s1);
        }
      } else {
        inc[0] = 128; so[0] = X.kstep0 * 128;
#pragma unroll
        for (int i = 0; i < LW; ++i)
          voff[i] = (unsigned)(i == 0 ? max(X.off0 + 2 * r8 + gw, 0) : X.off0 + 16 + 2 * l0 + 32 * ((i - 1) & 3) + ((i - 1) >> 2)) * (unsigned)X.ld * 2u + sw;
      }
    } else if constexpr (PSW) {
      gbase[0] = X.base; gbytes[0] = X.bytes;
      inc[0] = CW_BLK; so[0] = (X.off0 >> 6) * 16 * CW_BLK;
      const bool h = r8 == 3;
      const unsigned v0 = (h ? 68 + gw : l0) * 128 + sw, s2 = pow2sel(h, 8, 11), s1 = pow2sel(h, 10, 12);
#pragma unroll
      for (int i = 0; i < LW; ++i) voff[i] = i == 0 ? v0 : i == 2 ? v0 + s1 : i >= 4 ? voff[i - 4] + 16 * CW_BLK : voff[i - 1] + s2;
    } else if constexpr (NSUB == 1) {
      gbase[0] = X.base; gbytes[0] = X.bytes;
      inc[0] = 128 * X.ld; so[0] = X.kstep0 * inc[0] + X.off0 * 2;
#pragma unroll
      for (int i = 0; i < LW; ++i) voff[i] = (unsigned)((l0 + 16 * (i & 3)) * X.ld + (i >> 2) * X.cstride) * 2u + sw;
    } else {
      const bool isw2 = X.ld == S_;
      const unsigned sx = isw2 ? sw : ((sw & 48) << 1) | ((sw & 64) >> 2);
#pragma unroll
      for (int t = 0; t < NSUB; ++t) {
        const int lc = (X.off0 >> 6) ^ t;
        const bool slow = (((int)((size_t)X.base >> 7) + lc) & 7) == 3;
        gbase[t] = slow ? (const void*)X.alt : (const void*)X.base;
        gbytes[t] = slow ? (X.ld >> 9) * X.alt_nt * CW_BLK : X.bytes;
        inc[t] = slow ? CW_BLK : 128 * X.ld;
        so[t] = X.kstep0 * inc[t] + (slow ? (lc >> 3) * X.alt_nt * CW_BLK : lc * 128);
        const unsigned rowb = slow ? 128 : X.ld * 2;
        const bool h = (slow ? r8 : 0) == 3;
        const unsigned grpb = isw2 && !slow ? 2 * rowb : rowb;
        const unsigned v0 = (h ? 68 + gw : r8) * rowb + (h ? 0 : gw * 8 * grpb) + sx, s2 = (h ? 2 : 16) * grpb, s1 = (h ? 8 : 32) * grpb;
        jmp[t] = slow ? 27 * CW_BLK : (28 * 64 - 8) * rowb;
        voff[4 * t] = v0; voff[4 * t + 1] = v0 + s2; voff[4 * t + 2] = v0 + s1; voff[4 * t + 3] = v0 + s1 + s2;
      }
    }
  }
  __device__ __forceinline__ void pin() {
#pragma unroll
    for (int i = 0; i < LW; ++i) asm volatile("" : "+v"(voff[i]));
#pragma unroll
    for (int t = 0; t < NSUB; ++t) asm volatile("" : "+s"(so[t]));
  }
  __device__ __forceinline__ void skip(int n) {
#pragma unroll
    for (int t = 0; t < NSUB; ++t) so[t] += n * inc[t];
  }
  __device__ __forceinline__ void twice() {
#pragma unroll
    for (int t = 0; t < NSUB; ++t) inc[t] *= 2;
  }
  __device__ __forceinline__ void back() {
#pragma unroll
    for (int t = 0; t < NSUB; ++t) so[t] -= jmp[t];
  }
  template <int AUX, int NT_ = NSUB>
  __device__ __forceinline__ void stage(LAS char* dst, bool act0 = true) {
#pragma unroll
    for (int t = 0; t < NT_; ++t) {
      const auto rsrc = __builtin_amdgcn_make_buffer_rsrc((void*)gbase[t], 0, gbytes[t], 0x00020000);
#pragma unroll
      for (int i = t * (LW / NSUB); i < (t + 1) * (LW / NSUB); ++i) {
#if defined(__HIP_DEVICE_COMPILE__)
        if (i > 0 || act0) __builtin_amdgcn_raw_ptr_buffer_load_lds(rsrc, (LAS void*)(dst + i * 2048), 16, voff[i], so[t], 0, AUX);
#endif
      }
      so[t] += inc[t];
    }
  }
};

template <int N, class F>
__device__ __forceinline__ void static_for(F&& f) {
  [&]<int... I>(std::integer_sequence<int, I...>) { (f(std::integral_constant<int, I>{}), ...); }(std::make_integer_sequence<int, N>{});
}

template <int L, int MS, int B, int NS, int NT, int KID, bool SKIP0, class Hook>
__device__ __forceinline__ void g_run(LAS char* ring, Stream<L, MS, B, Hook::PSW>& st, int gw, int lane, Hook& hook, bool filled) {
  constexpr int LW = B / 16, STG = B * 128;
  constexpr int NPRE = Hook::PIN ? (Hook::COLD ? 2 : NT == 16 ? NP2 : NT == 32 ? NP3 : NP4) : NS;
  static_assert(Hook::PREALL, "cfg");
  static_assert(B % 16 == 0 && NS >= 3 && NT >= NS, "cfg");
  const bool act0 = !SKIP0 || lane >= 8;
  auto stage = [&](int slot) { st.template stage<Hook::AUX>(ring + slot * STG + gw * 1024, act0); };
  hook.ready();
  if (Hook::COLD && !Hook::PIN ? hook.cold() : !filled) {
    if (Hook::COLD && hook.cold()) st.skip(1); else stage(0);
    stage(1);
    if constexpr (Hook::PREALL) {
      if (!hook.cold()) {
#pragma unroll
        for (int i = 2; i < NPRE; ++i) stage(i);
      }
    }
  }
  SCHED0;
  if constexpr (Hook::COLD && !Hook::PIN) {
    if (hook.cold()) {
      waitv<LW>();
      BARRIER;
#pragma unroll
      for (int i = 2; i < NS; ++i) stage(i);
    }
    SCHED0;
    hook.first();
    if (!hook.cold()) {
      BARRIER;
      if (!filled) { stage(0); stage(1); stage(2); }
      BARRIER;
      waitv<(NS - 1) * LW>();
      BARRIER;
    }
  } else {
    if constexpr (!Hook::PIN) hook.first();
    if constexpr (!Hook::OWNP) {
      waitv<(NPRE - 1) * LW + Hook::NFIRST>();
      BARRIER;
    }
#pragma unroll
    for (int i = NPRE; i < NS; ++i) stage(i);
    if constexpr (Hook::PIN) hook.first();
  }
  if constexpr (Hook::UNROLL) {
    [&]<int... S>(std::integer_sequence<int, S...>) {
      (([&] {
         hook.template wait<S, (NS - 2) * LW>();
         BARRIER;
         stage(S % NS);
         hook.step(S);
         hook.template stepc<S>();
         SCHED0;
       }()), ...);
    }(std::make_integer_sequence<int, NT - NS>{});
  } else {
    int slot = 0;
#pragma unroll 1
    for (int s = 0; s < NT - NS - (Hook::EVOD ? 2 * KD : 0); ++s) {
      waitv<(NS - 2) * LW>();
      BARRIER;
      if (Hook::EVOD && s == 14 - NS) st.back();
      stage(slot);
      slot = (slot == NS - 1) ? 0 : slot + 1;
      hook.step(s);
    }
  }
  constexpr int LH = Hook::EVOD && L == KM ? LW / 2 : LW;
  if constexpr (Hook::EVOD) {
    static_for<2 * KD>([&](auto kc) {
      constexpr int k = decltype(kc)::value, n = NT - 2 * KD + k;
      waitv<(k > 1 ? LH : LW) + (k > 0 ? LH : LW)>();
      BARRIER;
      if constexpr (k == KD) st.so[0] += (hook.kh ? -14 - KD : 14 - KD) * st.inc[0];
      st.template stage<Hook::AUX, 1>(ring + (n % NS) * STG + gw * 1024);
    });
  }
  hook.last();
  [&]<int... T>(std::integer_sequence<int, T...>) {
    (([&] {
       hook.template tail<T>();
       if constexpr (T < NS - 2) hook.template wait<NT - NS + T, (NS - 2 - T) * LH + Hook::EXTRA + T * Hook::NEXT>();
       if constexpr (T == NS - 2) hook.template wait<NT - NS + T, T * Hook::NEXT>();
       BARRIER;
       hook.template next<T>();
       if constexpr (T == NS - 2) hook.mid();
     }()), ...);
  }(std::make_integer_sequence<int, NS>{});
}

template <int L, int MS, int B, int NS, int NT, int KID, bool SKIP0, class Hook>
__device__ __forceinline__ void g_loader(LAS char* ring, const Operand& X, int gw, int lane, Hook& hook, bool filled = false) {
  Stream<L, MS, B, Hook::PSW> st;
  st.init(X, gw, lane);
  if constexpr (Hook::PIN) st.pin();
  if (filled) st.skip(NS);
  g_run<L, MS, B, NS, NT, KID, SKIP0>(ring, st, gw, lane, hook, filled);
}

struct ResHook : NoHook {
  static constexpr bool PSW = true;
  static constexpr int AUX = 16;
  const u16* base; int row0, col0; LAS char* dst; int gw, lane;
  __device__ __forceinline__ void first() {
    if (base != nullptr) {
      const int r8 = lane >> 3, c8 = lane & 7;
      const unsigned vo = ((unsigned)(row0 + gw * 8 + r8) * D_ + col0) * 2u + (unsigned)((c8 ^ swz<KC, 32>(gw * 8 + r8)) * 16);
      const auto rsrc = __builtin_amdgcn_make_buffer_rsrc((void*)base, 0, S_ * D_ * 2, 0x00020000);
#pragma unroll
      for (int i = 0; i < 8; ++i) {
#if defined(__HIP_DEVICE_COMPILE__)
        __builtin_amdgcn_raw_ptr_buffer_load_lds(rsrc, (LAS void*)(dst + gw * 1024 + i * 2048), 16, vo, ((i & 3) * 16 * D_ + (i >> 2) * 64) * 2, 0, 0);
#endif
      }
    }
    WAITV(0); BARRIER;
  }
};

template <class Base, int NG, int GS, int NC, int NW, int NT>
struct SlowColHook : Base {
  const char* p;
  int st, inc8;
  unsigned scr;
  const u16* base_; int ld_, cb0_, kstep0_, j_, lane_; bool leader_;
  __device__ __forceinline__ void setup(const u16* base, int ld, int cb0, int kstep0, int j, int lane, bool leader, unsigned scratch) {
    base_ = base; ld_ = ld; cb0_ = cb0; kstep0_ = kstep0; j_ = j; lane_ = lane; leader_ = leader; scr = scratch;
  }
  __device__ __forceinline__ void setup2(const u16* base, int ld, int cb0, int kstep0, int j, int lane, bool leader, unsigned scratch) {
    asm volatile("" : "+v"(lane));
    constexpr int MAXC = (NC + 7) / 8, PER = (NG * MAXC * 64 + NW - 1) / NW;
    static_assert(PER * 8 <= 64, "lanes");
    const int cf = (3 - (int)(((size_t)base + cb0) >> 7)) & 7;
    const int li = j * PER + lane % PER;
    st = lane / PER;
    const int ci = li >> 6, k = li & 63, g = ci / MAXC, c = cf + 8 * (ci % MAXC);
    const bool act = leader && st < 8 && g < NG && c < NC;
    p = act ? (const char*)base + (size_t)((kstep0 + st) * 64 + k) * ld * 2 + cb0 + g * GS + c * 128 : nullptr;
    inc8 = 8 * 64 * ld * 2;
    scr = scratch;
  }
  __device__ __forceinline__ void touch(bool go = true) {
    if (go && p != nullptr && st < NT) touch_line(p, scr);
    p = p != nullptr ? p + inc8 : p;
    st += 8;
  }
  __device__ __forceinline__ void first() { setup2(base_, ld_, cb0_, kstep0_, j_, lane_, leader_, scr); touch(!Base::cold()); Base::first(); }
  __device__ __forceinline__ void step(int s) { if ((s & 7) == 0 && s + 8 < NT) touch(); Base::step(s); }
};

#define PSLP 3
__device__ __forceinline__ void flag_set(u64* p, u64 tag) { asm volatile("global_atomic_swap_x2 %0, %1, off sc1" ::"v"(p), "v"(tag) : "memory"); }
#define POLL1(r) "global_atomic_or_x2 %[" #r "], %[q], %[z], off sc0 sc1\n\ts_sleep %[sl]\n\t"
#define POLL2(r) "s_waitcnt vmcnt(3)\n\tv_cmp_ne_u64 vcc, %[tag], %[" #r "]\n\ts_cbranch_vccz 2f\n\t" POLL1(r)
#define POLL_LOOP "s_mov_b64 %[sv], exec\n\ts_mov_b64 exec, %[em]\n\ts_mov_b32 %[n], 0\n\t" POLL1(a) POLL1(b) POLL1(c) POLL1(d) POLL_BODY
#define POLL_BODY \
  "1:\n\t" POLL2(a) POLL2(b) POLL2(c) POLL2(d) \
  "s_add_u32 %[n], %[n], 1\n\ts_cmp_lt_u32 %[n], 0x8000\n\ts_cbranch_scc1 1b\n\t" \
  "2:\n\ts_mov_b64 exec, %[sv]\n\t"
#define PCHK(r, l) "v_cmp_ne_u64 vcc, %[tag], %[" #r "]\n\ts_and_b64 %[t], vcc, %[m1]\n\ts_cbranch_scc1 " #l "f\n\ts_cbranch_vccz 2f\n\ts_barrier\n\ts_mov_b64 %[m1], -1\n" #l ":\n\t"
#define POLL3(r, l) "s_waitcnt vmcnt(3)\n\t" PCHK(r, l) POLL1(r)
#define PEND "s_add_u32 %[n], %[n], 1\n\ts_cmp_lt_u32 %[n], 0x8000\n\ts_cbranch_scc1 1b\n" \
             "2:\n\ts_mov_b64 exec, -1\n\ts_cmp_eq_u64 %[m1], -1\n\ts_cbranch_scc1 8f\n\ts_barrier\n8:"
#define POLL0(r) "global_atomic_or_x2 %[" #r "], %[q], %[z], off sc0 sc1\n\t"
struct Gate {
  const u64* p; u64 em, sm, on, tag, a, b, c, d;
  __device__ __forceinline__ void prep(int wid, const u64* set, u64 sm_, const u64* get, u64 em_, u64 on_) {
    p = wid == 0 ? set : get; em = wid == 1 ? em_ : 0; sm = wid == 0 ? sm_ : 0; on = on_;
    tag = TAG;
    asm volatile("" : "+v"(p), "+v"(tag));
  }
  __device__ __forceinline__ void seam() {
    u64 z = vz();
    asm volatile("s_cmp_eq_u64 %[on], 0\n\ts_cbranch_scc1 7f\n\ts_waitcnt vmcnt(0)\n\ts_barrier\n\t"
                 "s_cmp_eq_u64 %[sm], 0\n\ts_cbranch_scc1 6f\n\ts_mov_b64 exec, %[sm]\n\tglobal_atomic_swap_x2 %[q], %[tag], off sc1\n"
                 "6:\n\ts_cmp_eq_u64 %[em], 0\n\ts_cbranch_scc1 7f\n\ts_mov_b64 exec, %[em]\n\t" POLL0(a) "\n7:\n\ts_mov_b64 exec, -1"
                 : [a] "=&v"(a) : [q] "v"(p), [z] "v"(z), [tag] "v"(tag), [sm] "s"(sm), [em] "s"(em), [on] "s"(on) : "memory", "scc");
  }
  u64 m1;
  __device__ __forceinline__ void pass() {
    u64 z = vz(), t;
    unsigned n;
    asm volatile("s_cmp_eq_u64 %[on], 0\n\ts_cbranch_scc1 8f\n\ts_cmp_eq_u64 %[em], 0\n\ts_cbranch_scc1 2f\n\ts_mov_b64 exec, %[em]\n\ts_mov_b32 %[n], 0\n\t"
                 POLL0(b) "s_waitcnt vmcnt(1)\n\t" PCHK(a, 3) POLL1(c) POLL1(d) POLL0(a)
                 "1:\n\t" POLL3(b, 4) POLL3(c, 5) POLL3(d, 6) POLL3(a, 7) PEND
                 : [a] "+v"(a), [b] "=&v"(b), [c] "=&v"(c), [d] "=&v"(d), [n] "=&s"(n), [t] "=&s"(t), [m1] "+s"(m1)
                 : [q] "v"(p), [z] "v"(z), [tag] "v"(tag), [em] "s"(em), [on] "s"(on), [sl] "i"(PSLP) : "memory", "vcc", "scc");
  }
  __device__ __forceinline__ void done() {
    asm volatile("s_cmp_eq_u64 %[em], 0\n\ts_cbranch_scc1 9f\n\ts_waitcnt vmcnt(0)\n9:" :: "v"(a), "v"(b), "v"(c), "v"(d), [em] "s"(em) : "memory", "scc");
  }
};
struct DrainGate {
  __device__ __forceinline__ void pass() { BARRIER; WAITV(0); }
  __device__ __forceinline__ void done() {}
};
struct WaitHook : NoHook {
  static constexpr int AUX = 16;
  static constexpr bool PIN = true;
  static constexpr bool PREALL = true;
  __device__ __forceinline__ void ready() {
    BARRIER;
  }
};
struct WaitHookX : WaitHook {
  __device__ __forceinline__ void ready() { WAITV(0); BARRIER; BARRIER; }
};
struct WaitHookP : WaitHook { static constexpr bool PSW = true; };
template <class Base, int NBAR>
struct SyncHook : Base {
  static constexpr bool PREALL = true;
  __device__ __forceinline__ void first() {
    Base::first();
#pragma unroll
    for (int i = 0; i < NBAR; ++i) {
      BARRIER;
    }
    Base::afterr();
  }
};

template <int L, int MS, int NF, bool PERM = false, int FS = 1, bool HS = false>
struct FragAddr {
  static constexpr int NA = (L == KC) ? (MS == 16 ? 2 : 4) : (HS ? 2 * NF : NF);
  unsigned a[NA];
  __device__ __forceinline__ void init(LAS char* buf, int f0, int lane, int fx = 0) {
    const unsigned base = (unsigned)(size_t)buf;
    if constexpr (MS == 16) {
      const int fr = lane & 15, fq = lane >> 4, tq = (lane & 15) >> 2, tp = lane & 3;
      if constexpr (L == KC) {
        const int row = f0 * 16 + (PERM ? tq * 8 + tp : fr);
        a[0] = base + row * 128 + ((fq ^ (fr & 7)) * 16);
        a[1] = base + row * 128 + (((4 + fq) ^ (fr & 7)) * 16);
      } else {
        const int gl = ((tq >> 1) & 1) | ((fq & 1) << 1);
#pragma unroll
        for (int i = 0; i < NF; ++i) {
          const int cb = HS ? (i >> 1) * 4 + f0 + (i & 1) : (f0 + i * FS) ^ fx;
          if constexpr (PERM) a[i] = base + (fq * 8 + tq) * 128 + (cb >> 2) * 8192 + ((((cb & 2) * 2 + tp) ^ (gl * 2)) * 16) + ((cb ^ (tp >> 1)) & 1) * 8;
          else a[i] = base + (fq * 8 + tq) * 128 + tp * 8 + (cb >> 2) * 8192 + (((cb & 3) ^ gl) * 32);
          if constexpr (HS) { a[i] += (fq >> 1) * 512; a[NF + i] = a[i] ^ 512; }
        }
      }
    } else {
      const int r = lane & 31, h = lane >> 5, g = lane >> 4, tq = (lane & 15) >> 2, tp = lane & 3;
      if constexpr (L == KC) {
#pragma unroll
        for (int t = 0; t < 4; ++t) a[t] = base + (f0 * 32 + r) * 128 + (((2 * t + h) ^ ((r >> 1) & 7)) * 16);
      } else {
#pragma unroll
        for (int i = 0; i < NF; ++i) {
          const int cb = (f0 + i) ^ fx;
          a[i] = base + (8 * (g >> 1) + tq) * 128 + (cb >> 1) * 8192 + (((cb & 1) ^ ((tq >> 1) & 1)) * 64) + (g & 1) * 32 + tp * 8;
        }
      }
    }
#pragma unroll
    for (int i = 0; i < NA; ++i) asm volatile("" : "+v"(a[i]));
  }
  __device__ __forceinline__ bf16x8 read(int so, int i, int t) const {
    if constexpr (L == KC) {
      return *(const LAS bf16x8*)((LAS char*)(size_t)a[t] + (so + (PERM ? (i >> 1) * 4096 + (i & 1) * 512 : i * (MS * 128))));
    } else {
      LAS char* p = (LAS char*)(size_t)a[i] + (so + t * (MS == 16 ? 4096 : 2048));
      s16x4 lo = __builtin_amdgcn_ds_read_tr16_b64_v4i16((LAS s16x4*)(p));
      s16x4 hi = __builtin_amdgcn_ds_read_tr16_b64_v4i16((LAS s16x4*)(HS ? (LAS char*)(size_t)a[HS ? NF + i : i] + (so + t * 4096) : p + 512));
      return __builtin_shufflevector(lo, hi, 0, 1, 2, 3, 4, 5, 6, 7);
    }
  }
};

template <int NM, int PER>
__device__ __forceinline__ void interleave() {
#pragma unroll
  for (int i = 0; i < NM; ++i) {
    __builtin_amdgcn_sched_group_barrier(0x8, 1, 0);
    __builtin_amdgcn_sched_group_barrier(0x100, PER, 0);
  }
}

template <int PL, int QL, int FP, int FQ, int NS, int PSTG, int QSTG, int NT, bool SPLIT, class G, class Late, class Mid, class Tick>
__device__ __forceinline__ void consumer16w(LAS char* pbuf, LAS char* qbuf, f32x4 (&acc)[FP][FQ], int fp0, int fq0, int lane, G& gate, Late&& late, Mid&& mid, Tick&& tick) {
  FragAddr<PL, 16, FP, true> pa; pa.init(pbuf, fp0, lane);
  FragAddr<QL, 16, FQ, false, 1, QL == KM && HSW> qa; qa.init(qbuf, fq0, lane);
  struct Fr { bf16x8 p[2][FP]; bf16x8 q[2][FQ]; };
  auto rdt = [&](Fr& f, int slot, int t) {
#pragma unroll
    for (int i = 0; i < FP; ++i) f.p[t][i] = pa.read(slot * PSTG, i, t);
#pragma unroll
    for (int j = 0; j < FQ; ++j) f.q[t][j] = qa.read(slot * QSTG, j, t);
  };
  auto rd = [&](Fr& f, int slot) { rdt(f, slot, 0); rdt(f, slot, 1); };
  auto mmr = [&](const Fr& f, int j0, int j1, int t0 = 0, int t1 = 2) {
#pragma unroll
    for (int t = t0; t < t1; ++t)
#pragma unroll
      for (int j = j0; j < j1; ++j)
#pragma unroll
        for (int ii = 0; ii < FP; ++ii) { const int i = (j & 1) ? FP - 1 - ii : ii; acc[i][j] = __builtin_amdgcn_mfma_f32_16x16x32_bf16(f.p[t][i], f.q[t][j], acc[i][j], 0, 0, 0); }
  };
  constexpr int U = NS;
  constexpr int NG = (NT - 2 - SPLIT) / U, REM = (NT - 1) % U;
  Fr f[2];
  auto step = [&](auto uc) {
    constexpr int u = (decltype(uc)::value + 1) % U;
    SCHED0; WAITL0; BARRIER; SCHED0;
    rd(f[(u + 1) & 1], (u + 1) % NS);
    mmr(f[u & 1], 0, FQ);
    interleave<2 * FP * FQ, FQ < 4 ? 2 : 1>();
  };
  if constexpr (FQ < 4) {
    gate.pass();
    late();
  }
#pragma unroll
  for (int i = 0; i < FP; ++i)
#pragma unroll
    for (int j = 0; j < FQ; ++j) asm volatile("" : "+v"(acc[i][j]));
  if constexpr (FQ == 4) gate.pass();
  BARRIER;
  rdt(f[0], 0, 0);
  SCHED0;
  rdt(f[0], 0, 1);
  SCHED0;
  mmr(f[0], 0, FQ, 0, 1);
  SCHED0; gate.done(); WAITL0; BARRIER; SCHED0;
  rd(f[1], 1);
  mmr(f[0], 0, FQ, 1, 2);
  interleave<FP * FQ, FQ < 4 ? 3 : 2>();
#pragma unroll 1
  for (int g = 0; g < NG; ++g) static_for<U>(step);
  if constexpr (!SPLIT) {
    static_for<REM - 1>(step);
    SCHED0; WAITL0; BARRIER; SCHED0;
    if constexpr (FQ < 4) {
      mmr(f[REM & 1], 0, 1);
      mmr(f[REM & 1], 1, 2);
    } else mid([&](int n0, int n1) {
#pragma unroll
      for (int n = n0; n < n1; ++n) {
        const int t = (n >> 2) & 1, i = (n >> 4) * 2 + (n & 1), j = ((n >> 3) & 1) * 2 + ((n >> 1) & 1);
        acc[i][j] = __builtin_amdgcn_mfma_f32_16x16x32_bf16(f[REM & 1].p[t][i], f[REM & 1].q[t][j], acc[i][j], 0, 0, 0);
      }
    });
  } else {
    static_for<(NT - 3) % U>(step);
    SCHED0; WAITL0; BARRIER; SCHED0;
    rd(f[REM & 1], REM % NS);
    mmr(f[(REM - 1) & 1], FQ / 2, FQ);
    interleave<FP * FQ, 2>();
    SCHED0; WAITL0; BARRIER; SCHED0;
    auto m1 = [&](const Fr& g, int t, int i, int j) { acc[i][j] = __builtin_amdgcn_mfma_f32_16x16x32_bf16(g.p[t][i], g.q[t][j], acc[i][j], 0, 0, 0); };
#pragma unroll
    for (int i = 0; i <= FP; ++i) {
      SCHED0;
#pragma unroll
      for (int n = 0; n < FQ; ++n) {
        if (i < FP) m1(f[REM & 1], n / (FQ / 2), i, FQ / 2 + n % (FQ / 2));
        else m1(f[(REM - 1) & 1], 0, n, 0);
      }
      if (i > 0) {
        mid(i - 1);
        __builtin_amdgcn_sched_group_barrier(0x8, 2, 0);
        __builtin_amdgcn_sched_group_barrier(0x2, 2, 0);
        __builtin_amdgcn_sched_group_barrier(0x8, 1, 0);
        __builtin_amdgcn_sched_group_barrier(0x2, 2, 0);
        __builtin_amdgcn_sched_group_barrier(0x8, 1, 0);
        __builtin_amdgcn_sched_group_barrier(0x2, 1, 0);
        __builtin_amdgcn_sched_group_barrier(0x40, 1, 0);
      }
    }
    SCHED0;
#pragma unroll
    for (int i = 0; i < FP; ++i) m1(f[(REM - 1) & 1], 1, i, 0);
    mmr(f[(REM - 1) & 1], 1, FQ / 2);
    static_for<2 * KD + 1>([&](auto kc) {
      constexpr int k = decltype(kc)::value, n = NT - 1 + k;
      SCHED0;
      if constexpr (k > 0) { tick(k - 1); WAITL0; BARRIER; SCHED0; }
      if constexpr (k < 2 * KD) {
#pragma unroll
        for (int t = 0; t < 2; ++t) {
#pragma unroll
          for (int i = 0; i < FP; ++i) f[(n + 1) & 1].p[t][i] = pa.read(((n + 1) % NS) * PSTG, i, t);
#pragma unroll
          for (int j = 0; j < FQ / 2; ++j) f[(n + 1) & 1].q[t][j] = qa.read(((n + 1) % NS) * QSTG, j, t);
        }
      } else late();
      mmr(f[n & 1], 0, FQ / 2);
      if constexpr (k < 2 * KD) interleave<FP * FQ, 2>();
    });
    SCHED0;
    tick(2 * KD);
  }
}

template <int NQT, int NPT, int RQ, int RP>
__device__ __forceinline__ void tile_map(unsigned BID, int& tq, int& tp) {
  static_assert((NQT / RQ) * (NPT / RP) == 8 && NQT % RQ == 0 && NPT % RP == 0, "8 regions");
  const int x = BID & 7, j = BID >> 3;
  constexpr int GP = NPT / RP;
  tq = (x / GP) * RQ + j % RQ;
  tp = (x % GP) * RP + j / RQ;
}

__device__ __forceinline__ void k4_map(unsigned BID, int& kh, int& tp, int& tq) {
  const int x8 = BID & 7, j8 = BID >> 3;
  kh = x8 & 1; tp = ((x8 >> 1) & 1) * 4 + (j8 >> 3); tq = (x8 >> 2) * 8 + (j8 & 7);
}

constexpr int K1_NS = 3, K1_PSTG = 192 * 128, K1_QSTG = 144 * 128;
constexpr int K1_QOFF = L_B, K1_ST = L_D;

__device__ __forceinline__ float max2(float a, float b) { return __builtin_elementwise_maximum(a, b); }
__device__ __forceinline__ float max3(float a, float b, float c) { return max2(max2(a, b), c); }
__device__ __forceinline__ float rows_sum(float v) {
  auto a = __builtin_amdgcn_permlane16_swap(__float_as_uint(v), __float_as_uint(v), false, false);
  v = __uint_as_float(a[0]) + __uint_as_float(a[1]);
  auto b = __builtin_amdgcn_permlane32_swap(__float_as_uint(v), __float_as_uint(v), false, false);
  return __uint_as_float(b[0]) + __uint_as_float(b[1]);
}

__device__ __forceinline__ float half_sum(float v) {
  auto b = __builtin_amdgcn_permlane32_swap(__float_as_uint(v), __float_as_uint(v), false, false);
  return __uint_as_float(b[0]) + __uint_as_float(b[1]);
}
__device__ __forceinline__ f32x2 ln_coef(float sm, float sq) {
  const float mean = sm * (1.0f / D_);
  const float var = fmaxf(__builtin_fmaf(-mean, mean, sq * (1.0f / D_)), 0.f);
  const float rs = __builtin_amdgcn_rcpf(rbf(__builtin_amdgcn_sqrtf(rbf(rbf(var) + 1e-5f))));
  return f32x2{rs, -rbf(mean) * rs};
}

constexpr u64 win_mask(int r, int lo, int hi) {
  u64 m = 0;
  for (int l = 0; l < 64; ++l) { const int d = 4 * (l >> 4) + r - (l & 15); if (d >= lo && d <= hi) m |= 1ull << l; }
  return m;
}
template <int R> __device__ __forceinline__ void win_bias(unsigned ninf, float& a, float& b) {
  constexpr u64 ma = ~win_mask(R, 9, 64), mb = ~win_mask(R, -7, 0);
  static_assert((unsigned)ma == ~0u, "mask");
  asm volatile("s_mov_b32 vcc_lo, %3\n\ts_mov_b32 vcc_hi, %4\n\tv_cndmask_b32 %1, 0, %2, vcc\n\t"
               "s_mov_b32 vcc_lo, -1\n\ts_mov_b32 vcc_hi, %5\n\tv_cndmask_b32 %0, 0, %2, vcc"
               : "=&v"(a), "=&v"(b) : "v"(ninf), "i"((int)mb), "i"((int)(mb >> 32)), "i"((int)(ma >> 32)) : "vcc");
}

template <class Pre>
__device__ __forceinline__ void k1_consumer(LAS char* lds, int lane, int wv, bool l0, int tq, const char* ob, Gate& gate, Pre&& pre) {
  u64 qm = l0 ? 0x0001000100010001ull : 0ull;
  asm volatile("" : "+v"(lane), "+s"(ob), "+s"(qm));
  const int p = wv & 1;
  f32x4 acc[6][4], hk, hv, hq;
  FragAddr<KM, 16, 3, false, 4> pa[2];
  pa[0].init(lds + L_A, wv, lane); pa[1].init(lds + L_A, wv ^ 1, lane);
  FragAddr<KC, 16, 4> qa, ha;
  qa.init(lds + K1_QOFF, 1 + 4 * p, lane);
#pragma unroll
  for (int t = 0; t < 2; ++t) { ha.a[t] = (qa.a[t] - (1 + 4 * p) * 2048) ^ ((1 - p) * 1024); asm volatile("" : "+v"(ha.a[t])); }
  struct Fr { bf16x8 p[6], q[4], h; };
  auto rd = [&](Fr& f, int slot, int t) {
    f.q[0] = qa.read(slot * K1_QSTG, 0, t);
#pragma unroll
    for (int i = 0; i < 6; ++i) f.p[i] = pa[i & 1].read(slot * K1_PSTG, i >> 1, t);
#pragma unroll
    for (int j = 1; j < 4; ++j) f.q[j] = qa.read(slot * K1_QSTG, j, t);
    f.h = ha.read(slot * K1_QSTG, 0, t);
  };
  auto m1 = [&](const Fr& f, int n) {
    const int j = n / 6, i = (j & 1) ? 5 - n % 6 : n % 6;
    acc[i][j] = i < 4 ? __builtin_amdgcn_mfma_f32_16x16x32_bf16(f.p[i], f.q[j], acc[i][j], 0, 0, 0) : __builtin_amdgcn_mfma_f32_16x16x32_bf16(f.q[j], f.p[i], acc[i][j], 0, 0, 0);
  };
  auto mh = [&](const Fr& f) {
    hk = __builtin_amdgcn_mfma_f32_16x16x32_bf16(f.p[2], f.h, hk, 0, 0, 0);
    hv = __builtin_amdgcn_mfma_f32_16x16x32_bf16(f.h, f.p[4], hv, 0, 0, 0);
  };
  auto mm = [&](const Fr& f) {
#pragma unroll
    for (int n = 0; n < HQP; ++n) m1(f, n);
    interleave<HQP, 1>();
    SCHED0;
    if (l0) hq = __builtin_amdgcn_mfma_f32_16x16x32_bf16(f.p[0], f.h, hq, 0, 0, 0);
    SCHED0;
#pragma unroll
    for (int n = HQP; n < 24; ++n) m1(f, n);
    mh(f);
    SCHED0;
  };
  const f32x4 z4 = {0.f, 0.f, 0.f, 0.f};
#pragma unroll
  for (int i = 0; i < 6; ++i)
#pragma unroll
    for (int j = 0; j < 4; ++j) { acc[i][j] = z4; asm volatile("" : "+v"(acc[i][j])); }
  hk = z4; hv = z4; hq = z4;
  asm volatile("" : "+v"(hk), "+v"(hv), "+v"(hq));
  Fr f0, f1;
  gate.pass();
  pre();
  BARRIER;
  rd(f0, 0, 0);
  SCHED0;
  gate.done(); WAITL0; SCHED0;
#pragma unroll 1
  for (int g = 0; g < 5; ++g)
    static_for<3>([&](auto uc) {
      constexpr int u = decltype(uc)::value;
      rd(f1, u, 1);
      mm(f0);
      if constexpr (u == 1) WAITV(0);
      WAITL0; BARRIER; SCHED0;
      rd(f0, (u + 1) % 3, 0);
      mm(f1);
      WAITL0;
      SCHED0;
    });
  constexpr int L_HX = L_B + 57344;
  asm volatile("" : "+v"(lane));
  const int fr = lane & 15, fq16 = lane & 48;
  unsigned dw = L_HX + wv * 2048 + (lane & 63) * 16, cq0 = L_CSQ + wv * 64 + fq16;
  f1.h = ha.read(0, 0, 1);
#pragma unroll
  for (int i = 0; i < 5; ++i) { const int k = (2 * i + 2) % 6 + (i >= 3); f1.p[k] = pa[k & 1].read(0, k >> 1, 1); }
  mh(f0);
#pragma unroll
  for (int n = 0; n < 9; ++n) m1(f0, n);
#pragma unroll
  for (int n = 0; n < 11; ++n) { __builtin_amdgcn_sched_group_barrier(0x8, 1, 0); __builtin_amdgcn_sched_group_barrier(0x100, 1, 0); __builtin_amdgcn_sched_group_barrier(0x2, 1, 0); }
  asm volatile("" : "+v"(dw), "+v"(cq0));
  SCHED0;
  mh(f1);
  if (l0) hq = __builtin_amdgcn_mfma_f32_16x16x32_bf16(f1.p[0], f1.h, __builtin_amdgcn_mfma_f32_16x16x32_bf16(f0.p[0], f0.h, hq, 0, 0, 0), 0, 0, 0);
  SCHED0;
  f1.p[1] = pa[1].read(0, 0, 1);
  f1.q[0] = qa.read(0, 0, 1);
#pragma unroll
  for (int n = 9; n < 12; ++n) m1(f0, n);
  interleave<3, 1>();
  SCHED0;
  *(LAS f32x4*)(size_t)dw = hk;
  m1(f0, 12); m1(f0, 13);
  SCHED0;
  *(LAS f32x4*)((LAS char*)(size_t)dw + 1024) = hv;
  m1(f0, 14); m1(f0, 15);
  SCHED0;
  asm volatile("s_mov_b64 exec, %2\n\tds_write_b128 %0, %1\n\ts_mov_b64 exec, -1" ::"v"(cq0), "v"(hq), "s"(qm) : "memory");
  SCHED0;
  unsigned a2 = K1_ST + (16 + 64 * p) * 8 + fr * 8, a4 = L_RS + (16 + 64 * p) * 4 + fq16;
  unsigned h2 = K1_ST + ((fr ^ ((1 - p) * 8)) * 8), h4 = L_RS + (fq16 ^ ((1 - p) * 32));
  unsigned ma = L_MSK + (lane & 63) * 16, ck0 = L_HX + wv * 2048 + fq16 * 16, cv0 = L_HX + 1024 + wv * 2048 + fr * 16;
#pragma unroll
  for (int j = 1; j < 4; ++j) f1.q[j] = qa.read(0, j, 1);
#pragma unroll
  for (int n = 16; n < 24; ++n) m1(f0, n);
#pragma unroll
  for (int n = 0; n < 8; ++n) { __builtin_amdgcn_sched_group_barrier(0x8, 1, 0); __builtin_amdgcn_sched_group_barrier(0x100, 1, 0); __builtin_amdgcn_sched_group_barrier(0x2, 2, 0); }
  asm volatile("" : "+v"(a2), "+v"(a4), "+v"(h2), "+v"(h4), "+v"(ma), "+v"(ck0), "+v"(cv0));
  SCHED0; WAITL0; BARRIER; SCHED0;
  unsigned oo[4];
  {
    const bool hole = ((p + (fr & 14)) & 7) == 3;
    oo[0] = phys<64>(p + (fr & 14)) * 128 + (fr & 1) * 64 + (((lane & 16) * 2) ^ (p * 32)) + ((lane & 32) >> 1);
    const unsigned e1 = hole ? 8 : 11, e2 = hole ? 10 : 12;
    oo[1] = oo[0] + (1u << e1); oo[2] = oo[0] + (1u << e2); oo[3] = oo[2] + (1u << e1);
  }
  f32x2 s2[5];
  f32x4 rs[5], mr[5], cs[4], pk_, pv_, bA, bB, b0;
  float cv[2];
  bA = *(const LAS f32x4*)(size_t)ma;
  cs[0] = *(const LAS f32x4*)(size_t)cq0; cs[1] = *(const LAS f32x4*)(size_t)(cq0 ^ 64);
  cs[2] = *(const LAS f32x4*)(size_t)ck0; cs[3] = *(const LAS f32x4*)(size_t)(ck0 ^ 2048);
#pragma unroll
  for (int j = 0; j < 5; ++j) s2[j] = j < 4 ? *(const LAS f32x2*)((LAS char*)(size_t)a2 + j * 128) : *(const LAS f32x2*)(size_t)h2;
  pk_ = *(const LAS f32x4*)(size_t)(dw ^ (2048 + 128)); pv_ = *(const LAS f32x4*)(size_t)(dw ^ (2048 + 1024 + 512));
  bB = *(const LAS f32x4*)((LAS char*)(size_t)ma + 1024);
  cv[0] = *(const LAS float*)(size_t)cv0; cv[1] = *(const LAS float*)(size_t)(cv0 ^ 2048);
#pragma unroll
  for (int j = 0; j < 5; ++j) {
    rs[j] = j < 4 ? *(const LAS f32x4*)((LAS char*)(size_t)a4 + j * 64) : *(const LAS f32x4*)(size_t)h4;
    mr[j] = j < 4 ? *(const LAS f32x4*)((LAS char*)(size_t)a4 + j * 64 + 576) : *(const LAS f32x4*)((LAS char*)(size_t)h4 + 576);
  }
  const unsigned nm = (unsigned)((tq - 1) >> 31) & 0xFF61B1E6u;
  int o1 = OPQ(0x3F803F80);
  bf16x8 ones = __builtin_bit_cast(bf16x8, u32x4{(unsigned)o1, (unsigned)o1, (unsigned)o1, (unsigned)o1});
#pragma unroll
  for (int n = 0; n < 24; ++n) m1(f1, n);
#pragma unroll
  for (int n = 0; n < 24; ++n) { __builtin_amdgcn_sched_group_barrier(0x8, 1, 0); __builtin_amdgcn_sched_group_barrier(0x2, 1, 0); __builtin_amdgcn_sched_group_barrier(0x100, 1, 0); __builtin_amdgcn_sched_group_barrier(0x2, 1, 0); }
  asm volatile("" : "+v"(oo[0]), "+v"(oo[1]), "+v"(oo[2]), "+v"(oo[3]), "+v"(ones));
  SCHED0;
  __builtin_amdgcn_s_waitcnt(0xCA7F);
  ALIGN8;
  auto qk = [&](const f32x4& a, const f32x4& c, const f32x2& s) { const f32x4 v = a * s[0] + c * s[1]; return u32x2{pk2(v[0], v[1]), pk2(v[2], v[3])}; };
  auto cat = [](const u32x2& a, const u32x2& b) { return __builtin_bit_cast(bf16x8, u32x4{a[0], a[1], b[0], b[1]}); };
  bf16x8 kf[5], qf[4];
#pragma unroll
  for (int j = 0; j < 4; ++j) {
    kf[j + 1] = cat(qk(acc[2][j], cs[2], s2[j]), qk(acc[3][j], cs[3], s2[j]));
    qf[j] = cat(qk(acc[0][j], cs[0], s2[j]), qk(acc[1][j], cs[1], s2[j]));
  }
  kf[0] = cat(qk(hk, cs[2], s2[4]), qk(pk_, cs[3], s2[4]));
  SCHED0;
  const float scale = 0.17677669529663687f, l2e = 1.4426950408889634f;
  f32x2 l[4][4];
  float mx[4];
#pragma unroll
  for (int r = 0; r < 4; ++r) b0[r] = __uint_as_float(__float_as_uint(bA[r]) | nm);
#pragma unroll
  for (int jj = 0; jj < 4; ++jj) {
    const int j = (jj + 1) & 3;
    const f32x4 sA = __builtin_amdgcn_mfma_f32_16x16x32_bf16(kf[j], qf[j], j == 0 ? b0 : bA, 0, 0, 0);
    const f32x4 sB = __builtin_amdgcn_mfma_f32_16x16x32_bf16(kf[j + 1], qf[j], bB, 0, 0, 0);
    l[j][0] = f32x2{sA[0], sA[1]}; l[j][1] = f32x2{sA[2], sA[3]}; l[j][2] = f32x2{sB[0], sB[1]}; l[j][3] = f32x2{sB[2], sB[3]};
    mx[j] = max3(max3(l[j][0][0], l[j][0][1], l[j][1][0]), max3(l[j][1][1], l[j][2][0], l[j][2][1]), max2(l[j][3][0], l[j][3][1]));
  }
  __builtin_amdgcn_sched_group_barrier(0x8, 2, 0);
#pragma unroll
  for (int n = 0; n < 6; ++n) { __builtin_amdgcn_sched_group_barrier(0x2, 2, 0); __builtin_amdgcn_sched_group_barrier(0x8, 1, 0); }
  SCHED0;
  WAITL0;
  ALIGN8;
  u32x12 vp[2];
  auto vv = [&](int e, int k, const f32x4& a, int j) { const f32x4 v = a * rs[j] + mr[j] * cv[e]; vp[e][2 * k] = pk2(v[0], v[1]); vp[e][2 * k + 1] = pk2(v[2], v[3]); };
  asm volatile("" : "=v"(vp[0]), "=v"(vp[1]));
  vv(0, 0, hv, 4); vv(1, 0, pv_, 4);
#pragma unroll
  for (int j = 0; j < 4; ++j) { vv(0, j + 1, acc[4][j], j); vv(1, j + 1, acc[5][j], j); }
  {
    auto mx2 = [](auto r) { return __float_as_uint(max2(__uint_as_float(r[0]), __uint_as_float(r[1]))); };
    const unsigned a = mx2(__builtin_amdgcn_permlane16_swap(__float_as_uint(mx[0]), __float_as_uint(mx[1]), false, false));
    const unsigned b = mx2(__builtin_amdgcn_permlane16_swap(__float_as_uint(mx[2]), __float_as_uint(mx[3]), false, false));
    const unsigned c = __float_as_uint(__uint_as_float(mx2(__builtin_amdgcn_permlane32_swap(a, b, false, false))) * (-scale * l2e));
    const auto d = __builtin_amdgcn_permlane32_swap(c, c, false, false);
    const auto e0 = __builtin_amdgcn_permlane16_swap(d[0], d[0], false, false), e1 = __builtin_amdgcn_permlane16_swap(d[1], d[1], false, false);
    mx[0] = __uint_as_float(e0[0]); mx[1] = __uint_as_float(e0[1]); mx[2] = __uint_as_float(e1[0]); mx[3] = __uint_as_float(e1[1]);
  }
#pragma unroll
  for (int j = 0; j < 4; ++j)
#pragma unroll
    for (int e = 0; e < 4; ++e) l[j][e] = __builtin_elementwise_fma(l[j][e], f32x2{scale * l2e, scale * l2e}, f32x2{mx[j], mx[j]});
  asm volatile("" : "+v"(vp[0]), "+v"(vp[1]));
  SCHED0;
  f32x4 oA[4], oB[4], zs[4];
  bf16x8 pf[4];
  auto ex = [&](int j) {
    u32x4 pp;
#pragma unroll
    for (int e = 0; e < 4; ++e) pp[e] = pk2(__builtin_amdgcn_exp2f(l[j][e][0]), __builtin_amdgcn_exp2f(l[j][e][1]));
    pf[j] = __builtin_bit_cast(bf16x8, pp);
  };
  auto pv = [&](auto jc) {
    constexpr int j = decltype(jc)::value;
    zs[j] = __builtin_amdgcn_mfma_f32_16x16x32_bf16(ones, pf[j], z4, 0, 0, 0);
    oA[j] = __builtin_amdgcn_mfma_f32_16x16x32_bf16(__builtin_bit_cast(bf16x8, __builtin_shufflevector(vp[0], vp[0], 2 * j, 2 * j + 1, 2 * j + 2, 2 * j + 3)), pf[j], z4, 0, 0, 0);
    oB[j] = __builtin_amdgcn_mfma_f32_16x16x32_bf16(__builtin_bit_cast(bf16x8, __builtin_shufflevector(vp[1], vp[1], 2 * j, 2 * j + 1, 2 * j + 2, 2 * j + 3)), pf[j], z4, 0, 0, 0);
  };
  auto out = [&](int j) {
    const float iz = __builtin_amdgcn_rcpf(zs[j][0]);
    asm volatile("" ::"v"(zs[j]));
    const f32x4 a = oA[j] * iz, b = oB[j] * iz;
    const auto x0 = __builtin_amdgcn_permlane16_swap(pk2(a[0], a[1]), pk2(b[0], b[1]), false, false);
    const auto x1 = __builtin_amdgcn_permlane16_swap(pk2(a[2], a[3]), pk2(b[2], b[3]), false, false);
    const u32x4 ov = {x0[0], x1[0], x0[1], x1[1]};
    asm volatile("global_store_dwordx4 %0, %1, %2 sc1\n\ts_nop 1" ::"v"(oo[j]), "v"(ov), "s"(ob) : "memory");
    SCHED0;
  };
  ex(0);
  SCHED0;
  static_for<3>([&](auto jc) {
    constexpr int j = decltype(jc)::value;
    pv(jc);
    ex(j + 1);
#pragma unroll
    for (int n = 0; n < 3; ++n) { __builtin_amdgcn_sched_group_barrier(0x8, 1, 0); __builtin_amdgcn_sched_group_barrier(0x400, 1, 0); }
    SCHED0;
    if constexpr (j == 2) { pv(std::integral_constant<int, 3>{}); SCHED0; }
    out(j);
  });
  out(3);
}

constexpr int L_FLG = L_A + 65536;
template <int OFF> __device__ __forceinline__ void fetch_flags(const void* p) {
  asm volatile("s_mov_b32 m0, %1\n\ts_nop 0\n\tglobal_load_lds_dwordx4 %0, off sc1" ::"v"(p), "i"(L_FLG + OFF) : "memory", "m0");
}
__device__ __forceinline__ bool flags_set(unsigned a, u64 tag) {
  u32x4 v;
  asm volatile("ds_read_b128 %0, %1\n\ts_waitcnt lgkmcnt(0)" : "=v"(v) : "v"(a) : "memory");
  const unsigned lo = (unsigned)tag, hi = (unsigned)(tag >> 32);
  return __builtin_amdgcn_ballot_w64((((v[0] ^ lo) | (v[1] ^ hi)) | ((v[2] ^ lo) | (v[3] ^ hi))) != 0) == 0;
}
template <int N>
__device__ __forceinline__ void wait_tags(const u64* p, u64 tag) {
#pragma unroll 1
  for (int spin = 0; spin < (1 << 16); ++spin) {
    bool ok = true;
#pragma unroll
    for (int i = 0; i < N; ++i) ok &= __hip_atomic_load(p + i, __ATOMIC_RELAXED, __HIP_MEMORY_SCOPE_AGENT) == tag;
    if (__all(ok)) break;
    __builtin_amdgcn_s_sleep(8);
  }
  CFENCE;
}
__device__ __forceinline__ const u64* first_flags(const u64* ctl0, int lane) {
  OPAQUE_BID;
  return ctl0 + C_OCNT + ((lane >> 2) * NR_O + (BID & 3) % NR_O) * RS1 + (lane & 3) * 4;
}
__device__ __forceinline__ void wait_first_phase(LAS char* lds, const u64* ctl0, int lane) { wait_tags<4>(first_flags(ctl0, lane), TAG); }
__device__ __forceinline__ const u64* x_flag(const u64* ctl, int tq, int tp, int lane) {
  return ctl + C_XCNT + (max(tq - ((lane >> 4) & 1), 0) * NR_X + ((tp + ((lane >> 4) & 1) * (NR_X / 2)) % NR_X)) * RS1 + (lane & 15);
}

__device__ __forceinline__ f32x2 uadd(f32x2 a, f32x2 b) {
  float x, y;
  asm("v_add_f32 %0, %2, %4\n\tv_add_f32 %1, %3, %5" : "=&v"(x), "=v"(y) : "v"(a[0]), "v"(a[1]), "v"(b[0]), "v"(b[1]));
  return f32x2{x, y};
}
constexpr size_t MB = 1 << 20;
constexpr size_t W_H = 4 * MB, W_O = 10 * MB, W_U = 16 * MB, W_PART = 26 * MB;
constexpr size_t W_XCH = 30 * MB, W_CTL = 31 * MB, W_PST = 33 * MB, W_EPOCH = 34 * MB, W_CW = 35 * MB;

#define SF1 5
#define SCW 13
#define SCO (SCW < 12 ? SCW + 3 : 99)
template <bool ISQ>
struct K1Hook : NoHook {
  static constexpr int AUX = ISQ ? 16 : 0;
  Stream<KM, 16, 128, true> so;
  static constexpr bool PREALL = true, PIN = ISQ, UNROLL = true, COLD = true;
  __device__ __forceinline__ bool cold() const { return l0; }
  static constexpr int SG = SF1 + 3;
  static constexpr int NG = ISQ ? 4 : 1;
  bool l0; int tq, tp, lw, lane; LAS char* lds;
  const u32x2* pstat; u64* ctl; u32x4* xch;
  const u16* wo; const u16* w1; const u16* w2; const u16* wq;
  const u32x2* sp; unsigned sa; const char* tw;
  f32x2 g[4 * NG];
  u64 fx; const u64* fp;
  __device__ __forceinline__ void ready() {
    if constexpr (ISQ) { if (!l0) { BARRIER; BARRIER; } }
  }
  __device__ __forceinline__ void first() {
    asm volatile("" : "+v"(lane));
    if constexpr (ISQ) {
      OPAQUE_BID;
      const int b = BID, l24 = lane % 24, r = l24 / 12, c = l24 - r * 12;
      tw = (const char*)wq + (256 + (((b >> 2) & 1) * 32 + (b >> 3)) * 4 + (lw - 2) * 2 + r) * 6144 + (c >> 2) * 2048 + ((b & 3) * 4 + (c & 3)) * 128;
      asm volatile("" : "+v"(tw));
      asm volatile("global_load_dwordx2 %0, %1, %2 sc1" : "=&v"(fx) : "v"(vconst<0>()), "s"((const char*)pstat + (W_EPOCH - W_PST)) : "memory");
    }
  }
  static constexpr int ng(int s) { return (s >= SG && s < SG + 4) ? NG : 0; }
    static constexpr int nt(int s) { return ISQ && s == 0 ? 3 : 0; }
  static constexpr int gs(int s) { return (!ISQ && s >= G0 && (s - G0) % GD == 0 && (s - G0) / GD < GN) ? (s - G0) / GD : -1; }
  int grp, duty; unsigned go;
  static constexpr int lx(int s) { return nt(s) + (!ISQ && s == FWS ? 1 : 0) + (s == WC0 || s == WC0 + 3 ? 2 : 0); }
  template <int S, int N> __device__ __forceinline__ void wait() {
    constexpr int A = N + (S < 13 ? ng(S - 1) + ng(S - 2) : 0), X = lx(S - 1) + lx(S - 2);
    if constexpr (S < 13 && (gs(S - 1) >= 0 || gs(S - 2) >= 0)) {
      if (grp == gs(S - 1) || grp == gs(S - 2)) waitv<A + X + 4>();
      else if (X > 0 && l0) waitv<A + X>();
      else waitv<A>();
    } else if constexpr (S == SCW + 1 || S == SCW + 2) {
      static_assert(X == 0, "copy step");
      if (duty >= 0) waitv<A + 4>(); else waitv<A>();
    } else {
      if (X > 0 && l0) waitv<A + X>(); else waitv<A>();
    }
  }
  template <int I> __device__ __forceinline__ f32x2 sum4() {
    asm volatile("" : "+v"(g[I]), "+v"(g[I + 1]), "+v"(g[I + 2]), "+v"(g[I + 3]));
    return uadd(uadd(g[I], g[I + 1]), uadd(g[I + 2], g[I + 3]));
  }
  template <int S> __device__ __forceinline__ void stepc() {
    if constexpr (ISQ && S == SF1) {
      fp = x_flag(ctl, tq, tp, lane & 31);
      asm volatile("global_load_dwordx2 %0, %1, off sc1" : "=&v"(fx) : "v"(fp) : "memory");
    }
    if constexpr (nt(S) > 0) {
      if (l0) {
#pragma unroll
        for (int i = 0; i < 3; ++i) touch_line(tw + i * 256 * 6144, (unsigned)(size_t)lds + L_SCR);
      }
    }
    if constexpr (!ISQ && S == 1) {
      OPAQUE_BID;
      int kh2, tq2, tp2;
      k4_map(BID, kh2, tp2, tq2);
      so.init(Operand{cw + 448 * CW_BLK / 2, D_, tp2 * 128, 64, 256 * CW_BLK, 0, false, nullptr, 0}, lw, lane);
      so.pin();
    }
    if constexpr (S == WC0) {
      if (l0) {
        const auto r = __builtin_amdgcn_make_buffer_rsrc((void*)wo, 0, D_ * D_ * 2, 0x00020000);
        const unsigned v = (lane >> 3) * (D_ * 2) + (lane & 7) * 16;
        const int o = ((tq * 64 + lw * 16) * D_ + tp * 64) * 2;
#if defined(__HIP_DEVICE_COMPILE__)
        __builtin_amdgcn_raw_ptr_buffer_load_lds(r, (LAS void*)(lds + L_C + lw * 4096), 16, v, o, 0, AC);
        __builtin_amdgcn_raw_ptr_buffer_load_lds(r, (LAS void*)(lds + L_C + lw * 4096 + 1024), 16, v, o + 8 * D_ * 2, 0, AC);
#endif
      }
    }
    if constexpr (S == WC0 + 3) {
      if (l0) {
        const unsigned d = phys<64>(lw * 16 + (lane >> 3)) * 128 + (lane & 7) * 16, d2 = d + pow2sel((lane >> 3) == 3, 7, 10);
        u32x4 a, b;
        asm volatile("ds_read_b128 %0, %2\n\tds_read_b128 %1, %2 offset:1024\n\ts_waitcnt lgkmcnt(0)" : "=&v"(a), "=&v"(b) : "v"(L_C + lw * 4096 + lane * 16) : "memory");
        asm volatile("s_nop 4\n\tglobal_store_dwordx4 %0, %2, %4 sc1\n\tglobal_store_dwordx4 %1, %3, %4 sc1\n\ts_nop 1"
                     ::"v"(d), "v"(d2), "v"(a), "v"(b), "s"((char*)cw + (448 + tp * 16 + tq) * CW_BLK) : "memory");
      }
    }
    if constexpr (ISQ && S == WC0 + 6) { if (l0 && lw == 2 && lane < NR_X) flag_set(ctl + C_XCNT + (tq * NR_X + lane) * RS1 + 16 + tp, tag_ld()); }
    if constexpr (!ISQ && S == 10) {
      OPAQUE_BID;
      int kh2, tq2, tp2;
      k4_map(BID, kh2, tp2, tq2);
      fp = ctl + C_XCNT + ((lane & 15) * NR_X + (BID & 7)) * RS1 + 16 + tp2 * 2 + ((lane >> 4) & 1);
      asm volatile("global_load_dwordx2 %0, %1, off sc1" : "=&v"(fx) : "v"(fp) : "memory");
    }
    if constexpr (!ISQ && S == 12) {
      asm volatile("" : "+v"(fx));
      if (l0 && lw == 1) { const u64 t = tag_ld(); if (!__all(fx == t)) wait_tags<1>(fp, t); }
    }
    if constexpr (S == (ISQ ? 4 : 2)) {
      asm volatile("" : "+v"(lane));
      const int trow = ISQ ? (lw - 2) * 64 + lane : 128 + (lane & 15);
      const int row = max(tq * 128 - 16 + trow, 0), line = (trow >> 1) + (trow < 16 ? (trow & 1) * 8 : 8 + (trow & 1) * 64);
      sp = pstat + (size_t)(row >> 5) * 512 + (row & 31) + (ISQ ? 0 : (lane >> 4) * 128);
      sa = (unsigned)(size_t)lds + K1_ST + line * 8;
      asm volatile("" : "+v"(sp), "+v"(sa));
    }
    if constexpr (!ISQ && S == ARMS) {
      if (lw == 0) { const unsigned m = vconst<~0u>(); st16(xch + ((tq * 4 + (lane >> 4)) * 16 + tp) * 16 + (lane & 15), u32x4{m, m, m, m}); }
    }
    if constexpr (!ISQ && S == FWS) {
      if (l0) {
        static_assert(NR_O == 8 && NR_H == 8 && NR_U == 8 && NR_X == 8 && (RS1 == RS2 || RS2 == 2 * RS1), "flag lines");
        const int i = opq_bid() * 8 + min(lane, 7);
        constexpr int N1 = 1024 + (C_END - C_UCNT) / RS1;
        const int wd = i < 1024 ? i * RSP : i < N1 ? C_UCNT + (i - 1024) * RS1 : i < N1 + 64 ? C_HCNT + 16 + (i - N1) * RS2 : C_XCNT + 16 + min(i - N1 - 64, 127) * RS1;
        touch_line(ctl + lw * C_WORDS + wd, L_SCR);
      }
    }
    if constexpr (S == 0) {
      const int c = (3 - (int)((size_t)wq >> 7)) & 7;
      duty = l0 && (tp & 7) != c ? tq * 14 + tp - (tp > c) - (tp > c + 8) : -1;
      asm volatile("" : "+s"(duty));
    }
    if constexpr (!ISQ && S == 0) {
      grp = duty >= 0 ? (int)((opq_bid() >> 3) % GN) : 99;
      const unsigned line = (duty * 2 + lw) * 64 + lane;
      go = ((((unsigned)((size_t)w1 >> 7) + line) & 7) == 3 ? line ^ 1 : line) * 128;
      asm volatile("" : "+v"(go));
    }
    if constexpr (gs(S) >= 0) {
      if (grp == gs(S)) {
        const char* b = (const char*)w1;
        asm volatile("s_mov_b32 m0, %5\n\ts_nop 0\n\tglobal_load_lds_dword %0, %1\n\tglobal_load_lds_dword %0, %2\n\tglobal_load_lds_dword %0, %3\n\tglobal_load_lds_dword %0, %4"
                     ::"v"(go), "s"(b), "s"(b + 28672 * 128), "s"(b + 2 * 28672 * 128), "s"(b + 3 * 28672 * 128), "i"(L_SCR) : "memory", "m0");
      }
    }
    if constexpr (ISQ && S == 1) {
      asm volatile("" : "+v"(fx));
      if (l0 && lw == 2) {
        const u64 tag = fx + 1;
        if (lane < NR_X) flag_set(ctl + C_XCNT + (tq * NR_X + lane) * RS1 + tp, tag);
        asm volatile("ds_write_b64 %0, %1\n\ts_waitcnt lgkmcnt(0)" ::"v"(vconst<L_TAG>()), "v"(tag) : "memory");
      }
    }
    if constexpr (S == SCO) copy_out();
    if constexpr (S == SCW) {
      if (duty >= 0) {
        const int q = (duty * 4 + lw) * 2, r8 = lane >> 3, c16 = (lane & 7) * 16;
        const auto r1 = __builtin_amdgcn_make_buffer_rsrc((void*)w1, 0, S_ * F_ * 2, 0x00020000);
        const auto r2 = __builtin_amdgcn_make_buffer_rsrc((void*)w2, 0, S_ * F_ * 2, 0x00020000);
        const int s1 = ((q & 255) * 8 * F_ + (((3 - (int)((size_t)w1 >> 7)) & 7) + 8 * (q >> 8)) * 64) * 2;
        const int s2 = ((q >> 2) * 8 * S_ + (((3 - (int)((size_t)w2 >> 7)) & 7) + 8 * (q & 3)) * 64) * 2;
        const unsigned v1 = r8 * (F_ * 2) + c16, v2 = r8 * (S_ * 2) + c16;
        LAS char* d = lds + L_C + lw * 4096;
#if defined(__HIP_DEVICE_COMPILE__)
        __builtin_amdgcn_raw_ptr_buffer_load_lds(r1, (LAS void*)d, 16, v1, s1, 0, AC);
        __builtin_amdgcn_raw_ptr_buffer_load_lds(r1, (LAS void*)(d + 1024), 16, v1, s1 + 8 * F_ * 2, 0, AC);
        __builtin_amdgcn_raw_ptr_buffer_load_lds(r2, (LAS void*)(d + 2048), 16, v2, s2, 0, AC);
        __builtin_amdgcn_raw_ptr_buffer_load_lds(r2, (LAS void*)(d + 3072), 16, v2, s2 + 1024, 0, AC);
#endif
      }
    }
    if constexpr (ISQ && S == SF1 + 2) {
      asm volatile("" : "+v"(fx));
      if (lw == 3) { const u64 t = tag_ld(); if (!__all(fx == t)) wait_tags<1>(fp, t); }
    }
    if constexpr (ng(S) == 4) {
      constexpr int i = (S - SG) * 4;
      asm volatile(
          "global_load_dwordx2 %0, %4, off offset:%5 sc1\n\tglobal_load_dwordx2 %1, %4, off offset:%5+256 sc1\n\t"
          "global_load_dwordx2 %2, %4, off offset:%5+512 sc1\n\tglobal_load_dwordx2 %3, %4, off offset:%5+768 sc1"
          : "=&v"(g[i]), "=&v"(g[i + 1]), "=&v"(g[i + 2]), "=&v"(g[i + 3]) : "v"(sp), "i"(i * 256) : "memory");
    }
    if constexpr (ng(S) == 1) asm volatile("global_load_dwordx2 %0, %1, off offset:%2 sc1" : "=&v"(g[S - SG]) : "v"(sp), "i"((S - SG) * 256) : "memory");
    if constexpr (ISQ) {
      if constexpr (S == SG + 3) { g[0] = sum4<0>(); asm volatile("" : "+v"(g[0])); }
      if constexpr (S == SG + 4) { g[0] = uadd(g[0], sum4<4>()); asm volatile("" : "+v"(g[0])); }
      if constexpr (S == SG + 5) { g[0] = uadd(g[0], uadd(sum4<8>(), sum4<12>())); asm volatile("" : "+v"(g[0])); }
    } else {
      if constexpr (S == SG + 5) { g[0] = sum4<0>(); float a = rows_sum(g[0][0]); asm volatile("" : "+v"(a)); g[0] = f32x2{a, rows_sum(g[0][1])}; asm volatile("" : "+v"(g[0])); }
    }
    if constexpr (S == SG + 6) {
      asm volatile("" : "+v"(g[0]));
      const f32x2 c = ln_coef(g[0][0], g[0][1]);
      asm volatile("ds_write_b64 %0, %1\n\tds_write2_b32 %2, %3, %4 offset1:144\n\ts_waitcnt lgkmcnt(0)" ::"v"(sa), "v"(c), "v"((sa >> 1) + (L_RS - K1_ST / 2)), "v"(c[0]), "v"(c[1]) : "memory");
    }
  }
  template <int T> __device__ __forceinline__ void next() {
    stepc<13 + T>();
    SCHED0;
    if constexpr (!ISQ) {
#pragma unroll
      for (int i = 0; i < (T == 3 ? 4 : 0); ++i) so.template stage<16>(lds + L_A + i * 16384 + lw * 1024);
      SCHED0;
    }
  }
  __device__ __forceinline__ void finish() {
    CFENCE;
    __builtin_amdgcn_s_waitcnt(0x0F70);
    CFENCE;
    if (SCO == 99) copy_out();
  }
  u16* cw;
  __device__ __forceinline__ void copy_out() {
    if (duty >= 0) {
      const int q = (OPQ(duty) * 4 + lw) * 2, r8 = lane >> 3, c16 = (lane & 7) * 16;
      const int k1 = (q & 7) * 8 + r8, k2 = ((q >> 3) & 7) * 8 + r8;
      const unsigned cd1 = (unsigned)((q >> 3) * CW_BLK + phys<64>(k1) * 128 + c16);
      const unsigned cd2 = (unsigned)((224 + (q & 3) * 56 + (q >> 6) * 2 + ((q >> 2) & 1)) * CW_BLK + phys<64>(k2) * 128 + c16);
      const unsigned d1 = pow2sel(r8 == 3, 7, 10);
      u32x4 cv[4];
      asm volatile("ds_read_b128 %0, %4\n\tds_read_b128 %1, %4 offset:1024\n\tds_read_b128 %2, %4 offset:2048\n\tds_read_b128 %3, %4 offset:3072\n\ts_waitcnt lgkmcnt(0)"
                   : "=&v"(cv[0]), "=&v"(cv[1]), "=&v"(cv[2]), "=&v"(cv[3]) : "v"(L_C + lw * 4096 + lane * 16) : "memory");
      asm volatile("s_nop 4\n\tglobal_store_dwordx4 %0, %4, %8 sc1\n\tglobal_store_dwordx4 %1, %5, %8 sc1\n\tglobal_store_dwordx4 %2, %6, %8 sc1\n\tglobal_store_dwordx4 %3, %7, %8 sc1\n\ts_nop 1"
                   ::"v"(cd1), "v"(cd1 + d1), "v"(cd2), "v"(cd2 + 56 * CW_BLK), "v"(cv[0]), "v"(cv[1]), "v"(cv[2]), "v"(cv[3]), "s"(cw) : "memory");
    }
  }
};
static_assert(SF1 + 3 + 5 == 13 && SCW != SF1 && SCO != SF1 && SCW != 11 && SCO != 11, "the fetches end two steps before the tail; statistics in LDS before the last barrier");

constexpr int K2_NSQ = 4, K2_NSP = 4;
__device__ __forceinline__ void k1_body(LAS char* lds, int wid, bool l0, const u16* __restrict__ w, const u16* x, u16* o, u32x4* xch, u64* ctl,
                                        u32x2* pstat, const u16* __restrict__ wo, const u16* __restrict__ w1,
                                        const u16* __restrict__ w2, u16* __restrict__ cw) {
  LAS char* pbuf = lds + L_A;
  LAS char* qbuf = lds + K1_QOFF;
  OPAQUE_BID;
  const int lane = lane_id();
  int tq, tp;
  tile_map<16, 16, 8, 4>(BID, tq, tp);
  const int s0 = tq * 128;
  if (wid >= 4) {
    auto fill = [&](auto& h) {
      h.l0 = l0; h.tq = tq; h.tp = tp; h.lw = wid - 4; h.lane = lane; h.lds = lds; h.pstat = pstat; h.ctl = ctl; h.xch = xch;
      h.wo = wo; h.w1 = w1; h.w2 = w2; h.wq = w; h.cw = cw;
    };
    if (wid >= 6) {
      if (lane < 8 * K1_NS) {
        const unsigned o1 = vconst<0x3F803F80u>();
        *(LAS u32x4*)(qbuf + (wid - 6) * 1024 + (lane >> 3) * K1_QSTG + (lane & 7) * 16) = u32x4{o1, o1, o1, o1};
      }
      WAITL0;
      K1Hook<true> hk;
      fill(hk);
      Operand Q{x, D_, s0 - 16, 64, S_ * D_ * 2, 0, false, nullptr, 0};
      g_loader<KC, 16, 144, K1_NS, 16, -1, true>(qbuf, Q, wid - 6, lane, hk);
      hk.finish();
    } else {
      Operand P{w, 3 * D_, tp * 64, D_, 3 * D_ * D_ * 2, 0, false, nullptr, 0};
      SlowColHook<K1Hook<false>, 3, 2 * D_, 4, 32, 16> hs;
      fill(hs);
      hs.setup(w, 3 * D_, (tp & ~3) * 128, 0, BID >> 3, lane, wid == 4, (unsigned)(size_t)lds + L_SCR);
      g_loader<KM, 16, 192, K1_NS, 16, 0, false>(pbuf, P, wid - 4, lane, hs, !l0 && ((((int)((size_t)w >> 7)) + tp) & 7) != 3);
      hs.template next<3>();
      hs.finish();
    }
  } else {
    Gate gate;
    {
      int kh, tp4, tq4;
      k4_map(BID, kh, tp4, tq4);
      gate.prep(l0 ? 2 : wid, ctl + C_XCNT + (tq4 * NR_X + lane) * RS1 + tp4 * 2 + kh, (1 << NR_X) - 1, x_flag(ctl, tq, tp, lane), 0xFFFFFFFFull, l0 ? 0ull : 5ull);
      gate.m1 = 0x00030003ull;
      gate.seam();
    }
    k1_consumer(lds, lane, wid, l0, tq, (const char*)o + (size_t)(tq * 16 + tp) * TB + (wid >> 1) * (TB / 2), gate, [&] {
      if (l0) {
        int ln = lane;
        asm volatile("" : "+v"(ln));
        const int fr = ln & 15, fq = (ln & 63) >> 4;
        if (wid == 3) {
          f32x4 ta, tb;
          const unsigned ninf = vconst<0xFF61B1E6u>();
          static_for<4>([&](auto rc) { float a, b; win_bias<decltype(rc)::value>(ninf, a, b); ta[decltype(rc)::value] = a; tb[decltype(rc)::value] = b; });
          asm volatile("ds_write_b128 %2, %0\n\tds_write_b128 %2, %1 offset:1024" ::"v"(ta), "v"(tb), "v"(ln * 16 + L_MSK) : "memory");
        }
        const unsigned o1 = vconst<0x3F803F80u>();
        const bf16x8 ones = __builtin_bit_cast(bf16x8, u32x4{o1, o1, o1, o1});
        if (wid & 1) WAITV(2); else WAITV(1);
        bf16x8 f[2][2];
#pragma unroll
        for (int k = 0; k < 4; ++k) f[k >> 1][k & 1] = *(const LAS bf16x8*)(lds + L_C + wid * 4096 + k * 1024 + ln * 16);
        const f32x4 z4 = {0.f, 0.f, 0.f, 0.f};
        u64 ps[2];
#pragma unroll
        for (int j = 0; j < 2; ++j) {
          const f32x4 gm = __builtin_amdgcn_mfma_f32_16x16x32_bf16(f[j][1], f[j][1], __builtin_amdgcn_mfma_f32_16x16x32_bf16(f[j][0], f[j][0], z4, 0, 0, 0), 0, 0, 0);
          const f32x4 sm = __builtin_amdgcn_mfma_f32_16x16x32_bf16(ones, f[j][1], __builtin_amdgcn_mfma_f32_16x16x32_bf16(ones, f[j][0], z4, 0, 0, 0), 0, 0, 0);
          const float da = (fr & 1) ? gm[1] : gm[0], db = (fr & 1) ? gm[3] : gm[2], dg = (fr & 2) ? db : da;
          ps[j] = ((u64)__float_as_uint(dg) << 32) | __float_as_uint(sm[0]);
        }
        if (fq == (fr >> 2)) {
#pragma unroll
          for (int j = 0; j < 2; ++j)
            __hip_atomic_store((u64*)pstat + (size_t)(tq * 4 + wid) * 512 + tp * 32 + j * 16 + fr, ps[j], __ATOMIC_RELAXED, __HIP_MEMORY_SCOPE_AGENT);
        }
        SCHED0;
      }
    });
  }
}

struct ResTile {
  LAS char* a;
  int sw;
  __device__ __forceinline__ void init(LAS char* rt, int lane) {
    a = rt + (lane & 15) * 128; sw = (lane & 15) >> 1;
  }
  __device__ __forceinline__ void load(u32x2 (&v)[8], int off) const {
#pragma unroll
    for (int t = 0; t < 8; ++t) v[t] = *(const LAS u32x2*)((LAS char*)(size_t)(wa[t & 3] - off) + (t >> 2) * 2048);
  }
  __device__ __forceinline__ void store(const u32x2 (&v)[8]) const {
#pragma unroll
    for (int t = 0; t < 8; ++t) *(LAS u32x2*)((LAS char*)(size_t)wa[t & 3] + (t >> 2) * 2048) = v[t];
  }
  unsigned wa[4], ra[8], go[4];
  __device__ __forceinline__ void prep_t(LAS char* rt, int d0, int s0, int lane) {
    const int g = lane >> 4, q4 = (lane & 15) >> 2, p4 = lane & 3, fr = lane & 15;
    const int rl = 8 * g + q4, rh = rl + 4;
    const unsigned al = (unsigned)(size_t)rt + rl * 128 + (p4 & 1) * 8;
#pragma unroll
    for (int t = 0; t < 4; ++t) wa[t] = (unsigned)(size_t)a + ((((t >> 1) * 4 + g) ^ sw) * 16) + ((t ^ (g >> 1)) & 1) * 8;
#pragma unroll
    for (int k = 0; k < 4; ++k) {
      const int ch = 2 * k + (p4 >> 1);
      ra[2 * k] = al + ((ch ^ ((rl >> 1) & 7)) * 16);
      ra[2 * k + 1] = al + 512 + ((ch ^ ((rh >> 1) & 7)) * 16);
    }
    {
      const unsigned s1 = pow2sel((fr >> 1) == 3, 7, 10);
      go[0] = phys<64>((d0 + fr) >> 1) * 128 + (fr & 1) * 64 + (s0 >> 5) * (TB / 2) + g * 16;
      go[1] = go[0] + s1; go[2] = go[1] + s1; go[3] = go[2] + s1;
    }
#pragma unroll
    for (int t = 0; t < 8; ++t) asm volatile("" : "+v"(wa[t & 3]), "+v"(ra[t]), "+v"(go[t & 3]));
  }
  __device__ __forceinline__ void load_t(f32x2 (&z)[16]) const {
#pragma unroll
    for (int k = 0; k < 8; ++k) {
      const u32x2 v = __builtin_bit_cast(u32x2, __builtin_amdgcn_ds_read_tr16_b64_v4i16((LAS s16x4*)(size_t)ra[k]));
      z[2 * k] = f32x2{lo2f(v[0]), hi2f(v[0])}; z[2 * k + 1] = f32x2{lo2f(v[1]), hi2f(v[1])};
    }
#pragma unroll
    for (int e = 0; e < 16; ++e) asm volatile("" : "+v"(z[e]));
  }
};

__device__ __forceinline__ u32x2 row_stats(const f32x2 (&z)[16]) {
  unsigned s1[2], q1[2];
#pragma unroll
  for (int j = 0; j < 2; ++j) {
    f32x2 sm[2], sq[2];
#pragma unroll
    for (int e = 0; e < 8; ++e) {
      const f32x2 v = z[8 * j + e];
      if (e < 2) { sm[e] = v; sq[e] = v * v; }
      else { sm[e & 1] += v; sq[e & 1] = __builtin_elementwise_fma(v, v, sq[e & 1]); }
    }
    const f32x2 s2 = sm[0] + sm[1], q2 = sq[0] + sq[1];
    s1[j] = __float_as_uint(s2[0] + s2[1]); q1[j] = __float_as_uint(q2[0] + q2[1]);
  }
  auto add2 = [](auto r) { return __float_as_uint(__uint_as_float(r[0]) + __uint_as_float(r[1])); };
  const unsigned sa = add2(__builtin_amdgcn_permlane16_swap(s1[0], s1[1], false, false)), qa = add2(__builtin_amdgcn_permlane16_swap(q1[0], q1[1], false, false));
  const unsigned t = add2(__builtin_amdgcn_permlane32_swap(sa, qa, false, false));
  const auto c = __builtin_amdgcn_permlane32_swap(t, t, false, false);
  return u32x2{c[0], c[1]};
}
#define XLD \
  "global_load_dwordx4 v[192:195], %[gx], off sc1\n\tglobal_load_dwordx4 v[196:199], %[gx], off offset:256 sc1\n\t" \
  "global_load_dwordx4 v[200:203], %[gx], off offset:512 sc1\n\tglobal_load_dwordx4 v[204:207], %[gx], off offset:768 sc1\n\t"
using u32x16 = __attribute__((ext_vector_type(16))) unsigned;
__device__ __forceinline__ void x_issue(u32x16& g, const char* gx) { asm volatile(XLD : "=&{v[192:207]}"(g) : [gx] "v"(gx) : "memory"); }
template <int SL>
__device__ __forceinline__ void x_gather(u32x16& g, const char* gx, f32x2& s0, f32x2& s1) {
  unsigned n;
  asm volatile("s_mov_b32 %[n], 0x8000\n\ts_waitcnt vmcnt(0)\n"
               "1:\n\tv_max3_u32 v208, v193, v195, v197\n\tv_max3_u32 v209, v199, v201, v203\n\tv_max3_u32 v208, v208, v205, v207\n\t"
               "v_pk_add_f32 v[192:193], v[192:193], v[196:197]\n\tv_max_u32 v208, v208, v209\n\tv_cmp_eq_u32 vcc, -1, v208\n\t"
               "v_pk_add_f32 v[194:195], v[194:195], v[198:199]\n\tv_pk_add_f32 v[200:201], v[200:201], v[204:205]\n\t"
               "v_pk_add_f32 v[202:203], v[202:203], v[206:207]\n\ts_cbranch_vccz 3f\n\t"
               "s_sleep %[sl]\n\t" XLD "s_waitcnt vmcnt(0)\n\t"
               "s_sub_u32 %[n], %[n], 1\n\ts_cbranch_scc0 1b\n"
               "3:\n\tv_pk_add_f32 %[s0], v[192:193], v[200:201]\n\tv_pk_add_f32 %[s1], v[194:195], v[202:203]\n\ts_nop 1"
               : "+{v[192:207]}"(g), [s0] "=&v"(s0), [s1] "=&v"(s1), [n] "=&s"(n)
               : [gx] "v"(gx), [sl] "i"(SL) : "memory", "vcc", "scc", "v208", "v209");
}

#define XSLEEP0 5
#define XSLEEP1 1
constexpr int K2_RES = L_C, K2_RES0 = L_B + 32768;
template <class Base>
struct K3NextHook : Base {
  static constexpr int NEXT = 8;
  Stream<KM, 16, 128> sn;
  bool on; int nev; const u16* w1; const u16* cw1; const u64* ctl; LAS char* lds; int gwn, lanen;
  __device__ __forceinline__ void afterr() {
    OPAQUE_BID;
    int tq, tp;
    tile_map<8, 28, 4, 7>(BID, tq, tp);
    sn.init(Operand{w1, F_, min(tp, 27) * 128, 64, S_ * F_ * 2, 0, false, cw1, 32}, gwn, lanen);
    sn.pin();
  }
  __device__ __forceinline__ void step(int s) {
    if (__builtin_expect(s == nev, 0)) {
      if (s == 6) { if (gwn == 0) fetch_flags<0>(first_flags(ctl, lanen)); else fetch_flags<1024>(first_flags(ctl, lanen) + 2); }
      else if (!flags_set(L_FLG + gwn * 1024 + lanen * 16, TAG)) wait_first_phase(lds, ctl, lanen);
      nev += 4;
      asm volatile("" : "+s"(nev));
    }
    Base::step(s);
  }
  template <int S, int N> __device__ __forceinline__ void wait() {
    if (on) waitv<N>(); else waitv<N - (S - 12) * NEXT>();
  }
  template <int T> __device__ __forceinline__ void next() { if (on) sn.template stage<A1>(lds + L_A + gwn * 1024 + T * 16384); }
};
__device__ __forceinline__ void k2_body(LAS char* lds, int wid, const u16* __restrict__ wo, const u16* o, const u16* res, u16* hout,
                                        unsigned long long* xch, u64* ctl, bool l0, const u16* w1, const u16* cw1) {
  LAS char* qbuf = lds + L_B;
  LAS char* pbuf = lds + L_A;
  OPAQUE_BID;
  const int lane = lane_id();
  int kh, tq, tp;
  k4_map(BID, kh, tp, tq);
  const int p0 = tp * 128, q0 = tq * 128 + kh * 64;
  if (wid >= 6) {
    Operand P{cw1 + 448 * CW_BLK / 2, D_, p0, 64, 256 * CW_BLK, 0, false, nullptr, 0};
    SyncHook<K3NextHook<ResHook>, 1> hk;
    hk.on = BID < 224; hk.nev = l0 ? 6 : 99; hk.w1 = w1; hk.cw1 = cw1; hk.ctl = ctl; hk.lds = lds; hk.gwn = wid - 6; hk.lanen = lane;
    static_cast<ResHook&>(hk) = ResHook{{}, l0 ? res : nullptr, q0, p0, lds + K2_RES0, wid - 6, lane};
    g_loader<KM, 16, 128, K2_NSP, 16, -1, false>(pbuf, P, wid - 6, lane, hk, true);
    __builtin_amdgcn_s_waitcnt(0x0F70);
    return;
  }
  if (wid >= 4) {
    Operand Q{o, 16, q0, 64, 16 * 16 * TB, 0, true, nullptr, 0};
    WaitHookX hk;
    g_loader<KC, 16, 64, K2_NSQ, 16, 1, false>(qbuf, Q, wid - 4, lane, hk);
    __builtin_amdgcn_s_waitcnt(0x0F70);
    return;
  }
  Gate gate;
  {
    int tq1, tp1;
    tile_map<16, 16, 8, 4>(BID, tq1, tp1);
    gate.prep(wid, ctl + C_OCNT + (tq1 * NR_O + lane) * RS1 + tp1, (1 << NR_O) - 1, ctl + C_OCNT + (tq * NR_O + (tp * 2 + kh) % NR_O) * RS1 + (lane & 15), 0xFFFFull, 1);
    gate.m1 = (1 << NP2) - 1;
    gate.seam();
  }
  f32x4 acc[4][2];
  const int wp = wid >> 1, wq = wid & 1, row0 = q0 + wq * 32;
  ResTile rt;
  rt.init(lds + K2_RES + wid * 4096, lane);
  u32x2 x[8];
  unsigned long long* xw = xch + (size_t)(row0 >> 5) * 512 + (tp * 2 + wp) * 32 + (lane & 15) * 2 + ((lane >> 4) & 1);
  const char* gx = (const char*)(xch + (size_t)(row0 >> 5) * 512) + (lane >> 4) * 1024 + (lane & 15) * 16;
  asm volatile("" : "+v"(xw), "+v"(gx));
  rt.prep_t(lds + K2_RES + wid * 4096, wp * 64, wq * 32, lane);
  const char* hb = (const char*)hout + (size_t)(tp * 32 + (q0 >> 6)) * TB;
  asm volatile("" : "+s"(hb));
  unsigned cwa = L_COF + wid * 256 + lane * 4, cra = L_COF + wid * 256 + (lane >> 4) * 32;
  asm volatile("" : "+v"(cwa), "+v"(cra));
  const bool hi = lane >= 32;
  consumer16w<KM, KC, 4, 2, K2_NSP, 128 * 128, 64 * 128, 16, false>(pbuf, qbuf, acc, wp * 4, wq * 2, lane, gate, [&] {
    rt.load(x, l0 ? K2_RES - K2_RES0 : 0);
#pragma unroll
    for (int t = 0; t < 8; ++t) acc[t & 3][t >> 2] = f32x4{lo2f(x[t][0]), hi2f(x[t][0]), lo2f(x[t][1]), hi2f(x[t][1])};
  }, [](auto&&) {}, [](int) {});
  f32x2 z[16];
#pragma unroll
  for (int e = 0; e < 16; ++e) z[e] = f32x2{acc[(e >> 1) & 3][e >> 3][2 * (e & 1)], acc[(e >> 1) & 3][e >> 3][2 * (e & 1) + 1]};
  ALIGN4;
  u32x2 ps = row_stats(z);
  ps[1] = min(ps[1], 0xFFFFFFFEu);
  asm volatile("s_mov_b32 exec_hi, 0\n\tglobal_store_dwordx2 %0, %1, off sc1\n\ts_mov_b32 exec_hi, -1" ::"v"(xw), "v"(ps) : "memory");
  SCHED0;
#pragma unroll
  for (int e = 0; e < 16; ++e) x[e >> 1][e & 1] = pk2(z[e][0], z[e][1]);
  rt.store(x);
  WAITL0;
  if (XSLEEP0) __builtin_amdgcn_s_sleep(XSLEEP0);
  f32x2 s0, s1;
  u32x16 xg;
  x_issue(xg, gx);
  rt.load_t(z);
  x_gather<XSLEEP1>(xg, gx, s0, s1);
  auto add2 = [](auto r) { return __uint_as_float(r[0]) + __uint_as_float(r[1]); };
  const float sm = half_sum(add2(__builtin_amdgcn_permlane16_swap(__float_as_uint(s0[0]), __float_as_uint(s1[0]), false, false)));
  const float sq = half_sum(add2(__builtin_amdgcn_permlane16_swap(__float_as_uint(s0[1]), __float_as_uint(s1[1]), false, false)));
  const float mean = sm * (1.0f / D_);
  const float var = fmaxf(__builtin_fmaf(-mean, mean, sq * (1.0f / D_)), 0.f);
  const float m1 = rbf(mean), i1 = __builtin_amdgcn_rcpf(rbf(__builtin_amdgcn_sqrtf(rbf(rbf(var) + 1e-5f))));
  const float m2 = rbf((mean - m1) * i1), i2 = __builtin_amdgcn_rcpf(rbf(__builtin_amdgcn_sqrtf(rbf(rbf(var * i1 * i1) + 1e-5f))));
  *(LAS float*)(size_t)cwa = (hi ? -(m1 * i1 + m2) : i1) * i2;
  CFENCE;
  f32x4 cf[4];
#pragma unroll
  for (int k = 0; k < 4; ++k) cf[k] = *(const LAS f32x4*)((LAS char*)(size_t)cra + (k & 1) * 16 + (k >> 1) * 128);
#pragma unroll
  for (int k = 0; k < 4; ++k) {
    u32x4 y;
#pragma unroll
    for (int e = 0; e < 4; ++e) {
      const f32x2 v = z[4 * k + e] * f32x2{cf[e >> 1][2 * (e & 1)], cf[e >> 1][2 * (e & 1) + 1]} + f32x2{cf[2 + (e >> 1)][2 * (e & 1)], cf[2 + (e >> 1)][2 * (e & 1) + 1]};
      y[e] = pk2(v[0], v[1]);
    }
    asm volatile("global_store_dwordx4 %0, %1, %2 sc1\n\ts_nop 1" ::"v"(rt.go[k]), "v"(y), "s"(hb) : "memory");
    SCHED0;
  }
}

constexpr int K4_NS = 4;
constexpr int K4_RES = L_C;
__device__ __forceinline__ void w2_init(Stream<KM, 16, 128>& st, const u16* w2, const u16* cw2, int gw, int lane) {
  OPAQUE_BID;
  int kh, tp, tq;
  k4_map(BID, kh, tp, tq);
  st.init(Operand{w2, S_, tq * 128 + kh * 64, 64, F_ * S_ * 2, kh * 28, false, cw2, 56}, gw, lane);
  st.twice();
  st.pin();
}
__device__ __forceinline__ void u_init(Stream<KC, 16, 128, true>& st, const u16* u, int gw, int lane) {
  OPAQUE_BID;
  int kh, tp, tq;
  k4_map(BID, kh, tp, tq);
  st.init(Operand{u, 56, tp * 128, 64, 8 * 56 * TB, kh * 28, true, nullptr, 0}, gw, lane);
  st.twice();
  st.pin();
}
template <class T> __device__ __forceinline__ T* OPV(T* p) { return (T*)(__attribute__((address_space(1))) T*)(size_t)p; }
struct UFlags {
  u64* set; const u64* get; u64 tag;
  __device__ __forceinline__ void init(LAS char* lds, u64* flags, int gw, int lane) {
    OPAQUE_BID;
    int kh, tp, tq, tq3, tp3;
    k4_map(BID, kh, tp, tq);
    tile_map<8, 28, 4, 7>(BID, tq3, tp3);
    u64* b = flags + (gw ? C_UCN2 : C_UCNT);
    set = b + ((tq3 * 2 + tp3 / 14) * NR_U + lane) * RS1 + tp3 % 14;
    get = b + ((tp * 2 + (gw ? (lane >> 3) & 1 : kh)) * NR_U + tq % NR_U) * RS1 + (gw ? min((lane & 7) * 2, 12) : min(lane, 13));
    tag = TAG;
    asm volatile("" : "+v"(set), "+v"(get), "+v"(tag));
  }
  __device__ __forceinline__ void publish() { asm volatile("s_mov_b64 exec, %2\n\tglobal_atomic_swap_x2 %0, %1, off sc1\n\ts_mov_b64 exec, -1" ::"v"(set), "v"(tag), "i"((1 << NR_U) - 1) : "memory"); }
};
struct K4NextHook : NoHook {
  static constexpr int AUX = A1;
  static constexpr int NEXT = 8;
  Stream<KM, 16, 128>* st; LAS char* lds; int gw;
  template <int T> __device__ __forceinline__ void next() { st->stage<A2>(lds + L_A + gw * 1024 + T * 16384); }
};
struct W2Base : NoHook { static constexpr int AUX = A2; };
struct UHook : WaitHookP { static constexpr bool EVOD = true; int kh; };
template <class Base>
struct W2Hook : Base {
  static constexpr bool PREALL = true, EVOD = true, OWNP = true;
  int kh;
  UFlags* uf; bool own; int gww;
  __device__ __forceinline__ void setup() { Base::first(); Base::afterr(); }
  __device__ __forceinline__ void first() {
    if (gww == 0) {
      u64 a, b, c, d, t, z = vz(), m1 = (1 << NP4) - 1;
      unsigned n;
      asm volatile("s_mov_b64 exec, 0x3FFF\n\ts_mov_b32 %[n], 0\n\t" POLL1(a) POLL1(b) POLL1(c) POLL0(d)
                   "1:\n\t" POLL3(a, 4) POLL3(b, 5) POLL3(c, 6) POLL3(d, 7) PEND
                   : [a] "=&v"(a), [b] "=&v"(b), [c] "=&v"(c), [d] "=&v"(d), [n] "=&s"(n), [t] "=&s"(t), [m1] "+s"(m1)
                   : [q] "v"(uf->get), [z] "v"(z), [tag] "v"(uf->tag), [sl] "i"(PSLP) : "memory", "vcc", "scc");
      asm volatile("s_barrier\n\ts_waitcnt vmcnt(0)" ::"v"(a), "v"(b), "v"(c), "v"(d) : "memory");
    } else { BARRIER; WAITV(0); BARRIER; }
  }
  int nev;
  __device__ __forceinline__ void step(int s) {
    if (__builtin_expect(s == nev, 0)) {
      if (s == 0) { if (own) uf->publish(); }
      else if (s == 4) fetch_flags<0>(uf->get);
      else if (!flags_set(L_FLG + Base::lanen * 16, uf->tag)) wait_tags<2>(OPV(uf->get), uf->tag);
      nev = s >= 8 ? 99 : s + 4;
      asm volatile("" : "+s"(nev));
    }
  }
};
template <class Base>
struct K1NextHook : Base {
  static constexpr int NEXT = 8;
  Stream<KM, 16, 192> sn;
  bool on; const u16* wq; LAS char* dstn; int gwn, lanen;
  __device__ __forceinline__ void afterr() {
    OPAQUE_BID;
    int tq, tp;
    tile_map<16, 16, 8, 4>(BID, tq, tp);
    sn.init(Operand{wq, 3 * D_, tp * 64, D_, 3 * D_ * D_ * 2, 0, false, nullptr, 0}, gwn, lanen);
#pragma unroll
    for (int i = 0; i < 4; ++i) asm volatile("" : "+v"(sn.voff[i]));
  }
  template <int U> __device__ __forceinline__ void unit() {
    const auto rsrc = __builtin_amdgcn_make_buffer_rsrc((void*)sn.gbase[0], 0, sn.gbytes[0], 0x00020000);
#pragma unroll
    for (int i = 0; i < 4; ++i) {
#if defined(__HIP_DEVICE_COMPILE__)
      __builtin_amdgcn_raw_ptr_buffer_load_lds(rsrc, (LAS void*)(dstn + U * 8192 + i * 2048), 16, sn.voff[i], sn.so[0] + (U / 3) * sn.inc[0] + (U % 3) * D_ * 2, 0, 0);
#endif
    }
  }
  __device__ __forceinline__ void last() { Base::last(); if (on) unit<8>(); }
  template <int S, int N> __device__ __forceinline__ void wait() {
    if (on) waitv<N + (S < 26 + KD ? 4 : 0)>(); else waitv<N - (S - 24 - KD) * NEXT>();
  }
  template <int T> __device__ __forceinline__ void next() { if (on) { unit<2 * ((KD + T) % 4)>(); unit<2 * ((KD + T) % 4) + 1>(); } }
};
__device__ __forceinline__ void k4_consumer(LAS char* lds, u16* out, u32x2* pstat, float* part, u64* flags, int wid) {
  LAS char* pbuf = lds + L_B;
  LAS char* qbuf = lds + L_A;
  OPAQUE_BID;
  const int lane = lane_id();
  int kh, tp, tq;
  k4_map(BID, kh, tp, tq);
  const int p0 = tp * 128, q0 = tq * 128;
  DrainGate gate;
  const int wp = wid >> 1, wq = wid & 1;
  f32x4 acc[4][4];
  const int row0 = q0 + kh * 64 + wq * 32;
  const size_t obase = (size_t)row0 * D_ + p0 + wp * 64;
#pragma unroll
  for (int j = 0; j < 4; ++j) {
    const float bq = j < 2 ? bf2f(*(const LAS u16*)(lds + L_BIA + 256 + (kh * 64 + wq * 32 + j * 16 + (lane & 15)) * 2)) : 0.f;
#pragma unroll
    for (int i = 0; i < 4; ++i) acc[i][j] = f32x4{bq, bq, bq, bq};
  }
  u32x4 x[2][2];
#define GLD "global_load_dwordx4 %[g0], %[go], %[gp] sc1\n\tglobal_load_dwordx4 %[g1], %[go], %[gp] offset:1024 sc1\n\t" \
            "global_load_dwordx4 %[g2], %[go], %[gp] offset:2048 sc1\n\tglobal_load_dwordx4 %[g3], %[go], %[gp] offset:3072 sc1"
  u64 ptag, a, a2, miss;
  u32x4 g[4];
  const char* gp = (const char*)part + (size_t)((BID ^ 1) * 4 + wid) * 4096;
  unsigned v0;
  const u64* myf = flags + (BID * 4 + wid) * RSP;
  const u64* paf = flags + ((BID ^ 1) * 4 + wid) * RSP;
  u64 zz;
  auto poll1 = [&](u64& r) {
    asm volatile("s_mov_b64 exec, 1\n\tglobal_atomic_or_x2 %0, %1, %2, %3 sc0 sc1\n\ts_mov_b64 exec, -1" : "=&v"(r) : "v"(v0), "v"(zz), "s"(paf) : "memory");
  };
  consumer16w<KC, KM, 4, 4, K4_NS, 128 * 128, 128 * 128, 28 - KD, true>(pbuf, qbuf, acc, wp * 4, wq * 2, lane, gate, [&] {
    const int fr = lane & 15, fq = lane >> 4;
#pragma unroll
    for (int t = 0; t < 4; ++t) x[t >> 1][t & 1] = *(const LAS u32x4*)(lds + K4_RES + wid * 4096 + (t >> 1) * 2048 + fr * 128 + ((((t & 1) * 4 + fq) ^ (fr >> 1)) * 16));
  }, [&](int t) {
    const auto rs = __builtin_amdgcn_make_buffer_rsrc((void*)part, 0, 256 * 16384, 0x00020000);
    const f32x4 a = acc[t][2], b = acc[t][3];
    __builtin_amdgcn_raw_buffer_store_b128(u32x4{pk2(a[0], a[1]), pk2(a[2], a[3]), pk2(b[0], b[1]), pk2(b[2], b[3])}, rs, lane * 16 + t * 1024, (BID * 4 + wid) * 4096, 16);
  }, [&](int k) {
    if (k == 0) asm volatile("s_nop 3\n\tv_mov_b32 %0, %3\n\tv_mov_b64 %2, 0\n\tds_read_b64 %1, %0" : "=&v"(v0), "=&v"(ptag), "=&v"(zz) : "i"(L_TAG) : "memory");
    if (k == FBK) {
      asm volatile("v_mov_b32 %0, 0\n\ts_waitcnt vmcnt(0)\n\ts_mov_b64 exec, 1\n\tglobal_atomic_swap_x2 %0, %1, %2 sc1\n\ts_mov_b64 exec, -1" : "+v"(v0), "+v"(ptag) : "s"(myf) : "memory");
    }
    if (k == CKK - 2) poll1(a);
    if (k == CKK - 1) poll1(a2);
    if (k == CKK)
      asm volatile("s_mov_b64 exec, 1\n\ts_waitcnt vmcnt(1)\n\tv_cmp_ne_u64 vcc, %[tag], %[a]\n\ts_mov_b64 exec, -1\n\ts_mov_b64 %[m], vcc\n\ts_cbranch_vccnz 1f\n\t" GLD "\n1:"
                   : [g0] "=&v"(g[0]), [g1] "=&v"(g[1]), [g2] "=&v"(g[2]), [g3] "=&v"(g[3]), [m] "=&s"(miss) : [tag] "v"(ptag), [a] "v"(a), [go] "v"(lane * 16), [gp] "s"(gp) : "memory", "vcc");
    if (k == CKK + 1)
      asm volatile("s_cmp_eq_u64 %[m], 0\n\ts_cbranch_scc1 1f\n\ts_mov_b64 exec, 1\n\ts_waitcnt vmcnt(0)\n\tv_cmp_ne_u64 vcc, %[tag], %[a]\n\ts_mov_b64 exec, -1\n\ts_mov_b64 %[m], vcc\n\ts_cbranch_vccnz 1f\n\t" GLD "\n1:"
                   : [g0] "+v"(g[0]), [g1] "+v"(g[1]), [g2] "+v"(g[2]), [g3] "+v"(g[3]), [m] "+s"(miss), [a] "+v"(a2) : [tag] "v"(ptag), [go] "v"(lane * 16), [gp] "s"(gp) : "memory", "vcc", "scc");
  });
  SCHED0;
  f32x2 z[16];
#pragma unroll
  for (int e = 0; e < 16; ++e) {
    const unsigned r = x[e >> 3][(e >> 2) & 1][e & 3];
    const f32x4 v = acc[(e >> 1) & 3][e >> 3];
    z[e] = f32x2{lo2f(r), hi2f(r)} + f32x2{v[2 * (e & 1)], v[2 * (e & 1) + 1]};
    asm volatile("" : "+v"(z[e]));
  }
  unsigned vo[4];
#pragma unroll
  for (int k = 0; k < 4; ++k) { vo[k] = (unsigned)(((lane & 7) + 8 * k) * D_ * 2 + (lane & 8) * 8 + (lane >> 4) * 16); asm volatile("" : "+v"(vo[k])); }
  const u16* ob = out + obase;
  asm volatile("" : "+s"(ob));
  {
    u64 c, d, sv;
    unsigned n;
    asm volatile("s_cmp_eq_u64 %[m], 0\n\ts_cbranch_scc1 9f\n\t" POLL_LOOP GLD "\n9:\n\ts_waitcnt vmcnt(0)"
        : [a] "=&v"(a), [b] "+v"(a2), [c] "=&v"(c), [d] "=&v"(d), [sv] "=&s"(sv), [n] "=&s"(n), [g0] "+v"(g[0]), [g1] "+v"(g[1]), [g2] "+v"(g[2]), [g3] "+v"(g[3])
        : [q] "v"(paf), [z] "v"(zz), [tag] "v"(ptag), [em] "s"(1ull), [sl] "i"(2), [m] "s"(miss), [go] "v"(lane * 16), [gp] "s"(gp) : "memory", "vcc", "scc");
  }
  {
#pragma unroll
    for (int e = 0; e < 16; ++e) {
      const unsigned r = g[(e >> 1) & 3][2 * (e >> 3) + (e & 1)];
      z[e] += f32x2{lo2f(r), hi2f(r)};
      x[e >> 3][(e >> 2) & 1][e & 3] = pk2(z[e][0], z[e][1]);
      if ((e & 7) == 7) {
        u32x4 n0, n1;
#pragma unroll
        for (int k = 0; k < 4; ++k) {
          n0[k] = __builtin_amdgcn_update_dpp(x[e >> 3][0][k], x[e >> 3][1][k], 0x128, 0xF, 0xC, false);
          n1[k] = __builtin_amdgcn_update_dpp(x[e >> 3][1][k], x[e >> 3][0][k], 0x128, 0xF, 0x3, false);
        }
        asm volatile("global_store_dwordx4 %0, %2, %4 sc1\n\tglobal_store_dwordx4 %1, %3, %4 sc1\n\ts_nop 1" ::"v"(vo[(e >> 3) * 2]), "v"(vo[(e >> 3) * 2 + 1]), "v"(n0), "v"(n1), "s"(ob) : "memory");
      }
    }
    {
      if (pstat != nullptr) {
        const u32x2 c = row_stats(z);
        if (lane < 32)
          __hip_atomic_store((unsigned long long*)pstat + (size_t)(row0 >> 5) * 512 + (tp * 2 + wp) * 32 + lane, ((unsigned long long)c[1] << 32) | c[0],
                             __ATOMIC_RELAXED, __HIP_MEMORY_SCOPE_AGENT);
        CFENCE;
#pragma unroll
        for (int t = 0; t < 4; ++t) *(LAS u32x4*)(lds + K4_RES + wid * 4096 + (t >> 1) * 2048 + (lane & 15) * 128 + ((((t & 1) * 4 + (lane >> 4)) ^ ((lane & 15) >> 1)) * 16)) = x[t >> 1][t & 1];
      }
    }
  }
}

constexpr int K3_NSP = 4, K3_NSQ = 4;
__device__ __forceinline__ u64* h_flag(u64* ctl, unsigned BID, int lane) {
  int kh, tq, tp;
  k4_map(BID, kh, tp, tq);
  return ctl + C_HCNT + (tp * NR_H + lane) * RS2 + tq * 2 + kh;
}
template <class T> __device__ __forceinline__ T* OPQ(T* p) {
  auto q = (__attribute__((address_space(1))) T*)p;
  asm volatile("" : "+s"(q));
  return (T*)q;
}
#define APOS 2
__device__ __forceinline__ void k34_body(LAS char* lds, int wid, bool l0, const u16* __restrict__ w1, const u16* h, u16* u, const u16* __restrict__ cw1, u64* flags,
                                         const u16* __restrict__ w2, u16* out, u32x2* pstat, float* part, const u16* __restrict__ wq_) {
  LAS char* pbuf = lds + L_A;
  LAS char* qbuf = lds + L_B;
  OPAQUE_BID;
  const int lane = lane_id();
  const bool own = BID < 224;
  if (!own) {
    WAITV(0);
    BARRIER;
    if (wid == 0 && lane < NR_H) flag_set(h_flag(flags, BID, lane), TAG);
    if (l0) {
      if (wid == 1) {
        u64 a, b, c, d, sv, z = vz(), tag = TAG;
        unsigned n;
        const u64* q = flags + C_HCNT + ((BID & 7) * NR_H + ((BID >> 3) & 3) * 2) * RS2 + (lane & 31);
        asm volatile(POLL_LOOP "s_waitcnt vmcnt(0)" : [a] "=&v"(a), [b] "=&v"(b), [c] "=&v"(c), [d] "=&v"(d), [n] "=&s"(n), [sv] "=&s"(sv)
                     : [q] "v"(q), [z] "v"(z), [tag] "v"(tag), [em] "s"(0xFFFFFFFFull), [sl] "i"(8) : "memory", "vcc", "scc");
      }
      BARRIER;
      for (int k = 0; k < WT0; ++k) __builtin_amdgcn_s_sleep(8);
      for (int k = 0; k < wid; ++k) __builtin_amdgcn_s_sleep(WSTG);
#pragma unroll 1
      for (int i = 0; i < 7; ++i) {
        const int g = 2 * i + (lane >> 5), par = g >= 7;
        touch_line(noslow(w2, (((BID >> 4) & 1) * 1792 + (g - 7 * par) * 256 + (BID & 15) * 16 + par * 8 + wid) * 32 + (lane & 31)), L_SCR);
        __builtin_amdgcn_s_sleep(WPER);
      }
    } else if (wid == 4) {
      __builtin_amdgcn_s_sleep(127); __builtin_amdgcn_s_sleep(127); __builtin_amdgcn_s_sleep(70);
    }
  }
  int tq, tp;
  tile_map<8, 28, 4, 7>(BID, tq, tp);
  const int p0 = tp * 128, q0 = tq * 128;
  if (wid >= 6) {
    Stream<KC, 16, 128, true> st;
    u_init(st, u, wid - 6, lane);
    if (own) {
      Operand Q{h, 32, q0, 64, 8 * 32 * TB, 0, true, nullptr, 0};
      WaitHookX hk;
      g_loader<KC, 16, 128, K3_NSQ, 32, -1, false>(qbuf, Q, wid - 6, lane, hk);
      BARRIER;
    }
    UHook hk;
    hk.kh = BID & 1;
    g_run<KC, 16, 128, K4_NS, 28 + KD, 3, false>(qbuf, st, wid - 6, lane, hk, false);
    __builtin_amdgcn_s_waitcnt(0x0F70);
    return;
  }
  if (wid >= 4) {
    Stream<KM, 16, 128> st;
    UFlags uf;
    w2_init(st, w2, cw1 + 224 * CW_BLK / 2, wid - 4, lane);
    uf.init(lds, flags, wid - 4, lane);
    int kh, tp4, tq4;
    k4_map(OPQ((int)BID), kh, tp4, tq4);
    W2Hook<K1NextHook<W2Base>> h4;
    h4.on = l0 && ((((int)((size_t)wq_ >> 7)) + (BID & 1) * 4 + (BID >> 6)) & 7) != 3; h4.wq = wq_; h4.dstn = lds + L_A + (wid - 4) * 1024; h4.gwn = wid - 4; h4.lanen = lane;
    h4.kh = kh; h4.uf = &uf; h4.own = own; h4.gww = wid - 4; h4.nev = wid == 5 ? 0 : 99;
    h4.setup();
    if (own) {
      Operand P{w1, F_, p0, 64, S_ * F_ * 2, 0, false, cw1, 32};
      SyncHook<K4NextHook, 2> hk;
      hk.st = &st; hk.lds = lds; hk.gw = wid - 4;
      g_loader<KM, 16, 128, K3_NSP, 32, 2, false>(pbuf, P, wid - 4, lane, hk, true);
      BARRIER;
      if (wid == 4) {
        uf.publish();
      }
    }
    g_run<KM, 16, 128, K4_NS, 28 + KD, -1, false>(pbuf, st, wid - 4, lane, h4, own);
    __builtin_amdgcn_s_waitcnt(0x0F70);
    return;
  }
  if (own) {
    Gate gate;
    gate.prep(wid, h_flag(flags, BID, lane), (1 << NR_H) - 1, flags + C_HCNT + (tq * NR_H + tp % NR_H) * RS2 + (lane & 31), 0xFFFFFFFFull, 1);
    gate.m1 = (1 << NP3) - 1;
    gate.seam();
    const int wp = wid >> 1, wq = wid & 1;
    f32x4 acc[4][4];
#pragma unroll
    for (int i = 0; i < 4; ++i) {
      const u32x2 bv = *(const LAS u32x2*)(lds + L_BIA + (wp * 64 + (i >> 1) * 8 + (lane >> 4) * 16 + ((i ^ (lane >> 5)) & 1) * 4) * 2);
#pragma unroll
      for (int j = 0; j < 4; ++j) acc[i][j] = f32x4{lo2f(bv[0]), hi2f(bv[0]), lo2f(bv[1]), hi2f(bv[1])};
    }
    consumer16w<KM, KC, 4, 4, K3_NSP, 128 * 128, 128 * 128, 32, false>(pbuf, qbuf, acc, wp * 4, wq * 4, lane, gate, [&] { }, [&](auto&& mm) {
      int ln = lane;
      asm volatile("" : "+v"(ln));
      const int fr = ln & 15, fq = (ln & 63) >> 4;
      const char* ub = (const char*)u + (size_t)(tq * 56 + tp * 2) * TB + wp * (TB / 2);
      unsigned uo[4];
      {
        const unsigned s1 = pow2sel((fr >> 1) == 3, 7, 10);
        uo[0] = phys<64>(wq * 32 + (fr >> 1)) * 128 + (fr & 1) * 64 + fq * 16; uo[1] = uo[0] + s1; uo[2] = uo[1] + s1; uo[3] = uo[2] + s1;
      }
      mm(0, 8);
#pragma unroll
      for (int n = 0; n < 8; ++n) { __builtin_amdgcn_sched_group_barrier(0x8, 1, 0); __builtin_amdgcn_sched_group_barrier(0x2, 2, 0); }
      asm volatile("" : "+v"(uo[0]), "+v"(uo[1]), "+v"(uo[2]), "+v"(uo[3]));
      const float c0 = -2.0f * 0.7978845608028654f * 1.4426950408889634f, c1 = c0 * 0.044715f;
      auto shade = [&](int n0) {
        if (n0 < 32) {
          mm(n0, n0 + GM);
#pragma unroll
          for (int n = 0; n < GM; ++n) { __builtin_amdgcn_sched_group_barrier(0x8, 1, 0); __builtin_amdgcn_sched_group_barrier(0x400, 1, 0); }
          __builtin_amdgcn_sched_group_barrier(0x400, 16 - GM, 0);
        }
      };
#pragma unroll
      for (int g = 0; g < 4; ++g) {
        f32x2 x[8], z[8];
        auto abar = [&](int pos) {
          if (g == 3 && pos == APOS) {
            if (pos > 0) asm volatile("" : "+v"(z[0]), "+v"(z[1]), "+v"(z[2]), "+v"(z[3]), "+v"(z[4]), "+v"(z[5]), "+v"(z[6]), "+v"(z[7]));
            SCHED0; WAITV(2); BARRIER; SCHED0;
          }
        };
        abar(0);
        ALIGN8;
#pragma unroll
        for (int e = 0; e < 8; ++e) {
          const f32x4 v = acc[(g >> 1) * 2 + ((e >> 1) & 1)][(g & 1) * 2 + (e >> 2)];
          x[e] = f32x2{v[2 * (e & 1)], v[2 * (e & 1) + 1]};
          z[e] = x[e] * x[e];
        }
#pragma unroll
        for (int e = 0; e < 8; ++e) z[e] = z[e] * c1 + c0;
#pragma unroll
        for (int e = 0; e < 8; ++e) z[e] *= x[e];
        SCHED0;
        abar(1);
#pragma unroll
        for (int e = 0; e < 8; ++e) z[e] = f32x2{__builtin_amdgcn_exp2f(z[e][0]), __builtin_amdgcn_exp2f(z[e][1])};
        shade(8 + g * GM);
        abar(2);
        ALIGN8;
#pragma unroll
        for (int e = 0; e < 8; ++e) z[e] += 1.0f;
        SCHED0;
#pragma unroll
        for (int e = 0; e < 8; ++e) z[e] = f32x2{__builtin_amdgcn_rcpf(z[e][0]), __builtin_amdgcn_rcpf(z[e][1])};
        abar(3);
        ALIGN8;
        u32x4 o[2];
#pragma unroll
        for (int e = 0; e < 8; ++e) z[e] *= x[e];
#pragma unroll
        for (int e = 0; e < 8; ++e) o[e >> 2][e & 3] = pk2(z[e][0], z[e][1]);
#pragma unroll
        for (int a = 0; a < 2; ++a)
          asm volatile("global_store_dwordx4 %0, %1, %2 sc1\n\ts_nop 1" ::"v"(uo[(g & 1) * 2 + a]), "v"(o[a]), "s"(ub + (g >> 1) * TB) : "memory");
      }
    }, [](int) {});
  }
  k4_consumer(lds, OPQ(out), pstat, OPQ(part), OPQ(flags), wid);
}

extern "C" __global__ __launch_bounds__(512, 2) void k_all(const u16* x0_, const u16* wqkv_, const u16* wo_, const u16* w1_, const u16* b1_, const u16* w2_,
                                                           const u16* b2_, u16* out_, char* ws_) {
  LAS char* lds = (LAS char*)(size_t)0;
  {
    unsigned long long pc;
    unsigned off;
    asm volatile("s_cmp_lt_u32 %3, %5\n\ts_cbranch_scc0 1f\n\ts_getpc_b64 %0\n\tv_lshlrev_b32 %1, 7, %2\n\ts_mov_b32 m0, %4\n\t"
                 "v_min_u32 %1, k9_end-k_all-256, %1\n\tglobal_load_lds_dword %1, %0\n1:"
                 : "=&s"(pc), "=&v"(off) : "v"(threadIdx.x), "s"(blockIdx.x), "i"(L_SCR), "i"(CWN) : "memory", "scc", "m0");
  }
  auto ka = (const __attribute__((address_space(4))) u64*)__builtin_amdgcn_kernarg_segment_ptr();
  asm volatile("" : "+s"(ka));
  typedef __attribute__((address_space(1))) u16 gu16;
#define ARGS auto kb = ka; asm volatile("" : "+s"(kb)); \
  const u16 *x0 = (u16*)(gu16*)kb[0], *wqkv = (u16*)(gu16*)kb[1], *wo = (u16*)(gu16*)kb[2], *w1 = (u16*)(gu16*)kb[3], *b1 = (u16*)(gu16*)kb[4], *w2 = (u16*)(gu16*)kb[5], *b2 = (u16*)(gu16*)kb[6]; \
  u16* out = (u16*)(gu16*)kb[7]; char* w = OPQ(ws)
  char* ws = (char*)(u16*)(gu16*)ka[8];
  const int wid = __builtin_amdgcn_readfirstlane(threadIdx.x >> 6);
  {
    const u16 *x0 = (u16*)(gu16*)ka[0], *wqkv = (u16*)(gu16*)ka[1];
    const int lane = threadIdx.x & 63;
    int tq, tp;
    tile_map<16, 16, 8, 4>(blockIdx.x, tq, tp);
    if (wid >= 6) {
      Stream<KC, 16, 144> st;
      st.init(Operand{x0, D_, tq * 128 - 16, 64, S_ * D_ * 2, 0, false, nullptr, 0}, wid - 6, lane);
      st.stage<16>(lds + K1_QOFF + (wid - 6) * 1024, lane >= 8);
    } else if (wid >= 4) {
      Stream<KM, 16, 192> st;
      st.init(Operand{wqkv, 3 * D_, tp * 64, D_, 3 * D_ * D_ * 2, 0, false, nullptr, 0}, wid - 4, lane);
      st.stage<0>(lds + L_A + (wid - 4) * 1024);
    } else {
      const u16* xb = x0 + (size_t)(tq * 128 + wid * 32) * D_ + tp * 64;
      int t3q, t3p, kh4, t4p, t4q;
      tile_map<8, 28, 4, 7>(blockIdx.x, t3q, t3p);
      k4_map(blockIdx.x, kh4, t4p, t4q);
      const char* bp = wid >= 2 ? ws + W_EPOCH : (const char*)(u16*)(gu16*)ka[4 + 2 * (wid & 1)] + (wid ? t4q : min(t3p, 27)) * 256;
      asm volatile("s_mov_b32 m0, %[lx]\n\ts_nop 0\n\tglobal_load_lds_dwordx4 %[xo], %[xb]\n\ts_add_u32 m0, %[lx], 1024 - 64\n\ts_nop 0\n\tglobal_load_lds_dwordx4 %[xo], %[xb] offset:64\n\t"
                   "s_add_u32 m0, %[lx], 2048\n\ts_nop 0\n\tglobal_load_lds_dwordx4 %[xo], %[xc]\n\ts_add_u32 m0, %[lx], 3072 - 64\n\ts_nop 0\n\tglobal_load_lds_dwordx4 %[xo], %[xc] offset:64\n\t"
                   "s_mov_b32 m0, %[lb]\n\ts_nop 0\n\tglobal_load_lds_dword %[bo], %[bp] sc1"
                   :: [xo] "v"((lane & 15) * (D_ * 2) + (lane & 48)), [xb] "s"(xb), [xc] "s"(xb + 16 * D_), [bo] "v"(lane * 4), [bp] "s"(bp),
                      [lx] "s"(L_C + wid * 4096), [lb] "s"(wid >= 2 ? L_RS + wid * 256 : L_BIA + wid * 256) : "memory", "m0", "scc");
      if (wid == 1) {
        const int b = blockIdx.x, l = min(lane, 35), r = l / 12, c = l - r * 12;
        touch_line((const char*)wqkv + (64 + 64 * r + ((b >> 2) & 1) * 32 + (b >> 3)) * 6144 + (c >> 2) * 2048 + ((b & 3) * 4 + (c & 3)) * 128, L_SCR);
      }
      if (wid == 3) {
        SlowColHook<NoHook, 3, 2 * D_, 4, 32, 16> hs;
        hs.setup2(wqkv, 3 * D_, (tp & ~3) * 128, 0, blockIdx.x >> 3, lane, true, L_SCR);
        touch_line(hs.p != nullptr ? hs.p : (const char*)wqkv, L_SCR);
      }
    }
    CFENCE;
  }
#pragma unroll 1
  for (int layer = 0; layer < 2; ++layer) {
    {
      ARGS;
      const int l = OPQ(layer);
      k1_body(lds, OPQ(wid), l == 0, wqkv, (u16*)(gu16*)kb[l * 7], (u16*)(w + W_O), (u32x4*)(w + W_XCH),
              (u64*)(w + W_CTL) + l * C_WORDS, (u32x2*)(w + W_PST), wo, w1, w2, (u16*)(w + W_CW));
    }
    {
      ARGS;
      const int l = OPQ(layer);
      k2_body(lds, OPQ(wid), wo, (const u16*)(w + W_O), x0, (u16*)(w + W_H),
              (unsigned long long*)(w + W_XCH), (u64*)(w + W_CTL) + l * C_WORDS, l == 0, w1, (const u16*)(w + W_CW));
    }
    {
      ARGS;
      const int l = OPQ(layer);
      k34_body(lds, OPQ(wid), l == 0, w1, (const u16*)(w + W_H), (u16*)(w + W_U), (const u16*)(w + W_CW), (u64*)(w + W_CTL) + l * C_WORDS,
               w2, out, l == 0 ? (u32x2*)(w + W_PST) : nullptr, (float*)(w + W_PART), wqkv);
    }
  }
  ARGS;
  if (blockIdx.x == 0 && wid == 7 && lane_id() == 0) __hip_atomic_store((u64*)(w + W_EPOCH), TAG, __ATOMIC_RELAXED, __HIP_MEMORY_SCOPE_AGENT);
}

extern "C" __global__ void k9_end() {}

extern "C" void kernel_launch(void* const* d_in, const int* in_sizes, int n_in, void* d_out, int out_size, void* d_ws,
                              size_t ws_size, hipStream_t stream) {
  (void)hipFuncSetAttribute((const void*)k_all, hipFuncAttributeMaxDynamicSharedMemorySize, L_SHM);
  k_all<<<256, 512, L_SHM, stream>>>((const u16*)d_in[0], (const u16*)d_in[1], (const u16*)d_in[2], (const u16*)d_in[4], (const u16*)d_in[5],
                                     (const u16*)d_in[6], (const u16*)d_in[7], (u16*)d_out, (char*)d_ws);
}
```

```cpp
#include <hip/hip_runtime.h>
#include <hip/hip_bf16.h>
#include <utility>
#include <type_traits>

typedef unsigned short u16;
using bf16x8 = __attribute__((ext_vector_type(8))) short;
using s16x4 = __attribute__((ext_vector_type(4))) short;
using f32x2 = __attribute__((ext_vector_type(2))) float;
using f32x4 = __attribute__((ext_vector_type(4))) float;
using f32x16 = __attribute__((ext_vector_type(16))) float;
using u32x2 = __attribute__((ext_vector_type(2))) unsigned;
using u32x4 = __attribute__((ext_vector_type(4))) unsigned;
using u32x12 = __attribute__((ext_vector_type(12))) unsigned;
typedef __bf16 bf16x2_t __attribute__((ext_vector_type(2)));
#define LAS __attribute__((address_space(3)))

constexpr int S_ = 2048, D_ = 1024, F_ = 3584;
template <int N> __device__ __forceinline__ int phys(int r) { return (r & 7) != 3 ? r : N + 4 + ((r >> 5) << 3) + ((r >> 3) & 3); }
constexpr int TB = 160 * 128;

__device__ __forceinline__ unsigned pk2(float a, float b) {
  f32x2 v = {a, b};
  return __builtin_bit_cast(unsigned, __builtin_convertvector(v, bf16x2_t));
}
__device__ __forceinline__ float lo2f(unsigned v) { return __uint_as_float(v << 16); }
__device__ __forceinline__ float hi2f(unsigned v) { return __uint_as_float(v & 0xFFFF0000u); }
__device__ __forceinline__ float bf2f(u16 v) { return __uint_as_float((unsigned)v << 16); }
__device__ __forceinline__ float rbf(float f) { return lo2f(pk2(f, f)); }
__device__ __forceinline__ u16 f2bf(float f) { return (u16)(pk2(f, f) & 0xFFFFu); }

enum { KC = 0, KM = 1 };
#define OPAQUE_BID unsigned BID = blockIdx.x; asm volatile("" : "+s"(BID))
__device__ __forceinline__ int OPQ(int v) { asm volatile("" : "+s"(v)); return v; }
__device__ __forceinline__ unsigned opq_bid() { OPAQUE_BID; return BID; }
__device__ __forceinline__ int lane_id() {
  const unsigned l = __builtin_amdgcn_mbcnt_hi(~0u, __builtin_amdgcn_mbcnt_lo(~0u, OPQ(0)));
  __builtin_assume(l < 64);
  return l;
}
constexpr int L_A = 0, L_B = 73728, L_C = L_B + 65536, L_D = L_C + 16384, L_TAG = L_D + 1280, L_CSQ = L_D + 1408, L_SCR = L_D + 1792, L_BIA = L_D + 2048, L_RS = L_D + 2560, L_MSK = L_RS + 1152, L_COF = L_MSK + 2048, L_SHM = L_COF + 1024;
typedef unsigned long long u64;
#define NR_O 8
#define NR_H 8
#define NR_U 8
#define NR_X 8
#define RS1 32
#define RS2 64
#define RSP 32
constexpr int C_FLAG = 0, C_UCNT = 1024 * RSP, C_HCNT = C_UCNT + 16 * NR_U * RS1, C_OCNT = C_HCNT + 8 * NR_H * RS2, C_XCNT = C_OCNT + 16 * NR_O * RS1,
              C_UCN2 = C_XCNT + 16 * NR_X * RS1, C_END = C_UCN2 + 16 * NR_U * RS1, C_WORDS = C_END <= 32768 ? 32768 : 131072;
static_assert(C_END <= C_WORDS, "control words");
#define TAG (*(volatile LAS u64*)(size_t)vconst<L_TAG>())

#define A1 16
#define A2 16
#define AC 2
#define CWN 8
#define FWS 1
#define KD 3
#define CKK 5
#define FBK 1
#define NP2 3
#define NP3 4
#define NP4 2
#define HQP 21
#define WT0 13
#define WSTG 2
#define WPER 16
#define ARMS 13
#define WC0 1
#define GM 8
#define HSW 1
#define GN 8
#define G0 3
#define GD 1
#define CFENCE asm volatile("" ::: "memory")
#define WAITV(n) asm volatile("s_waitcnt vmcnt(%0)" ::"i"(n) : "memory")
#define WAITL0 do { CFENCE; __builtin_amdgcn_s_waitcnt(0xC07F); CFENCE; } while (0)
#define BARRIER do { CFENCE; __builtin_amdgcn_s_barrier(); CFENCE; } while (0)
#define SCHED0 __builtin_amdgcn_sched_barrier(0)
#define ALIGN8 do { SCHED0; asm volatile(".p2align 3"); SCHED0; } while (0)

struct Operand {
  const u16* base;
  int ld;
  int off0;
  int cstride;
  int bytes;
  int kstep0;
  bool holes;
  const u16* alt;
  int alt_nt;
};
constexpr int CW_BLK = 80 * 128;

template <int L, int MS>
__device__ __forceinline__ int swz(int line) {
  if constexpr (L == KC) {
    return MS == 16 ? (line & 7) : ((line >> 1) & 7);
  } else {
    return MS == 16 ? ((((line >> 1) & 1) | (((line >> 3) & 1) << 1)) << 1) : (((line >> 1) & 1) << 2);
  }
}
template <int N> __device__ __forceinline__ void waitv() { WAITV(N); }
__device__ __forceinline__ unsigned pow2sel(bool c, int a, int b) { unsigned e = c ? a : b; asm volatile("" : "+v"(e)); return 1u << e; }
template <unsigned C> __device__ __forceinline__ unsigned vconst() { unsigned v; asm volatile("v_mov_b32 %0, %1" : "=v"(v) : "i"(C)); return v; }

__device__ __forceinline__ u64 vz() { u64 v; asm volatile("v_mov_b64 %0, 0" : "=v"(v)); return v; }

__device__ __forceinline__ u64 tag_ld() {
  u64 t;
  asm volatile("ds_read_b64 %0, %1\n\ts_waitcnt lgkmcnt(0)" : "=v"(t) : "v"(vconst<L_TAG>()) : "memory");
  return t;
}
__device__ __forceinline__ void st16(void* p, u32x4 v) {
  asm volatile("global_store_dwordx4 %0, %1, off sc1\n\ts_nop 1" ::"v"(p), "v"(v) : "memory");
}

__device__ __forceinline__ const char* noslow(const void* base, unsigned line) {
  return (const char*)base + (size_t)((((unsigned)((size_t)base >> 7) + line) & 7) == 3 ? line ^ 1 : line) * 128;
}
__device__ __forceinline__ void touch_line(const void* p, unsigned lds_scratch) {
  unsigned keep;
  asm volatile("s_mov_b32 %0, m0\n\ts_mov_b32 m0, %2\n\ts_nop 0\n\tglobal_load_lds_dword %1, off\n\ts_mov_b32 m0, %0"
               : "=&s"(keep) : "v"(p), "s"(lds_scratch) : "memory");
}

struct NoHook {
  static constexpr int EXTRA = 0, NFIRST = 0, AUX = 0, NEXT = 0;
  static constexpr bool EVOD = false;
  static constexpr bool OWNP = false;
  template <int T> __device__ __forceinline__ void next() {}
  static constexpr bool PREALL = false;
  static constexpr bool PIN = false, UNROLL = false, COLD = false, PSW = false;
  __device__ __forceinline__ bool cold() const { return false; }
  template <int S> __device__ __forceinline__ void stepc() {}
  template <int S, int N> __device__ __forceinline__ void wait() { waitv<N>(); }
  template <int T> __device__ __forceinline__ void tail() {}
  __device__ __forceinline__ void ready() {}
  __device__ __forceinline__ void first() {}
  __device__ __forceinline__ void afterr() {}
  __device__ __forceinline__ void last() {}
  __device__ __forceinline__ void mid() {}
  __device__ __forceinline__ void step(int) {}
};

template <int L, int MS, int B, bool PSW = false>
struct Stream {
  static constexpr int LW = B / 16, STG = B * 128;
  static constexpr int NSUB = (L == KM && B == 128 && !PSW) ? 2 : 1;
  unsigned voff[LW];
  const void* gbase[NSUB];
  int gbytes[NSUB], so[NSUB], inc[NSUB], jmp[NSUB];
  __device__ __forceinline__ void init(const Operand& X, int gw, int lane) {
    const int r8 = lane >> 3, c8 = lane & 7, l0 = gw * 8 + r8;
    const unsigned sw = (unsigned)((c8 ^ (PSW && L == KC ? (r8 & 3) | (gw << 2) : swz<L, MS>(l0))) * 16);
    if constexpr (L == KC) {
      gbase[0] = X.base; gbytes[0] = X.bytes;
      if (X.holes) {
        jmp[0] = 27 * TB;
        inc[0] = TB; so[0] = ((X.off0 >> 7) * X.ld + X.kstep0) * TB;
        {
          const bool iso = X.ld == 16;
          const int ln = !iso ? l0 >> 1 : ((l0 >> 1) & ~1) | (l0 & 1), ps = !iso ? l0 & 1 : (l0 >> 1) & 1;
          const bool h = ln == 3;
          const unsigned v0 = (h ? 68 : ln) * 128 + ps * 64 + (sw & 48) + (sw >> 6) * (TB / 2), s1 = pow2sel(h, 7, 10), s4 = pow2sel(h, 10, 12);
#pragma unroll
          for (int i = 0; i < LW; ++i) voff[i] = i == 0 ? v0 + ((X.off0 & 64) ? s4 : 0) : (i == 4 ? v0 + s4 : voff[i - 1] + s1);
        }
      } else {
        inc[0] = 128; so[0] = X.kstep0 * 128;
#pragma unroll
        for (int i = 0; i < LW; ++i)
          voff[i] = (unsigned)(i == 0 ? max(X.off0 + 2 * r8 + gw, 0) : X.off0 + 16 + 2 * l0 + 32 * ((i - 1) & 3) + ((i - 1) >> 2)) * (unsigned)X.ld * 2u + sw;
      }
    } else if constexpr (PSW) {
      gbase[0] = X.base; gbytes[0] = X.bytes;
      inc[0] = CW_BLK; so[0] = (X.off0 >> 6) * 16 * CW_BLK;
      const bool h = r8 == 3;
      const unsigned v0 = (h ? 68 + gw : l0) * 128 + sw, s2 = pow2sel(h, 8, 11), s1 = pow2sel(h, 10, 12);
#pragma unroll
      for (int i = 0; i < LW; ++i) voff[i] = i == 0 ? v0 : i == 2 ? v0 + s1 : i >= 4 ? voff[i - 4] + 16 * CW_BLK : voff[i - 1] + s2;
    } else if constexpr (NSUB == 1) {
      gbase[0] = X.base; gbytes[0] = X.bytes;
      inc[0] = 128 * X.ld; so[0] = X.kstep0 * inc[0] + X.off0 * 2;
#pragma unroll
      for (int i = 0; i < LW; ++i) voff[i] = (unsigned)((l0 + 16 * (i & 3)) * X.ld + (i >> 2) * X.cstride) * 2u + sw;
    } else {
      const bool isw2 = X.ld == S_;
      const unsigned sx = isw2 ? sw : ((sw & 48) << 1) | ((sw & 64) >> 2);
#pragma unroll
      for (int t = 0; t < NSUB; ++t) {
        const int lc = (X.off0 >> 6) ^ t;
        const bool slow = (((int)((size_t)X.base >> 7) + lc) & 7) == 3;
        gbase[t] = slow ? (const void*)X.alt : (const void*)X.base;
        gbytes[t] = slow ? (X.ld >> 9) * X.alt_nt * CW_BLK : X.bytes;
        inc[t] = slow ? CW_BLK : 128 * X.ld;
        so[t] = X.kstep0 * inc[t] + (slow ? (lc >> 3) * X.alt_nt * CW_BLK : lc * 128);
        const unsigned rowb = slow ? 128 : X.ld * 2;
        const bool h = (slow ? r8 : 0) == 3;
        const unsigned grpb = isw2 && !slow ? 2 * rowb : rowb;
        const unsigned v0 = (h ? 68 + gw : r8) * rowb + (h ? 0 : gw * 8 * grpb) + sx, s2 = (h ? 2 : 16) * grpb, s1 = (h ? 8 : 32) * grpb;
        jmp[t] = slow ? 27 * CW_BLK : (28 * 64 - 8) * rowb;
        voff[4 * t] = v0; voff[4 * t + 1] = v0 + s2; voff[4 * t + 2] = v0 + s1; voff[4 * t + 3] = v0 + s1 + s2;
      }
    }
  }
  __device__ __forceinline__ void pin() {
#pragma unroll
    for (int i = 0; i < LW; ++i) asm volatile("" : "+v"(voff[i]));
#pragma unroll
    for (int t = 0; t < NSUB; ++t) asm volatile("" : "+s"(so[t]));
  }
  __device__ __forceinline__ void skip(int n) {
#pragma unroll
    for (int t = 0; t < NSUB; ++t) so[t] += n * inc[t];
  }
  __device__ __forceinline__ void twice() {
#pragma unroll
    for (int t = 0; t < NSUB; ++t) inc[t] *= 2;
  }
  __device__ __forceinline__ void back() {
#pragma unroll
    for (int t = 0; t < NSUB; ++t) so[t] -= jmp[t];
  }
  template <int AUX, int NT_ = NSUB>
  __device__ __forceinline__ void stage(LAS char* dst, bool act0 = true) {
#pragma unroll
    for (int t = 0; t < NT_; ++t) {
      const auto rsrc = __builtin_amdgcn_make_buffer_rsrc((void*)gbase[t], 0, gbytes[t], 0x00020000);
#pragma unroll
      for (int i = t * (LW / NSUB); i < (t + 1) * (LW / NSUB); ++i) {
#if defined(__HIP_DEVICE_COMPILE__)
        if (i > 0 || act0) __builtin_amdgcn_raw_ptr_buffer_load_lds(rsrc, (LAS void*)(dst + i * 2048), 16, voff[i], so[t], 0, AUX);
#endif
      }
      so[t] += inc[t];
    }
  }
};

template <int N, class F>
__device__ __forceinline__ void static_for(F&& f) {
  [&]<int... I>(std::integer_sequence<int, I...>) { (f(std::integral_constant<int, I>{}), ...); }(std::make_integer_sequence<int, N>{});
}

template <int L, int MS, int B, int NS, int NT, int KID, bool SKIP0, class Hook>
__device__ __forceinline__ void g_run(LAS char* ring, Stream<L, MS, B, Hook::PSW>& st, int gw, int lane, Hook& hook, bool filled) {
  constexpr int LW = B / 16, STG = B * 128;
  constexpr int NPRE = Hook::PIN ? (Hook::COLD ? 2 : NT == 16 ? NP2 : NT == 32 ? NP3 : NP4) : NS;
  static_assert(Hook::PREALL, "cfg");
  static_assert(B % 16 == 0 && NS >= 3 && NT >= NS, "cfg");
  const bool act0 = !SKIP0 || lane >= 8;
  auto stage = [&](int slot) { st.template stage<Hook::AUX>(ring + slot * STG + gw * 1024, act0); };
  hook.ready();
  if (Hook::COLD && !Hook::PIN ? hook.cold() : !filled) {
    if (Hook::COLD && hook.cold()) st.skip(1); else stage(0);
    stage(1);
    if constexpr (Hook::PREALL) {
      if (!hook.cold()) {
#pragma unroll
        for (int i = 2; i < NPRE; ++i) stage(i);
      }
    }
  }
  SCHED0;
  if constexpr (Hook::COLD && !Hook::PIN) {
    if (hook.cold()) {
      waitv<LW>();
      BARRIER;
#pragma unroll
      for (int i = 2; i < NS; ++i) stage(i);
    }
    SCHED0;
    hook.first();
    if (!hook.cold()) {
      BARRIER;
      if (!filled) { stage(0); stage(1); stage(2); }
      BARRIER;
      waitv<(NS - 1) * LW>();
      BARRIER;
    }
  } else {
    if constexpr (!Hook::PIN) hook.first();
    if constexpr (!Hook::OWNP) {
      waitv<(NPRE - 1) * LW + Hook::NFIRST>();
      BARRIER;
    }
#pragma unroll
    for (int i = NPRE; i < NS; ++i) stage(i);
    if constexpr (Hook::PIN) hook.first();
  }
  if constexpr (Hook::UNROLL) {
    [&]<int... S>(std::integer_sequence<int, S...>) {
      (([&] {
         hook.template wait<S, (NS - 2) * LW>();
         BARRIER;
         stage(S % NS);
         hook.step(S);
         hook.template stepc<S>();
         SCHED0;
       }()), ...);
    }(std::make_integer_sequence<int, NT - NS>{});
  } else {
    int slot = 0;
#pragma unroll 1
    for (int s = 0; s < NT - NS - (Hook::EVOD ? 2 * KD : 0); ++s) {
      waitv<(NS - 2) * LW>();
      BARRIER;
      if (Hook::EVOD && s == 14 - NS) st.back();
      stage(slot);
      slot = (slot == NS - 1) ? 0 : slot + 1;
      hook.step(s);
    }
  }
  constexpr int LH = Hook::EVOD && L == KM ? LW / 2 : LW;
  if constexpr (Hook::EVOD) {
    static_for<2 * KD>([&](auto kc) {
      constexpr int k = decltype(kc)::value, n = NT - 2 * KD + k;
      waitv<(k > 1 ? LH : LW) + (k > 0 ? LH : LW)>();
      BARRIER;
      if constexpr (k == KD) st.so[0] += (hook.kh ? -14 - KD : 14 - KD) * st.inc[0];
      st.template stage<Hook::AUX, 1>(ring + (n % NS) * STG + gw * 1024);
    });
  }
  hook.last();
  [&]<int... T>(std::integer_sequence<int, T...>) {
    (([&] {
       hook.template tail<T>();
       if constexpr (T < NS - 2) hook.template wait<NT - NS + T, (NS - 2 - T) * LH + Hook::EXTRA + T * Hook::NEXT>();
       if constexpr (T == NS - 2) hook.template wait<NT - NS + T, T * Hook::NEXT>();
       BARRIER;
       hook.template next<T>();
       if constexpr (T == NS - 2) hook.mid();
     }()), ...);
  }(std::make_integer_sequence<int, NS>{});
}

template <int L, int MS, int B, int NS, int NT, int KID, bool SKIP0, class Hook>
__device__ __forceinline__ void g_loader(LAS char* ring, const Operand& X, int gw, int lane, Hook& hook, bool filled = false) {
  Stream<L, MS, B, Hook::PSW> st;
  st.init(X, gw, lane);
  if constexpr (Hook::PIN) st.pin();
  if (filled) st.skip(NS);
  g_run<L, MS, B, NS, NT, KID, SKIP0>(ring, st, gw, lane, hook, filled);
}

struct ResHook : NoHook {
  static constexpr bool PSW = true;
  static constexpr int AUX = 16;
  const u16* base; int row0, col0; LAS char* dst; int gw, lane;
  __device__ __forceinline__ void first() {
    if (base != nullptr) {
      const int r8 = lane >> 3, c8 = lane & 7;
      const unsigned vo = ((unsigned)(row0 + gw * 8 + r8) * D_ + col0) * 2u + (unsigned)((c8 ^ swz<KC, 32>(gw * 8 + r8)) * 16);
      const auto rsrc = __builtin_amdgcn_make_buffer_rsrc((void*)base, 0, S_ * D_ * 2, 0x00020000);
#pragma unroll
      for (int i = 0; i < 8; ++i) {
#if defined(__HIP_DEVICE_COMPILE__)
        __builtin_amdgcn_raw_ptr_buffer_load_lds(rsrc, (LAS void*)(dst + gw * 1024 + i * 2048), 16, vo, ((i & 3) * 16 * D_ + (i >> 2) * 64) * 2, 0, 0);
#endif
      }
    }
    WAITV(0); BARRIER;
  }
};

template <class Base, int NG, int GS, int NC, int NW, int NT>
struct SlowColHook : Base {
  const char* p;
  int st, inc8;
  unsigned scr;
  const u16* base_; int ld_, cb0_, kstep0_, j_, lane_; bool leader_;
  __device__ __forceinline__ void setup(const u16* base, int ld, int cb0, int kstep0, int j, int lane, bool leader, unsigned scratch) {
    base_ = base; ld_ = ld; cb0_ = cb0; kstep0_ = kstep0; j_ = j; lane_ = lane; leader_ = leader; scr = scratch;
  }
  __device__ __forceinline__ void setup2(const u16* base, int ld, int cb0, int kstep0, int j, int lane, bool leader, unsigned scratch) {
    asm volatile("" : "+v"(lane));
    constexpr int MAXC = (NC + 7) / 8, PER = (NG * MAXC * 64 + NW - 1) / NW;
    static_assert(PER * 8 <= 64, "lanes");
    const int cf = (3 - (int)(((size_t)base + cb0) >> 7)) & 7;
    const int li = j * PER + lane % PER;
    st = lane / PER;
    const int ci = li >> 6, k = li & 63, g = ci / MAXC, c = cf + 8 * (ci % MAXC);
    const bool act = leader && st < 8 && g < NG && c < NC;
    p = act ? (const char*)base + (size_t)((kstep0 + st) * 64 + k) * ld * 2 + cb0 + g * GS + c * 128 : nullptr;
    inc8 = 8 * 64 * ld * 2;
    scr = scratch;
  }
  __device__ __forceinline__ void touch(bool go = true) {
    if (go && p != nullptr && st < NT) touch_line(p, scr);
    p = p != nullptr ? p + inc8 : p;
    st += 8;
  }
  __device__ __forceinline__ void first() { setup2(base_, ld_, cb0_, kstep0_, j_, lane_, leader_, scr); touch(!Base::cold()); Base::first(); }
  __device__ __forceinline__ void step(int s) { if ((s & 7) == 0 && s + 8 < NT) touch(); Base::step(s); }
};

#define PSLP 3
__device__ __forceinline__ void flag_set(u64* p, u64 tag) { asm volatile("global_atomic_swap_x2 %0, %1, off sc1" ::"v"(p), "v"(tag) : "memory"); }
#define POLL1(r) "global_atomic_or_x2 %[" #r "], %[q], %[z], off sc0 sc1\n\ts_sleep %[sl]\n\t"
#define POLL2(r) "s_waitcnt vmcnt(3)\n\tv_cmp_ne_u64 vcc, %[tag], %[" #r "]\n\ts_cbranch_vccz 2f\n\t" POLL1(r)
#define POLL_LOOP "s_mov_b64 %[sv], exec\n\ts_mov_b64 exec, %[em]\n\ts_mov_b32 %[n], 0\n\t" POLL1(a) POLL1(b) POLL1(c) POLL1(d) POLL_BODY
#define POLL_BODY \
  "1:\n\t" POLL2(a) POLL2(b) POLL2(c) POLL2(d) \
  "s_add_u32 %[n], %[n], 1\n\ts_cmp_lt_u32 %[n], 0x8000\n\ts_cbranch_scc1 1b\n\t" \
  "2:\n\ts_mov_b64 exec, %[sv]\n\t"
#define PCHK(r, l) "v_cmp_ne_u64 vcc, %[tag], %[" #r "]\n\ts_and_b64 %[t], vcc, %[m1]\n\ts_cbranch_scc1 " #l "f\n\ts_cbranch_vccz 2f\n\ts_barrier\n\ts_mov_b64 %[m1], -1\n" #l ":\n\t"
#define POLL3(r, l) "s_waitcnt vmcnt(3)\n\t" PCHK(r, l) POLL1(r)
#define PEND "s_add_u32 %[n], %[n], 1\n\ts_cmp_lt_u32 %[n], 0x8000\n\ts_cbranch_scc1 1b\n" \
             "2:\n\ts_mov_b64 exec, -1\n\ts_cmp_eq_u64 %[m1], -1\n\ts_cbranch_scc1 8f\n\ts_barrier\n8:"
#define POLL0(r) "global_atomic_or_x2 %[" #r "], %[q], %[z], off sc0 sc1\n\t"
struct Gate {
  const u64* p; u64 em, sm, on, tag, a, b, c, d;
  __device__ __forceinline__ void prep(int wid, const u64* set, u64 sm_, const u64* get, u64 em_, u64 on_) {
    p = wid == 0 ? set : get; em = wid == 1 ? em_ : 0; sm = wid == 0 ? sm_ : 0; on = on_;
    tag = TAG;
    asm volatile("" : "+v"(p), "+v"(tag));
  }
  __device__ __forceinline__ void seam() {
    u64 z = vz();
    asm volatile("s_cmp_eq_u64 %[on], 0\n\ts_cbranch_scc1 7f\n\ts_waitcnt vmcnt(0)\n\ts_barrier\n\t"
                 "s_cmp_eq_u64 %[sm], 0\n\ts_cbranch_scc1 6f\n\ts_mov_b64 exec, %[sm]\n\tglobal_atomic_swap_x2 %[q], %[tag], off sc1\n"
                 "6:\n\ts_cmp_eq_u64 %[em], 0\n\ts_cbranch_scc1 7f\n\ts_mov_b64 exec, %[em]\n\t" POLL0(a) "\n7:\n\ts_mov_b64 exec, -1"
                 : [a] "=&v"(a) : [q] "v"(p), [z] "v"(z), [tag] "v"(tag), [sm] "s"(sm), [em] "s"(em), [on] "s"(on) : "memory", "scc");
  }
  u64 m1;
  __device__ __forceinline__ void pass() {
    u64 z = vz(), t;
    unsigned n;
    asm volatile("s_cmp_eq_u64 %[on], 0\n\ts_cbranch_scc1 8f\n\ts_cmp_eq_u64 %[em], 0\n\ts_cbranch_scc1 2f\n\ts_mov_b64 exec, %[em]\n\ts_mov_b32 %[n], 0\n\t"
                 POLL0(b) "s_waitcnt vmcnt(1)\n\t" PCHK(a, 3) POLL1(c) POLL1(d) POLL0(a)
                 "1:\n\t" POLL3(b, 4) POLL3(c, 5) POLL3(d, 6) POLL3(a, 7) PEND
                 : [a] "+v"(a), [b] "=&v"(b), [c] "=&v"(c), [d] "=&v"(d), [n] "=&s"(n), [t] "=&s"(t), [m1] "+s"(m1)
                 : [q] "v"(p), [z] "v"(z), [tag] "v"(tag), [em] "s"(em), [on] "s"(on), [sl] "i"(PSLP) : "memory", "vcc", "scc");
  }
  __device__ __forceinline__ void done() {
    asm volatile("s_cmp_eq_u64 %[em], 0\n\ts_cbranch_scc1 9f\n\ts_waitcnt vmcnt(0)\n9:" :: "v"(a), "v"(b), "v"(c), "v"(d), [em] "s"(em) : "memory", "scc");
  }
};
struct DrainGate {
  __device__ __forceinline__ void pass() { BARRIER; WAITV(0); }
  __device__ __forceinline__ void done() {}
};
struct WaitHook : NoHook {
  static constexpr int AUX = 16;
  static constexpr bool PIN = true;
  static constexpr bool PREALL = true;
  __device__ __forceinline__ void ready() {
    BARRIER;
  }
};
struct WaitHookX : WaitHook {
  __device__ __forceinline__ void ready() { WAITV(0); BARRIER; BARRIER; }
};
struct WaitHookP : WaitHook { static constexpr bool PSW = true; };
template <class Base, int NBAR>
struct SyncHook : Base {
  static constexpr bool PREALL = true;
  __device__ __forceinline__ void first() {
    Base::first();
#pragma unroll
    for (int i = 0; i < NBAR; ++i) {
      BARRIER;
    }
    Base::afterr();
  }
};

template <int L, int MS, int NF, bool PERM = false, int FS = 1, bool HS = false>
struct FragAddr {
  static constexpr int NA = (L == KC) ? (MS == 16 ? 2 : 4) : (HS ? 2 * NF : NF);
  unsigned a[NA];
  __device__ __forceinline__ void init(LAS char* buf, int f0, int lane, int fx = 0) {
    const unsigned base = (unsigned)(size_t)buf;
    if constexpr (MS == 16) {
      const int fr = lane & 15, fq = lane >> 4, tq = (lane & 15) >> 2, tp = lane & 3;
      if constexpr (L == KC) {
        const int row = f0 * 16 + (PERM ? tq * 8 + tp : fr);
        a[0] = base + row * 128 + ((fq ^ (fr & 7)) * 16);
        a[1] = base + row * 128 + (((4 + fq) ^ (fr & 7)) * 16);
      } else {
        const int gl = ((tq >> 1) & 1) | ((fq & 1) << 1);
#pragma unroll
        for (int i = 0; i < NF; ++i) {
          const int cb = HS ? (i >> 1) * 4 + f0 + (i & 1) : (f0 + i * FS) ^ fx;
          if constexpr (PERM) a[i] = base + (fq * 8 + tq) * 128 + (cb >> 2) * 8192 + ((((cb & 2) * 2 + tp) ^ (gl * 2)) * 16) + ((cb ^ (tp >> 1)) & 1) * 8;
          else a[i] = base + (fq * 8 + tq) * 128 + tp * 8 + (cb >> 2) * 8192 + (((cb & 3) ^ gl) * 32);
          if constexpr (HS) { a[i] += (fq >> 1) * 512; a[NF + i] = a[i] ^ 512; }
        }
      }
    } else {
      const int r = lane & 31, h = lane >> 5, g = lane >> 4, tq = (lane & 15) >> 2, tp = lane & 3;
      if constexpr (L == KC) {
#pragma unroll
        for (int t = 0; t < 4; ++t) a[t] = base + (f0 * 32 + r) * 128 + (((2 * t + h) ^ ((r >> 1) & 7)) * 16);
      } else {
#pragma unroll
        for (int i = 0; i < NF; ++i) {
          const int cb = (f0 + i) ^ fx;
          a[i] = base + (8 * (g >> 1) + tq) * 128 + (cb >> 1) * 8192 + (((cb & 1) ^ ((tq >> 1) & 1)) * 64) + (g & 1) * 32 + tp * 8;
        }
      }
    }
#pragma unroll
    for (int i = 0; i < NA; ++i) asm volatile("" : "+v"(a[i]));
  }
  __device__ __forceinline__ bf16x8 read(int so, int i, int t) const {
    if constexpr (L == KC) {
      return *(const LAS bf16x8*)((LAS char*)(size_t)a[t] + (so + (PERM ? (i >> 1) * 4096 + (i & 1) * 512 : i * (MS * 128))));
    } else {
      LAS char* p = (LAS char*)(size_t)a[i] + (so + t * (MS == 16 ? 4096 : 2048));
      s16x4 lo = __builtin_amdgcn_ds_read_tr16_b64_v4i16((LAS s16x4*)(p));
      s16x4 hi = __builtin_amdgcn_ds_read_tr16_b64_v4i16((LAS s16x4*)(HS ? (LAS char*)(size_t)a[HS ? NF + i : i] + (so + t * 4096) : p + 512));
      return __builtin_shufflevector(lo, hi, 0, 1, 2, 3, 4, 5, 6, 7);
    }
  }
};

template <int NM, int PER>
__device__ __forceinline__ void interleave() {
#pragma unroll
  for (int i = 0; i < NM; ++i) {
    __builtin_amdgcn_sched_group_barrier(0x8, 1, 0);
    __builtin_amdgcn_sched_group_barrier(0x100, PER, 0);
  }
}

template <int PL, int QL, int FP, int FQ, int NS, int PSTG, int QSTG, int NT, bool SPLIT, class G, class Late, class Mid, class Tick>
__device__ __forceinline__ void consumer16w(LAS char* pbuf, LAS char* qbuf, f32x4 (&acc)[FP][FQ], int fp0, int fq0, int lane, G& gate, Late&& late, Mid&& mid, Tick&& tick) {
  FragAddr<PL, 16, FP, true> pa; pa.init(pbuf, fp0, lane);
  FragAddr<QL, 16, FQ, false, 1, QL == KM && HSW> qa; qa.init(qbuf, fq0, lane);
  struct Fr { bf16x8 p[2][FP]; bf16x8 q[2][FQ]; };
  auto rdt = [&](Fr& f, int slot, int t) {
#pragma unroll
    for (int i = 0; i < FP; ++i) f.p[t][i] = pa.read(slot * PSTG, i, t);
#pragma unroll
    for (int j = 0; j < FQ; ++j) f.q[t][j] = qa.read(slot * QSTG, j, t);
  };
  auto rd = [&](Fr& f, int slot) { rdt(f, slot, 0); rdt(f, slot, 1); };
  auto mmr = [&](const Fr& f, int j0, int j1, int t0 = 0, int t1 = 2) {
#pragma unroll
    for (int t = t0; t < t1; ++t)
#pragma unroll
      for (int j = j0; j < j1; ++j)
#pragma unroll
        for (int ii = 0; ii < FP; ++ii) { const int i = (j & 1) ? FP - 1 - ii : ii; acc[i][j] = __builtin_amdgcn_mfma_f32_16x16x32_bf16(f.p[t][i], f.q[t][j], acc[i][j], 0, 0, 0); }
  };
  constexpr int U = NS;
  constexpr int NG = (NT - 2 - SPLIT) / U, REM = (NT - 1) % U;
  Fr f[2];
  auto step = [&](auto uc) {
    constexpr int u = (decltype(uc)::value + 1) % U;
    SCHED0; WAITL0; BARRIER; SCHED0;
    rd(f[(u + 1) & 1], (u + 1) % NS);
    mmr(f[u & 1], 0, FQ);
    interleave<2 * FP * FQ, FQ < 4 ? 2 : 1>();
  };
  if constexpr (FQ < 4) {
    gate.pass();
    late();
  }
#pragma unroll
  for (int i = 0; i < FP; ++i)
#pragma unroll
    for (int j = 0; j < FQ; ++j) asm volatile("" : "+v"(acc[i][j]));
  if constexpr (FQ == 4) gate.pass();
  BARRIER;
  rdt(f[0], 0, 0);
  SCHED0;
  rdt(f[0], 0, 1);
  SCHED0;
  mmr(f[0], 0, FQ, 0, 1);
  SCHED0; gate.done(); WAITL0; BARRIER; SCHED0;
  rd(f[1], 1);
  mmr(f[0], 0, FQ, 1, 2);
  interleave<FP * FQ, FQ < 4 ? 3 : 2>();
#pragma unroll 1
  for (int g = 0; g < NG; ++g) static_for<U>(step);
  if constexpr (!SPLIT) {
    static_for<REM - 1>(step);
    SCHED0; WAITL0; BARRIER; SCHED0;
    if constexpr (FQ < 4) {
      mmr(f[REM & 1], 0, 1);
      mmr(f[REM & 1], 1, 2);
    } else mid([&](int n0, int n1) {
#pragma unroll
      for (int n = n0; n < n1; ++n) {
        const int t = (n >> 2) & 1, i = (n >> 4) * 2 + (n & 1), j = ((n >> 3) & 1) * 2 + ((n >> 1) & 1);
        acc[i][j] = __builtin_amdgcn_mfma_f32_16x16x32_bf16(f[REM & 1].p[t][i], f[REM & 1].q[t][j], acc[i][j], 0, 0, 0);
      }
    });
  } else {
    static_for<(NT - 3) % U>(step);
    SCHED0; WAITL0; BARRIER; SCHED0;
    rd(f[REM & 1], REM % NS);
    mmr(f[(REM - 1) & 1], FQ / 2, FQ);
    interleave<FP * FQ, 2>();
    SCHED0; WAITL0; BARRIER; SCHED0;
    auto m1 = [&](const Fr& g, int t, int i, int j) { acc[i][j] = __builtin_amdgcn_mfma_f32_16x16x32_bf16(g.p[t][i], g.q[t][j], acc[i][j], 0, 0, 0); };
#pragma unroll
    for (int i = 0; i <= FP; ++i) {
      SCHED0;
#pragma unroll
      for (int n = 0; n < FQ; ++n) {
        if (i < FP) m1(f[REM & 1], n / (FQ / 2), i, FQ / 2 + n % (FQ / 2));
        else m1(f[(REM - 1) & 1], 0, n, 0);
      }
      if (i > 0) {
        mid(i - 1);
        __builtin_amdgcn_sched_group_barrier(0x8, 2, 0);
        __builtin_amdgcn_sched_group_barrier(0x2, 2, 0);
        __builtin_amdgcn_sched_group_barrier(0x8, 1, 0);
        __builtin_amdgcn_sched_group_barrier(0x2, 2, 0);
        __builtin_amdgcn_sched_group_barrier(0x8, 1, 0);
        __builtin_amdgcn_sched_group_barrier(0x2, 1, 0);
        __builtin_amdgcn_sched_group_barrier(0x40, 1, 0);
      }
    }
    SCHED0;
#pragma unroll
    for (int i = 0; i < FP; ++i) m1(f[(REM - 1) & 1], 1, i, 0);
    mmr(f[(REM - 1) & 1], 1, FQ / 2);
    static_for<2 * KD + 1>([&](auto kc) {
      constexpr int k = decltype(kc)::value, n = NT - 1 + k;
      SCHED0;
      if constexpr (k > 0) { tick(k - 1); WAITL0; BARRIER; SCHED0; }
      if constexpr (k < 2 * KD) {
#pragma unroll
        for (int t = 0; t < 2; ++t) {
#pragma unroll
          for (int i = 0; i < FP; ++i) f[(n + 1) & 1].p[t][i] = pa.read(((n + 1) % NS) * PSTG, i, t);
#pragma unroll
          for (int j = 0; j < FQ / 2; ++j) f[(n + 1) & 1].q[t][j] = qa.read(((n + 1) % NS) * QSTG, j, t);
        }
      } else late();
      mmr(f[n & 1], 0, FQ / 2);
      if constexpr (k < 2 * KD) interleave<FP * FQ, 2>();
    });
    SCHED0;
    tick(2 * KD);
  }
}

template <int NQT, int NPT, int RQ, int RP>
__device__ __forceinline__ void tile_map(unsigned BID, int& tq, int& tp) {
  static_assert((NQT / RQ) * (NPT / RP) == 8 && NQT % RQ == 0 && NPT % RP == 0, "8 regions");
  const int x = BID & 7, j = BID >> 3;
  constexpr int GP = NPT / RP;
  tq = (x / GP) * RQ + j % RQ;
  tp = (x % GP) * RP + j / RQ;
}

__device__ __forceinline__ void k4_map(unsigned BID, int& kh, int& tp, int& tq) {
  const int x8 = BID & 7, j8 = BID >> 3;
  kh = x8 & 1; tp = ((x8 >> 1) & 1) * 4 + (j8 >> 3); tq = (x8 >> 2) * 8 + (j8 & 7);
}

constexpr int K1_NS = 3, K1_PSTG = 192 * 128, K1_QSTG = 144 * 128;
constexpr int K1_QOFF = L_B, K1_ST = L_D;

__device__ __forceinline__ float max2(float a, float b) { return __builtin_elementwise_maximum(a, b); }
__device__ __forceinline__ float max3(float a, float b, float c) { return max2(max2(a, b), c); }
__device__ __forceinline__ float rows_sum(float v) {
  auto a = __builtin_amdgcn_permlane16_swap(__float_as_uint(v), __float_as_uint(v), false, false);
  v = __uint_as_float(a[0]) + __uint_as_float(a[1]);
  auto b = __builtin_amdgcn_permlane32_swap(__float_as_uint(v), __float_as_uint(v), false, false);
  return __uint_as_float(b[0]) + __uint_as_float(b[1]);
}

__device__ __forceinline__ float half_sum(float v) {
  auto b = __builtin_amdgcn_permlane32_swap(__float_as_uint(v), __float_as_uint(v), false, false);
  return __uint_as_float(b[0]) + __uint_as_float(b[1]);
}
__device__ __forceinline__ f32x2 ln_coef(float sm, float sq) {
  const float mean = sm * (1.0f / D_);
  const float var = fmaxf(__builtin_fmaf(-mean, mean, sq * (1.0f / D_)), 0.f);
  const float rs = __builtin_amdgcn_rcpf(rbf(__builtin_amdgcn_sqrtf(rbf(rbf(var) + 1e-5f))));
  return f32x2{rs, -rbf(mean) * rs};
}

constexpr u64 win_mask(int r, int lo, int hi) {
  u64 m = 0;
  for (int l = 0; l < 64; ++l) { const int d = 4 * (l >> 4) + r - (l & 15); if (d >= lo && d <= hi) m |= 1ull << l; }
  return m;
}
template <int R> __device__ __forceinline__ void win_bias(unsigned ninf, float& a, float& b) {
  constexpr u64 ma = ~win_mask(R, 9, 64), mb = ~win_mask(R, -7, 0);
  static_assert((unsigned)ma == ~0u, "mask");
  asm volatile("s_mov_b32 vcc_lo, %3\n\ts_mov_b32 vcc_hi, %4\n\tv_cndmask_b32 %1, 0, %2, vcc\n\t"
               "s_mov_b32 vcc_lo, -1\n\ts_mov_b32 vcc_hi, %5\n\tv_cndmask_b32 %0, 0, %2, vcc"
               : "=&v"(a), "=&v"(b) : "v"(ninf), "i"((int)mb), "i"((int)(mb >> 32)), "i"((int)(ma >> 32)) : "vcc");
}

template <class Pre>
__device__ __forceinline__ void k1_consumer(LAS char* lds, int lane, int wv, bool l0, int tq, const char* ob, Gate& gate, Pre&& pre) {
  u64 qm = l0 ? 0x0001000100010001ull : 0ull;
  asm volatile("" : "+v"(lane), "+s"(ob), "+s"(qm));
  const int p = wv & 1;
  f32x4 acc[6][4], hk, hv, hq;
  FragAddr<KM, 16, 3, false, 4> pa[2];
  pa[0].init(lds + L_A, wv, lane); pa[1].init(lds + L_A, wv ^ 1, lane);
  FragAddr<KC, 16, 4> qa, ha;
  qa.init(lds + K1_QOFF, 1 + 4 * p, lane);
#pragma unroll
  for (int t = 0; t < 2; ++t) { ha.a[t] = (qa.a[t] - (1 + 4 * p) * 2048) ^ ((1 - p) * 1024); asm volatile("" : "+v"(ha.a[t])); }
  struct Fr { bf16x8 p[6], q[4], h; };
  auto rd = [&](Fr& f, int slot, int t) {
    f.q[0] = qa.read(slot * K1_QSTG, 0, t);
#pragma unroll
    for (int i = 0; i < 6; ++i) f.p[i] = pa[i & 1].read(slot * K1_PSTG, i >> 1, t);
#pragma unroll
    for (int j = 1; j < 4; ++j) f.q[j] = qa.read(slot * K1_QSTG, j, t);
    f.h = ha.read(slot * K1_QSTG, 0, t);
  };
  auto m1 = [&](const Fr& f, int n) {
    const int j = n / 6, i = (j & 1) ? 5 - n % 6 : n % 6;
    acc[i][j] = i < 4 ? __builtin_amdgcn_mfma_f32_16x16x32_bf16(f.p[i], f.q[j], acc[i][j], 0, 0, 0) : __builtin_amdgcn_mfma_f32_16x16x32_bf16(f.q[j], f.p[i], acc[i][j], 0, 0, 0);
  };
  auto mh = [&](const Fr& f) {
    hk = __builtin_amdgcn_mfma_f32_16x16x32_bf16(f.p[2], f.h, hk, 0, 0, 0);
    hv = __builtin_amdgcn_mfma_f32_16x16x32_bf16(f.h, f.p[4], hv, 0, 0, 0);
  };
  auto mm = [&](const Fr& f) {
#pragma unroll
    for (int n = 0; n < HQP; ++n) m1(f, n);
    interleave<HQP, 1>();
    SCHED0;
    if (l0) hq = __builtin_amdgcn_mfma_f32_16x16x32_bf16(f.p[0], f.h, hq, 0, 0, 0);
    SCHED0;
#pragma unroll
    for (int n = HQP; n < 24; ++n) m1(f, n);
    mh(f);
    SCHED0;
  };
  const f32x4 z4 = {0.f, 0.f, 0.f, 0.f};
#pragma unroll
  for (int i = 0; i < 6; ++i)
#pragma unroll
    for (int j = 0; j < 4; ++j) { acc[i][j] = z4; asm volatile("" : "+v"(acc[i][j])); }
  hk = z4; hv = z4; hq = z4;
  asm volatile("" : "+v"(hk), "+v"(hv), "+v"(hq));
  Fr f0, f1;
  gate.pass();
  pre();
  BARRIER;
  rd(f0, 0, 0);
  SCHED0;
  gate.done(); WAITL0; SCHED0;
#pragma unroll 1
  for (int g = 0; g < 5; ++g)
    static_for<3>([&](auto uc) {
      constexpr int u = decltype(uc)::value;
      rd(f1, u, 1);
      mm(f0);
      if constexpr (u == 1) WAITV(0);
      WAITL0; BARRIER; SCHED0;
      rd(f0, (u + 1) % 3, 0);
      mm(f1);
      WAITL0;
      SCHED0;
    });
  constexpr int L_HX = L_B + 57344;
  asm volatile("" : "+v"(lane));
  const int fr = lane & 15, fq16 = lane & 48;
  unsigned dw = L_HX + wv * 2048 + (lane & 63) * 16, cq0 = L_CSQ + wv * 64 + fq16;
  f1.h = ha.read(0, 0, 1);
#pragma unroll
  for (int i = 0; i < 5; ++i) { const int k = (2 * i + 2) % 6 + (i >= 3); f1.p[k] = pa[k & 1].read(0, k >> 1, 1); }
  mh(f0);
#pragma unroll
  for (int n = 0; n < 9; ++n) m1(f0, n);
#pragma unroll
  for (int n = 0; n < 11; ++n) { __builtin_amdgcn_sched_group_barrier(0x8, 1, 0); __builtin_amdgcn_sched_group_barrier(0x100, 1, 0); __builtin_amdgcn_sched_group_barrier(0x2, 1, 0); }
  asm volatile("" : "+v"(dw), "+v"(cq0));
  SCHED0;
  mh(f1);
  if (l0) hq = __builtin_amdgcn_mfma_f32_16x16x32_bf16(f1.p[0], f1.h, __builtin_amdgcn_mfma_f32_16x16x32_bf16(f0.p[0], f0.h, hq, 0, 0, 0), 0, 0, 0);
  SCHED0;
  f1.p[1] = pa[1].read(0, 0, 1);
  f1.q[0] = qa.read(0, 0, 1);
#pragma unroll
  for (int n = 9; n < 12; ++n) m1(f0, n);
  interleave<3, 1>();
  SCHED0;
  *(LAS f32x4*)(size_t)dw = hk;
  m1(f0, 12); m1(f0, 13);
  SCHED0;
  *(LAS f32x4*)((LAS char*)(size_t)dw + 1024) = hv;
  m1(f0, 14); m1(f0, 15);
  SCHED0;
  asm volatile("s_mov_b64 exec, %2\n\tds_write_b128 %0, %1\n\ts_mov_b64 exec, -1" ::"v"(cq0), "v"(hq), "s"(qm) : "memory");
  SCHED0;
  unsigned a2 = K1_ST + (16 + 64 * p) * 8 + fr * 8, a4 = L_RS + (16 + 64 * p) * 4 + fq16;
  unsigned h2 = K1_ST + ((fr ^ ((1 - p) * 8)) * 8), h4 = L_RS + (fq16 ^ ((1 - p) * 32));
  unsigned ma = L_MSK + (lane & 63) * 16, ck0 = L_HX + wv * 2048 + fq16 * 16, cv0 = L_HX + 1024 + wv * 2048 + fr * 16;
#pragma unroll
  for (int j = 1; j < 4; ++j) f1.q[j] = qa.read(0, j, 1);
#pragma unroll
  for (int n = 16; n < 24; ++n) m1(f0, n);
#pragma unroll
  for (int n = 0; n < 8; ++n) { __builtin_amdgcn_sched_group_barrier(0x8, 1, 0); __builtin_amdgcn_sched_group_barrier(0x100, 1, 0); __builtin_amdgcn_sched_group_barrier(0x2, 2, 0); }
  asm volatile("" : "+v"(a2), "+v"(a4), "+v"(h2), "+v"(h4), "+v"(ma), "+v"(ck0), "+v"(cv0));
  SCHED0; WAITL0; BARRIER; SCHED0;
  unsigned oo[4];
  {
    const bool hole = ((p + (fr & 14)) & 7) == 3;
    oo[0] = phys<64>(p + (fr & 14)) * 128 + (fr & 1) * 64 + (((lane & 16) * 2) ^ (p * 32)) + ((lane & 32) >> 1);
    const unsigned e1 = hole ? 8 : 11, e2 = hole ? 10 : 12;
    oo[1] = oo[0] + (1u << e1); oo[2] = oo[0] + (1u << e2); oo[3] = oo[2] + (1u << e1);
  }
  f32x2 s2[5];
  f32x4 rs[5], mr[5], cs[4], pk_, pv_, bA, bB, b0;
  float cv[2];
  bA = *(const LAS f32x4*)(size_t)ma;
  cs[0] = *(const LAS f32x4*)(size_t)cq0; cs[1] = *(const LAS f32x4*)(size_t)(cq0 ^ 64);
  cs[2] = *(const LAS f32x4*)(size_t)ck0; cs[3] = *(const LAS f32x4*)(size_t)(ck0 ^ 2048);
#pragma unroll
  for (int j = 0; j < 5; ++j) s2[j] = j < 4 ? *(const LAS f32x2*)((LAS char*)(size_t)a2 + j * 128) : *(const LAS f32x2*)(size_t)h2;
  pk_ = *(const LAS f32x4*)(size_t)(dw ^ (2048 + 128)); pv_ = *(const LAS f32x4*)(size_t)(dw ^ (2048 + 1024 + 512));
  bB = *(const LAS f32x4*)((LAS char*)(size_t)ma + 1024);
  cv[0] = *(const LAS float*)(size_t)cv0; cv[1] = *(const LAS float*)(size_t)(cv0 ^ 2048);
#pragma unroll
  for (int j = 0; j < 5; ++j) {
    rs[j] = j < 4 ? *(const LAS f32x4*)((LAS char*)(size_t)a4 + j * 64) : *(const LAS f32x4*)(size_t)h4;
    mr[j] = j < 4 ? *(const LAS f32x4*)((LAS char*)(size_t)a4 + j * 64 + 576) : *(const LAS f32x4*)((LAS char*)(size_t)h4 + 576);
  }
  const unsigned nm = (unsigned)((tq - 1) >> 31) & 0xFF61B1E6u;
  int o1 = OPQ(0x3F803F80);
  bf16x8 ones = __builtin_bit_cast(bf16x8, u32x4{(unsigned)o1, (unsigned)o1, (unsigned)o1, (unsigned)o1});
#pragma unroll
  for (int n = 0; n < 24; ++n) m1(f1, n);
#pragma unroll
  for (int n = 0; n < 24; ++n) { __builtin_amdgcn_sched_group_barrier(0x8, 1, 0); __builtin_amdgcn_sched_group_barrier(0x2, 1, 0); __builtin_amdgcn_sched_group_barrier(0x100, 1, 0); __builtin_amdgcn_sched_group_barrier(0x2, 1, 0); }
  asm volatile("" : "+v"(oo[0]), "+v"(oo[1]), "+v"(oo[2]), "+v"(oo[3]), "+v"(ones));
  SCHED0;
  __builtin_amdgcn_s_waitcnt(0xCA7F);
  ALIGN8;
  auto qk = [&](const f32x4& a, const f32x4& c, const f32x2& s) { const f32x4 v = a * s[0] + c * s[1]; return u32x2{pk2(v[0], v[1]), pk2(v[2], v[3])}; };
  auto cat = [](const u32x2& a, const u32x2& b) { return __builtin_bit_cast(bf16x8, u32x4{a[0], a[1], b[0], b[1]}); };
  bf16x8 kf[5], qf[4];
#pragma unroll
  for (int j = 0; j < 4; ++j) {
    kf[j + 1] = cat(qk(acc[2][j], cs[2], s2[j]), qk(acc[3][j], cs[3], s2[j]));
    qf[j] = cat(qk(acc[0][j], cs[0], s2[j]), qk(acc[1][j], cs[1], s2[j]));
  }
  kf[0] = cat(qk(hk, cs[2], s2[4]), qk(pk_, cs[3], s2[4]));
  SCHED0;
  const float scale = 0.17677669529663687f, l2e = 1.4426950408889634f;
  f32x2 l[4][4];
  float mx[4];
#pragma unroll
  for (int r = 0; r < 4; ++r) b0[r] = __uint_as_float(__float_as_uint(bA[r]) | nm);
#pragma unroll
  for (int jj = 0; jj < 4; ++jj) {
    const int j = (jj + 1) & 3;
    const f32x4 sA = __builtin_amdgcn_mfma_f32_16x16x32_bf16(kf[j], qf[j], j == 0 ? b0 : bA, 0, 0, 0);
    const f32x4 sB = __builtin_amdgcn_mfma_f32_16x16x32_bf16(kf[j + 1], qf[j], bB, 0, 0, 0);
    l[j][0] = f32x2{sA[0], sA[1]}; l[j][1] = f32x2{sA[2], sA[3]}; l[j][2] = f32x2{sB[0], sB[1]}; l[j][3] = f32x2{sB[2], sB[3]};
    mx[j] = max3(max3(l[j][0][0], l[j][0][1], l[j][1][0]), max3(l[j][1][1], l[j][2][0], l[j][2][1]), max2(l[j][3][0], l[j][3][1]));
  }
  __builtin_amdgcn_sched_group_barrier(0x8, 2, 0);
#pragma unroll
  for (int n = 0; n < 6; ++n) { __builtin_amdgcn_sched_group_barrier(0x2, 2, 0); __builtin_amdgcn_sched_group_barrier(0x8, 1, 0); }
  SCHED0;
  WAITL0;
  ALIGN8;
  u32x12 vp[2];
  auto vv = [&](int e, int k, const f32x4& a, int j) { const f32x4 v = a * rs[j] + mr[j] * cv[e]; vp[e][2 * k] = pk2(v[0], v[1]); vp[e][2 * k + 1] = pk2(v[2], v[3]); };
  asm volatile("" : "=v"(vp[0]), "=v"(vp[1]));
  vv(0, 0, hv, 4); vv(1, 0, pv_, 4);
#pragma unroll
  for (int j = 0; j < 4; ++j) { vv(0, j + 1, acc[4][j], j); vv(1, j + 1, acc[5][j], j); }
  {
    auto mx2 = [](auto r) { return __float_as_uint(max2(__uint_as_float(r[0]), __uint_as_float(r[1]))); };
    const unsigned a = mx2(__builtin_amdgcn_permlane16_swap(__float_as_uint(mx[0]), __float_as_uint(mx[1]), false, false));
    const unsigned b = mx2(__builtin_amdgcn_permlane16_swap(__float_as_uint(mx[2]), __float_as_uint(mx[3]), false, false));
    const unsigned c = __float_as_uint(__uint_as_float(mx2(__builtin_amdgcn_permlane32_swap(a, b, false, false))) * (-scale * l2e));
    const auto d = __builtin_amdgcn_permlane32_swap(c, c, false, false);
    const auto e0 = __builtin_amdgcn_permlane16_swap(d[0], d[0], false, false), e1 = __builtin_amdgcn_permlane16_swap(d[1], d[1], false, false);
    mx[0] = __uint_as_float(e0[0]); mx[1] = __uint_as_float(e0[1]); mx[2] = __uint_as_float(e1[0]); mx[3] = __uint_as_float(e1[1]);
  }
#pragma unroll
  for (int j = 0; j < 4; ++j)
#pragma unroll
    for (int e = 0; e < 4; ++e) l[j][e] = __builtin_elementwise_fma(l[j][e], f32x2{scale * l2e, scale * l2e}, f32x2{mx[j], mx[j]});
  asm volatile("" : "+v"(vp[0]), "+v"(vp[1]));
  SCHED0;
  f32x4 oA[4], oB[4], zs[4];
  bf16x8 pf[4];
  auto ex = [&](int j) {
    u32x4 pp;
#pragma unroll
    for (int e = 0; e < 4; ++e) pp[e] = pk2(__builtin_amdgcn_exp2f(l[j][e][0]), __builtin_amdgcn_exp2f(l[j][e][1]));
    pf[j] = __builtin_bit_cast(bf16x8, pp);
  };
  auto pv = [&](auto jc) {
    constexpr int j = decltype(jc)::value;
    zs[j] = __builtin_amdgcn_mfma_f32_16x16x32_bf16(ones, pf[j], z4, 0, 0, 0);
    oA[j] = __builtin_amdgcn_mfma_f32_16x16x32_bf16(__builtin_bit_cast(bf16x8, __builtin_shufflevector(vp[0], vp[0], 2 * j, 2 * j + 1, 2 * j + 2, 2 * j + 3)), pf[j], z4, 0, 0, 0);
    oB[j] = __builtin_amdgcn_mfma_f32_16x16x32_bf16(__builtin_bit_cast(bf16x8, __builtin_shufflevector(vp[1], vp[1], 2 * j, 2 * j + 1, 2 * j + 2, 2 * j + 3)), pf[j], z4, 0, 0, 0);
  };
  auto out = [&](int j) {
    const float iz = __builtin_amdgcn_rcpf(zs[j][0]);
    asm volatile("" ::"v"(zs[j]));
    const f32x4 a = oA[j] * iz, b = oB[j] * iz;
    const auto x0 = __builtin_amdgcn_permlane16_swap(pk2(a[0], a[1]), pk2(b[0], b[1]), false, false);
    const auto x1 = __builtin_amdgcn_permlane16_swap(pk2(a[2], a[3]), pk2(b[2], b[3]), false, false);
    const u32x4 ov = {x0[0], x1[0], x0[1], x1[1]};
    asm volatile("global_store_dwordx4 %0, %1, %2 sc1\n\ts_nop 1" ::"v"(oo[j]), "v"(ov), "s"(ob) : "memory");
    SCHED0;
  };
  ex(0);
  SCHED0;
  static_for<3>([&](auto jc) {
    constexpr int j = decltype(jc)::value;
    pv(jc);
    ex(j + 1);
#pragma unroll
    for (int n = 0; n < 3; ++n) { __builtin_amdgcn_sched_group_barrier(0x8, 1, 0); __builtin_amdgcn_sched_group_barrier(0x400, 1, 0); }
    SCHED0;
    if constexpr (j == 2) { pv(std::integral_constant<int, 3>{}); SCHED0; }
    out(j);
  });
  out(3);
}

constexpr int L_FLG = L_A + 65536;
template <int OFF> __device__ __forceinline__ void fetch_flags(const void* p) {
  asm volatile("s_mov_b32 m0, %1\n\ts_nop 0\n\tglobal_load_lds_dwordx4 %0, off sc1" ::"v"(p), "i"(L_FLG + OFF) : "memory", "m0");
}
__device__ __forceinline__ bool flags_set(unsigned a, u64 tag) {
  u32x4 v;
  asm volatile("ds_read_b128 %0, %1\n\ts_waitcnt lgkmcnt(0)" : "=v"(v) : "v"(a) : "memory");
  const unsigned lo = (unsigned)tag, hi = (unsigned)(tag >> 32);
  return __builtin_amdgcn_ballot_w64((((v[0] ^ lo) | (v[1] ^ hi)) | ((v[2] ^ lo) | (v[3] ^ hi))) != 0) == 0;
}
template <int N>
__device__ __forceinline__ void wait_tags(const u64* p, u64 tag) {
#pragma unroll 1
  for (int spin = 0; spin < (1 << 16); ++spin) {
    bool ok = true;
#pragma unroll
    for (int i = 0; i < N; ++i) ok &= __hip_atomic_load(p + i, __ATOMIC_RELAXED, __HIP_MEMORY_SCOPE_AGENT) == tag;
    if (__all(ok)) break;
    __builtin_amdgcn_s_sleep(8);
  }
  CFENCE;
}
__device__ __forceinline__ const u64* first_flags(const u64* ctl0, int lane) {
  OPAQUE_BID;
  return ctl0 + C_OCNT + ((lane >> 2) * NR_O + (BID & 3) % NR_O) * RS1 + (lane & 3) * 4;
}
__device__ __forceinline__ void wait_first_phase(LAS char* lds, const u64* ctl0, int lane) { wait_tags<4>(first_flags(ctl0, lane), TAG); }
__device__ __forceinline__ const u64* x_flag(const u64* ctl, int tq, int tp, int lane) {
  return ctl + C_XCNT + (max(tq - ((lane >> 4) & 1), 0) * NR_X + ((tp + ((lane >> 4) & 1) * (NR_X / 2)) % NR_X)) * RS1 + (lane & 15);
}

__device__ __forceinline__ f32x2 uadd(f32x2 a, f32x2 b) {
  float x, y;
  asm("v_add_f32 %0, %2, %4\n\tv_add_f32 %1, %3, %5" : "=&v"(x), "=v"(y) : "v"(a[0]), "v"(a[1]), "v"(b[0]), "v"(b[1]));
  return f32x2{x, y};
}
constexpr size_t MB = 1 << 20;
constexpr size_t W_H = 4 * MB, W_O = 10 * MB, W_U = 16 * MB, W_PART = 26 * MB;
constexpr size_t W_XCH = 30 * MB, W_CTL = 31 * MB, W_PST = 33 * MB, W_EPOCH = 34 * MB, W_CW = 35 * MB;

#define SF1 5
#define SCW 13
#define SCO (SCW < 12 ? SCW + 3 : 99)
template <bool ISQ>
struct K1Hook : NoHook {
  static constexpr int AUX = ISQ ? 16 : 0;
  Stream<KM, 16, 128, true> so;
  static constexpr bool PREALL = true, PIN = ISQ, UNROLL = true, COLD = true;
  __device__ __forceinline__ bool cold() const { return l0; }
  static constexpr int SG = SF1 + 3;
  static constexpr int NG = ISQ ? 4 : 1;
  bool l0; int tq, tp, lw, lane; LAS char* lds;
  const u32x2* pstat; u64* ctl; u32x4* xch;
  const u16* wo; const u16* w1; const u16* w2; const u16* wq;
  const u32x2* sp; unsigned sa; const char* tw;
  f32x2 g[4 * NG];
  u64 fx; const u64* fp;
  __device__ __forceinline__ void ready() {
    if constexpr (ISQ) { if (!l0) { BARRIER; BARRIER; } }
  }
  __device__ __forceinline__ void first() {
    asm volatile("" : "+v"(lane));
    if constexpr (ISQ) {
      OPAQUE_BID;
      const int b = BID, l24 = lane % 24, r = l24 / 12, c = l24 - r * 12;
      tw = (const char*)wq + (256 + (((b >> 2) & 1) * 32 + (b >> 3)) * 4 + (lw - 2) * 2 + r) * 6144 + (c >> 2) * 2048 + ((b & 3) * 4 + (c & 3)) * 128;
      asm volatile("" : "+v"(tw));
      asm volatile("global_load_dwordx2 %0, %1, %2 sc1" : "=&v"(fx) : "v"(vconst<0>()), "s"((const char*)pstat + (W_EPOCH - W_PST)) : "memory");
    }
  }
  static constexpr int ng(int s) { return (s >= SG && s < SG + 4) ? NG : 0; }
    static constexpr int nt(int s) { return ISQ && s == 0 ? 3 : 0; }
  static constexpr int gs(int s) { return (!ISQ && s >= G0 && (s - G0) % GD == 0 && (s - G0) / GD < GN) ? (s - G0) / GD : -1; }
  int grp, duty; unsigned go;
  static constexpr int lx(int s) { return nt(s) + (!ISQ && s == FWS ? 1 : 0) + (s == WC0 || s == WC0 + 3 ? 2 : 0); }
  template <int S, int N> __device__ __forceinline__ void wait() {
    constexpr int A = N + (S < 13 ? ng(S - 1) + ng(S - 2) : 0), X = lx(S - 1) + lx(S - 2);
    if constexpr (S < 13 && (gs(S - 1) >= 0 || gs(S - 2) >= 0)) {
      if (grp == gs(S - 1) || grp == gs(S - 2)) waitv<A + X + 4>();
      else if (X > 0 && l0) waitv<A + X>();
      else waitv<A>();
    } else if constexpr (S == SCW + 1 || S == SCW + 2) {
      static_assert(X == 0, "copy step");
      if (duty >= 0) waitv<A + 4>(); else waitv<A>();
    } else {
      if (X > 0 && l0) waitv<A + X>(); else waitv<A>();
    }
  }
  template <int I> __device__ __forceinline__ f32x2 sum4() {
    asm volatile("" : "+v"(g[I]), "+v"(g[I + 1]), "+v"(g[I + 2]), "+v"(g[I + 3]));
    return uadd(uadd(g[I], g[I + 1]), uadd(g[I + 2], g[I + 3]));
  }
  template <int S> __device__ __forceinline__ void stepc() {
    if constexpr (ISQ && S == SF1) {
      fp = x_flag(ctl, tq, tp, lane & 31);
      asm volatile("global_load_dwordx2 %0, %1, off sc1" : "=&v"(fx) : "v"(fp) : "memory");
    }
    if constexpr (nt(S) > 0) {
      if (l0) {
#pragma unroll
        for (int i = 0; i < 3; ++i) touch_line(tw + i * 256 * 6144, (unsigned)(size_t)lds + L_SCR);
      }
    }
    if constexpr (!ISQ && S == 1) {
      OPAQUE_BID;
      int kh2, tq2, tp2;
      k4_map(BID, kh2, tp2, tq2);
      so.init(Operand{cw + 448 * CW_BLK / 2, D_, tp2 * 128, 64, 256 * CW_BLK, 0, false, nullptr, 0}, lw, lane);
      so.pin();
    }
    if constexpr (S == WC0) {
      if (l0) {
        const auto r = __builtin_amdgcn_make_buffer_rsrc((void*)wo, 0, D_ * D_ * 2, 0x00020000);
        const unsigned v = (lane >> 3) * (D_ * 2) + (lane & 7) * 16;
        const int o = ((tq * 64 + lw * 16) * D_ + tp * 64) * 2;
#if defined(__HIP_DEVICE_COMPILE__)
        __builtin_amdgcn_raw_ptr_buffer_load_lds(r, (LAS void*)(lds + L_C + lw * 4096), 16, v, o, 0, AC);
        __builtin_amdgcn_raw_ptr_buffer_load_lds(r, (LAS void*)(lds + L_C + lw * 4096 + 1024), 16, v, o + 8 * D_ * 2, 0, AC);
#endif
      }
    }
    if constexpr (S == WC0 + 3) {
      if (l0) {
        const unsigned d = phys<64>(lw * 16 + (lane >> 3)) * 128 + (lane & 7) * 16, d2 = d + pow2sel((lane >> 3) == 3, 7, 10);
        u32x4 a, b;
        asm volatile("ds_read_b128 %0, %2\n\tds_read_b128 %1, %2 offset:1024\n\ts_waitcnt lgkmcnt(0)" : "=&v"(a), "=&v"(b) : "v"(L_C + lw * 4096 + lane * 16) : "memory");
        asm volatile("s_nop 4\n\tglobal_store_dwordx4 %0, %2, %4 sc1\n\tglobal_store_dwordx4 %1, %3, %4 sc1\n\ts_nop 1"
                     ::"v"(d), "v"(d2), "v"(a), "v"(b), "s"((char*)cw + (448 + tp * 16 + tq) * CW_BLK) : "memory");
      }
    }
    if constexpr (ISQ && S == WC0 + 6) { if (l0 && lw == 2 && lane < NR_X) flag_set(ctl + C_XCNT + (tq * NR_X + lane) * RS1 + 16 + tp, tag_ld()); }
    if constexpr (!ISQ && S == 10) {
      OPAQUE_BID;
      int kh2, tq2, tp2;
      k4_map(BID, kh2, tp2, tq2);
      fp = ctl + C_XCNT + ((lane & 15) * NR_X + (BID & 7)) * RS1 + 16 + tp2 * 2 + ((lane >> 4) & 1);
      asm volatile("global_load_dwordx2 %0, %1, off sc1" : "=&v"(fx) : "v"(fp) : "memory");
    }
    if constexpr (!ISQ && S == 12) {
      asm volatile("" : "+v"(fx));
      if (l0 && lw == 1) { const u64 t = tag_ld(); if (!__all(fx == t)) wait_tags<1>(fp, t); }
    }
    if constexpr (S == (ISQ ? 4 : 2)) {
      asm volatile("" : "+v"(lane));
      const int trow = ISQ ? (lw - 2) * 64 + lane : 128 + (lane & 15);
      const int row = max(tq * 128 - 16 + trow, 0), line = (trow >> 1) + (trow < 16 ? (trow & 1) * 8 : 8 + (trow & 1) * 64);
      sp = pstat + (size_t)(row >> 5) * 512 + (row & 31) + (ISQ ? 0 : (lane >> 4) * 128);
      sa = (unsigned)(size_t)lds + K1_ST + line * 8;
      asm volatile("" : "+v"(sp), "+v"(sa));
    }
    if constexpr (!ISQ && S == ARMS) {
      if (lw == 0) { const unsigned m = vconst<~0u>(); st16(xch + ((tq * 4 + (lane >> 4)) * 16 + tp) * 16 + (lane & 15), u32x4{m, m, m, m}); }
    }
    if constexpr (!ISQ && S == FWS) {
      if (l0) {
        static_assert(NR_O == 8 && NR_H == 8 && NR_U == 8 && NR_X == 8 && (RS1 == RS2 || RS2 == 2 * RS1), "flag lines");
        const int i = opq_bid() * 8 + min(lane, 7);
        constexpr int N1 = 1024 + (C_END - C_UCNT) / RS1;
        const int wd = i < 1024 ? i * RSP : i < N1 ? C_UCNT + (i - 1024) * RS1 : i < N1 + 64 ? C_HCNT + 16 + (i - N1) * RS2 : C_XCNT + 16 + min(i - N1 - 64, 127) * RS1;
        touch_line(ctl + lw * C_WORDS + wd, L_SCR);
      }
    }
    if constexpr (S == 0) {
      const int c = (3 - (int)((size_t)wq >> 7)) & 7;
      duty = l0 && (tp & 7) != c ? tq * 14 + tp - (tp > c) - (tp > c + 8) : -1;
      asm volatile("" : "+s"(duty));
    }
    if constexpr (!ISQ && S == 0) {
      grp = duty >= 0 ? (int)((opq_bid() >> 3) % GN) : 99;
      const unsigned line = (duty * 2 + lw) * 64 + lane;
      go = ((((unsigned)((size_t)w1 >> 7) + line) & 7) == 3 ? line ^ 1 : line) * 128;
      asm volatile("" : "+v"(go));
    }
    if constexpr (gs(S) >= 0) {
      if (grp == gs(S)) {
        const char* b = (const char*)w1;
        asm volatile("s_mov_b32 m0, %5\n\ts_nop 0\n\tglobal_load_lds_dword %0, %1\n\tglobal_load_lds_dword %0, %2\n\tglobal_load_lds_dword %0, %3\n\tglobal_load_lds_dword %0, %4"
                     ::"v"(go), "s"(b), "s"(b + 28672 * 128), "s"(b + 2 * 28672 * 128), "s"(b + 3 * 28672 * 128), "i"(L_SCR) : "memory", "m0");
      }
    }
    if constexpr (ISQ && S == 1) {
      asm volatile("" : "+v"(fx));
      if (l0 && lw == 2) {
        const u64 tag = fx + 1;
        if (lane < NR_X) flag_set(ctl + C_XCNT + (tq * NR_X + lane) * RS1 + tp, tag);
        asm volatile("ds_write_b64 %0, %1\n\ts_waitcnt lgkmcnt(0)" ::"v"(vconst<L_TAG>()), "v"(tag) : "memory");
      }
    }
    if constexpr (S == SCO) copy_out();
    if constexpr (S == SCW) {
      if (duty >= 0) {
        const int q = (duty * 4 + lw) * 2, r8 = lane >> 3, c16 = (lane & 7) * 16;
        const auto r1 = __builtin_amdgcn_make_buffer_rsrc((void*)w1, 0, S_ * F_ * 2, 0x00020000);
        const auto r2 = __builtin_amdgcn_make_buffer_rsrc((void*)w2, 0, S_ * F_ * 2, 0x00020000);
        const int s1 = ((q & 255) * 8 * F_ + (((3 - (int)((size_t)w1 >> 7)) & 7) + 8 * (q >> 8)) * 64) * 2;
        const int s2 = ((q >> 2) * 8 * S_ + (((3 - (int)((size_t)w2 >> 7)) & 7) + 8 * (q & 3)) * 64) * 2;
        const unsigned v1 = r8 * (F_ * 2) + c16, v2 = r8 * (S_ * 2) + c16;
        LAS char* d = lds + L_C + lw * 4096;
#if defined(__HIP_DEVICE_COMPILE__)
        __builtin_amdgcn_raw_ptr_buffer_load_lds(r1, (LAS void*)d, 16, v1, s1, 0, AC);
        __builtin_amdgcn_raw_ptr_buffer_load_lds(r1, (LAS void*)(d + 1024), 16, v1, s1 + 8 * F_ * 2, 0, AC);
        __builtin_amdgcn_raw_ptr_buffer_load_lds(r2, (LAS void*)(d + 2048), 16, v2, s2, 0, AC);
        __builtin_amdgcn_raw_ptr_buffer_load_lds(r2, (LAS void*)(d + 3072), 16, v2, s2 + 1024, 0, AC);
#endif
      }
    }
    if constexpr (ISQ && S == SF1 + 2) {
      asm volatile("" : "+v"(fx));
      if (lw == 3) { const u64 t = tag_ld(); if (!__all(fx == t)) wait_tags<1>(fp, t); }
    }
    if constexpr (ng(S) == 4) {
      constexpr int i = (S - SG) * 4;
      asm volatile(
          "global_load_dwordx2 %0, %4, off offset:%5 sc1\n\tglobal_load_dwordx2 %1, %4, off offset:%5+256 sc1\n\t"
          "global_load_dwordx2 %2, %4, off offset:%5+512 sc1\n\tglobal_load_dwordx2 %3, %4, off offset:%5+768 sc1"
          : "=&v"(g[i]), "=&v"(g[i + 1]), "=&v"(g[i + 2]), "=&v"(g[i + 3]) : "v"(sp), "i"(i * 256) : "memory");
    }
    if constexpr (ng(S) == 1) asm volatile("global_load_dwordx2 %0, %1, off offset:%2 sc1" : "=&v"(g[S - SG]) : "v"(sp), "i"((S - SG) * 256) : "memory");
    if constexpr (ISQ) {
      if constexpr (S == SG + 3) { g[0] = sum4<0>(); asm volatile("" : "+v"(g[0])); }
      if constexpr (S == SG + 4) { g[0] = uadd(g[0], sum4<4>()); asm volatile("" : "+v"(g[0])); }
      if constexpr (S == SG + 5) { g[0] = uadd(g[0], uadd(sum4<8>(), sum4<12>())); asm volatile("" : "+v"(g[0])); }
    } else {
      if constexpr (S == SG + 5) { g[0] = sum4<0>(); float a = rows_sum(g[0][0]); asm volatile("" : "+v"(a)); g[0] = f32x2{a, rows_sum(g[0][1])}; asm volatile("" : "+v"(g[0])); }
    }
    if constexpr (S == SG + 6) {
      asm volatile("" : "+v"(g[0]));
      const f32x2 c = ln_coef(g[0][0], g[0][1]);
      asm volatile("ds_write_b64 %0, %1\n\tds_write2_b32 %2, %3, %4 offset1:144\n\ts_waitcnt lgkmcnt(0)" ::"v"(sa), "v"(c), "v"((sa >> 1) + (L_RS - K1_ST / 2)), "v"(c[0]), "v"(c[1]) : "memory");
    }
  }
  template <int T> __device__ __forceinline__ void next() {
    stepc<13 + T>();
    SCHED0;
    if constexpr (!ISQ) {
#pragma unroll
      for (int i = 0; i < (T == 3 ? 4 : 0); ++i) so.template stage<16>(lds + L_A + i * 16384 + lw * 1024);
      SCHED0;
    }
  }
  __device__ __forceinline__ void finish() {
    CFENCE;
    __builtin_amdgcn_s_waitcnt(0x0F70);
    CFENCE;
    if (SCO == 99) copy_out();
  }
  u16* cw;
  __device__ __forceinline__ void copy_out() {
    if (duty >= 0) {
      const int q = (OPQ(duty) * 4 + lw) * 2, r8 = lane >> 3, c16 = (lane & 7) * 16;
      const int k1 = (q & 7) * 8 + r8, k2 = ((q >> 3) & 7) * 8 + r8;
      const unsigned cd1 = (unsigned)((q >> 3) * CW_BLK + phys<64>(k1) * 128 + c16);
      const unsigned cd2 = (unsigned)((224 + (q & 3) * 56 + (q >> 6) * 2 + ((q >> 2) & 1)) * CW_BLK + phys<64>(k2) * 128 + c16);
      const unsigned d1 = pow2sel(r8 == 3, 7, 10);
      u32x4 cv[4];
      asm volatile("ds_read_b128 %0, %4\n\tds_read_b128 %1, %4 offset:1024\n\tds_read_b128 %2, %4 offset:2048\n\tds_read_b128 %3, %4 offset:3072\n\ts_waitcnt lgkmcnt(0)"
                   : "=&v"(cv[0]), "=&v"(cv[1]), "=&v"(cv[2]), "=&v"(cv[3]) : "v"(L_C + lw * 4096 + lane * 16) : "memory");
      asm volatile("s_nop 4\n\tglobal_store_dwordx4 %0, %4, %8 sc1\n\tglobal_store_dwordx4 %1, %5, %8 sc1\n\tglobal_store_dwordx4 %2, %6, %8 sc1\n\tglobal_store_dwordx4 %3, %7, %8 sc1\n\ts_nop 1"
                   ::"v"(cd1), "v"(cd1 + d1), "v"(cd2), "v"(cd2 + 56 * CW_BLK), "v"(cv[0]), "v"(cv[1]), "v"(cv[2]), "v"(cv[3]), "s"(cw) : "memory");
    }
  }
};
static_assert(SF1 + 3 + 5 == 13 && SCW != SF1 && SCO != SF1 && SCW != 11 && SCO != 11, "the fetches end two steps before the tail; statistics in LDS before the last barrier");

constexpr int K2_NSQ = 4, K2_NSP = 4;
__device__ __forceinline__ void k1_body(LAS char* lds, int wid, bool l0, const u16* __restrict__ w, const u16* x, u16* o, u32x4* xch, u64* ctl,
                                        u32x2* pstat, const u16* __restrict__ wo, const u16* __restrict__ w1,
                                        const u16* __restrict__ w2, u16* __restrict__ cw) {
  LAS char* pbuf = lds + L_A;
  LAS char* qbuf = lds + K1_QOFF;
  OPAQUE_BID;
  const int lane = lane_id();
  int tq, tp;
  tile_map<16, 16, 8, 4>(BID, tq, tp);
  const int s0 = tq * 128;
  if (wid >= 4) {
    auto fill = [&](auto& h) {
      h.l0 = l0; h.tq = tq; h.tp = tp; h.lw = wid - 4; h.lane = lane; h.lds = lds; h.pstat = pstat; h.ctl = ctl; h.xch = xch;
      h.wo = wo; h.w1 = w1; h.w2 = w2; h.wq = w; h.cw = cw;
    };
    if (wid >= 6) {
      if (lane < 8 * K1_NS) {
        const unsigned o1 = vconst<0x3F803F80u>();
        *(LAS u32x4*)(qbuf + (wid - 6) * 1024 + (lane >> 3) * K1_QSTG + (lane & 7) * 16) = u32x4{o1, o1, o1, o1};
      }
      WAITL0;
      K1Hook<true> hk;
      fill(hk);
      Operand Q{x, D_, s0 - 16, 64, S_ * D_ * 2, 0, false, nullptr, 0};
      g_loader<KC, 16, 144, K1_NS, 16, -1, true>(qbuf, Q, wid - 6, lane, hk);
      hk.finish();
    } else {
      Operand P{w, 3 * D_, tp * 64, D_, 3 * D_ * D_ * 2, 0, false, nullptr, 0};
      SlowColHook<K1Hook<false>, 3, 2 * D_, 4, 32, 16> hs;
      fill(hs);
      hs.setup(w, 3 * D_, (tp & ~3) * 128, 0, BID >> 3, lane, wid == 4, (unsigned)(size_t)lds + L_SCR);
      g_loader<KM, 16, 192, K1_NS, 16, 0, false>(pbuf, P, wid - 4, lane, hs, !l0 && ((((int)((size_t)w >> 7)) + tp) & 7) != 3);
      hs.template next<3>();
      hs.finish();
    }
  } else {
    Gate gate;
    {
      int kh, tp4, tq4;
      k4_map(BID, kh, tp4, tq4);
      gate.prep(l0 ? 2 : wid, ctl + C_XCNT + (tq4 * NR_X + lane) * RS1 + tp4 * 2 + kh, (1 << NR_X) - 1, x_flag(ctl, tq, tp, lane), 0xFFFFFFFFull, l0 ? 0ull : 5ull);
      gate.m1 = 0x00030003ull;
      gate.seam();
    }
    k1_consumer(lds, lane, wid, l0, tq, (const char*)o + (size_t)(tq * 16 + tp) * TB + (wid >> 1) * (TB / 2), gate, [&] {
      if (l0) {
        int ln = lane;
        asm volatile("" : "+v"(ln));
        const int fr = ln & 15, fq = (ln & 63) >> 4;
        if (wid == 3) {
          f32x4 ta, tb;
          const unsigned ninf = vconst<0xFF61B1E6u>();
          static_for<4>([&](auto rc) { float a, b; win_bias<decltype(rc)::value>(ninf, a, b); ta[decltype(rc)::value] = a; tb[decltype(rc)::value] = b; });
          asm volatile("ds_write_b128 %2, %0\n\tds_write_b128 %2, %1 offset:1024" ::"v"(ta), "v"(tb), "v"(ln * 16 + L_MSK) : "memory");
        }
        const unsigned o1 = vconst<0x3F803F80u>();
        const bf16x8 ones = __builtin_bit_cast(bf16x8, u32x4{o1, o1, o1, o1});
        if (wid & 1) WAITV(2); else WAITV(1);
        bf16x8 f[2][2];
#pragma unroll
        for (int k = 0; k < 4; ++k) f[k >> 1][k & 1] = *(const LAS bf16x8*)(lds + L_C + wid * 4096 + k * 1024 + ln * 16);
        const f32x4 z4 = {0.f, 0.f, 0.f, 0.f};
        u64 ps[2];
#pragma unroll
        for (int j = 0; j < 2; ++j) {
          const f32x4 gm = __builtin_amdgcn_mfma_f32_16x16x32_bf16(f[j][1], f[j][1], __builtin_amdgcn_mfma_f32_16x16x32_bf16(f[j][0], f[j][0], z4, 0, 0, 0), 0, 0, 0);
          const f32x4 sm = __builtin_amdgcn_mfma_f32_16x16x32_bf16(ones, f[j][1], __builtin_amdgcn_mfma_f32_16x16x32_bf16(ones, f[j][0], z4, 0, 0, 0), 0, 0, 0);
          const float da = (fr & 1) ? gm[1] : gm[0], db = (fr & 1) ? gm[3] : gm[2], dg = (fr & 2) ? db : da;
          ps[j] = ((u64)__float_as_uint(dg) << 32) | __float_as_uint(sm[0]);
        }
        if (fq == (fr >> 2)) {
#pragma unroll
          for (int j = 0; j < 2; ++j)
            __hip_atomic_store((u64*)pstat + (size_t)(tq * 4 + wid) * 512 + tp * 32 + j * 16 + fr, ps[j], __ATOMIC_RELAXED, __HIP_MEMORY_SCOPE_AGENT);
        }
        SCHED0;
      }
    });
  }
}

struct ResTile {
  LAS char* a;
  int sw;
  __device__ __forceinline__ void init(LAS char* rt, int lane) {
    a = rt + (lane & 15) * 128; sw = (lane & 15) >> 1;
  }
  __device__ __forceinline__ void load(u32x2 (&v)[8], int off) const {
#pragma unroll
    for (int t = 0; t < 8; ++t) v[t] = *(const LAS u32x2*)((LAS char*)(size_t)(wa[t & 3] - off) + (t >> 2) * 2048);
  }
  __device__ __forceinline__ void store(const u32x2 (&v)[8]) const {
#pragma unroll
    for (int t = 0; t < 8; ++t) *(LAS u32x2*)((LAS char*)(size_t)wa[t & 3] + (t >> 2) * 2048) = v[t];
  }
  unsigned wa[4], ra[8], go[4];
  __device__ __forceinline__ void prep_t(LAS char* rt, int d0, int s0, int lane) {
    const int g = lane >> 4, q4 = (lane & 15) >> 2, p4 = lane & 3, fr = lane & 15;
    const int rl = 8 * g + q4, rh = rl + 4;
    const unsigned al = (unsigned)(size_t)rt + rl * 128 + (p4 & 1) * 8;
#pragma unroll
    for (int t = 0; t < 4; ++t) wa[t] = (unsigned)(size_t)a + ((((t >> 1) * 4 + g) ^ sw) * 16) + ((t ^ (g >> 1)) & 1) * 8;
#pragma unroll
    for (int k = 0; k < 4; ++k) {
      const int ch = 2 * k + (p4 >> 1);
      ra[2 * k] = al + ((ch ^ ((rl >> 1) & 7)) * 16);
      ra[2 * k + 1] = al + 512 + ((ch ^ ((rh >> 1) & 7)) * 16);
    }
    {
      const unsigned s1 = pow2sel((fr >> 1) == 3, 7, 10);
      go[0] = phys<64>((d0 + fr) >> 1) * 128 + (fr & 1) * 64 + (s0 >> 5) * (TB / 2) + g * 16;
      go[1] = go[0] + s1; go[2] = go[1] + s1; go[3] = go[2] + s1;
    }
#pragma unroll
    for (int t = 0; t < 8; ++t) asm volatile("" : "+v"(wa[t & 3]), "+v"(ra[t]), "+v"(go[t & 3]));
  }
  __device__ __forceinline__ void load_t(f32x2 (&z)[16]) const {
#pragma unroll
    for (int k = 0; k < 8; ++k) {
      const u32x2 v = __builtin_bit_cast(u32x2, __builtin_amdgcn_ds_read_tr16_b64_v4i16((LAS s16x4*)(size_t)ra[k]));
      z[2 * k] = f32x2{lo2f(v[0]), hi2f(v[0])}; z[2 * k + 1] = f32x2{lo2f(v[1]), hi2f(v[1])};
    }
#pragma unroll
    for (int e = 0; e < 16; ++e) asm volatile("" : "+v"(z[e]));
  }
};

__device__ __forceinline__ u32x4 row_stats(const u32x4 (&f)[2][2]) {
  const unsigned o1 = OPQ(0x3F803F80);
  const bf16x8 ones = __builtin_bit_cast(bf16x8, u32x4{o1, o1, o1, o1});
  const f32x4 z4 = {0.f, 0.f, 0.f, 0.f};
  f32x4 gm[2], sm[2];
#pragma unroll
  for (int a = 0; a < 2; ++a)
#pragma unroll
    for (int j = 0; j < 2; ++j) {
      const bf16x8 v = __builtin_bit_cast(bf16x8, f[j][a]);
      gm[j] = __builtin_amdgcn_mfma_f32_16x16x32_bf16(v, v, a ? gm[j] : z4, 0, 0, 0);
      sm[j] = __builtin_amdgcn_mfma_f32_16x16x32_bf16(ones, v, a ? sm[j] : z4, 0, 0, 0);
    }
  unsigned ka, kc;
  asm volatile("s_mov_b32 %0, 0xAAAAAAAA\n\ts_mov_b32 %1, 0xCCCCCCCC" : "=s"(ka), "=s"(kc));
  const bool b0 = __builtin_amdgcn_inverse_ballot_w64(((u64)ka << 32) | ka), b1 = __builtin_amdgcn_inverse_ballot_w64(((u64)kc << 32) | kc);
  u32x4 r;
#pragma unroll
  for (int j = 0; j < 2; ++j) {
    const float g0 = gm[j][0], g1 = gm[j][1], g2 = gm[j][2], g3 = gm[j][3];
    const float da = b0 ? g1 : g0, db = b0 ? g3 : g2;
    r[2 * j] = __float_as_uint(sm[j][0]); r[2 * j + 1] = __builtin_elementwise_min(__float_as_uint(b1 ? db : da), 0xFFFFFFFEu);
    asm volatile("" ::"v"(sm[j]));
  }
  return r;
}
#define DIAG "s_mov_b32 exec_lo, 0x00F0000F\n\ts_mov_b32 exec_hi, 0xF0000F00\n\t"
#define XLD \
  "global_load_dwordx4 v[192:195], %[gx], off sc1\n\tglobal_load_dwordx4 v[196:199], %[gx], off offset:256 sc1\n\t" \
  "global_load_dwordx4 v[200:203], %[gx], off offset:512 sc1\n\tglobal_load_dwordx4 v[204:207], %[gx], off offset:768 sc1\n\t"
using u32x16 = __attribute__((ext_vector_type(16))) unsigned;
__device__ __forceinline__ void x_issue(u32x16& g, const char* gx) { asm volatile(XLD : "=&{v[192:207]}"(g) : [gx] "v"(gx) : "memory"); }
template <int SL>
__device__ __forceinline__ void x_gather(u32x16& g, const char* gx, f32x2& s0, f32x2& s1) {
  unsigned n;
  asm volatile("s_mov_b32 %[n], 0x8000\n\ts_waitcnt vmcnt(0)\n"
               "1:\n\tv_max3_u32 v208, v193, v195, v197\n\tv_max3_u32 v209, v199, v201, v203\n\tv_max3_u32 v208, v208, v205, v207\n\t"
               "v_pk_add_f32 v[192:193], v[192:193], v[196:197]\n\tv_max_u32 v208, v208, v209\n\tv_cmp_eq_u32 vcc, -1, v208\n\t"
               "v_pk_add_f32 v[194:195], v[194:195], v[198:199]\n\tv_pk_add_f32 v[200:201], v[200:201], v[204:205]\n\t"
               "v_pk_add_f32 v[202:203], v[202:203], v[206:207]\n\ts_cbranch_vccz 3f\n\t"
               "s_sleep %[sl]\n\t" XLD "s_waitcnt vmcnt(0)\n\t"
               "s_sub_u32 %[n], %[n], 1\n\ts_cbranch_scc0 1b\n"
               "3:\n\tv_pk_add_f32 %[s0], v[192:193], v[200:201]\n\tv_pk_add_f32 %[s1], v[194:195], v[202:203]\n\ts_nop 1"
               : "+{v[192:207]}"(g), [s0] "=&v"(s0), [s1] "=&v"(s1), [n] "=&s"(n)
               : [gx] "v"(gx), [sl] "i"(SL) : "memory", "vcc", "scc", "v208", "v209");
}

#define XSLEEP0 5
#define XSLEEP1 1
constexpr int K2_RES = L_C, K2_RES0 = L_B + 32768;
template <class Base>
struct K3NextHook : Base {
  static constexpr int NEXT = 8;
  Stream<KM, 16, 128> sn;
  bool on; int nev; const u16* w1; const u16* cw1; const u64* ctl; LAS char* lds; int gwn, lanen;
  __device__ __forceinline__ void afterr() {
    OPAQUE_BID;
    int tq, tp;
    tile_map<8, 28, 4, 7>(BID, tq, tp);
    sn.init(Operand{w1, F_, min(tp, 27) * 128, 64, S_ * F_ * 2, 0, false, cw1, 32}, gwn, lanen);
    sn.pin();
  }
  __device__ __forceinline__ void step(int s) {
    if (__builtin_expect(s == nev, 0)) {
      if (s == 6) { if (gwn == 0) fetch_flags<0>(first_flags(ctl, lanen)); else fetch_flags<1024>(first_flags(ctl, lanen) + 2); }
      else if (!flags_set(L_FLG + gwn * 1024 + lanen * 16, TAG)) wait_first_phase(lds, ctl, lanen);
      nev += 4;
      asm volatile("" : "+s"(nev));
    }
    Base::step(s);
  }
  template <int S, int N> __device__ __forceinline__ void wait() {
    if (on) waitv<N>(); else waitv<N - (S - 12) * NEXT>();
  }
  template <int T> __device__ __forceinline__ void next() { if (on) sn.template stage<A1>(lds + L_A + gwn * 1024 + T * 16384); }
};
__device__ __forceinline__ void k2_body(LAS char* lds, int wid, const u16* __restrict__ wo, const u16* o, const u16* res, u16* hout,
                                        unsigned long long* xch, u64* ctl, bool l0, const u16* w1, const u16* cw1) {
  LAS char* qbuf = lds + L_B;
  LAS char* pbuf = lds + L_A;
  OPAQUE_BID;
  const int lane = lane_id();
  int kh, tq, tp;
  k4_map(BID, kh, tp, tq);
  const int p0 = tp * 128, q0 = tq * 128 + kh * 64;
  if (wid >= 6) {
    Operand P{cw1 + 448 * CW_BLK / 2, D_, p0, 64, 256 * CW_BLK, 0, false, nullptr, 0};
    SyncHook<K3NextHook<ResHook>, 1> hk;
    hk.on = BID < 224; hk.nev = l0 ? 6 : 99; hk.w1 = w1; hk.cw1 = cw1; hk.ctl = ctl; hk.lds = lds; hk.gwn = wid - 6; hk.lanen = lane;
    static_cast<ResHook&>(hk) = ResHook{{}, l0 ? res : nullptr, q0, p0, lds + K2_RES0, wid - 6, lane};
    g_loader<KM, 16, 128, K2_NSP, 16, -1, false>(pbuf, P, wid - 6, lane, hk, true);
    __builtin_amdgcn_s_waitcnt(0x0F70);
    return;
  }
  if (wid >= 4) {
    Operand Q{o, 16, q0, 64, 16 * 16 * TB, 0, true, nullptr, 0};
    WaitHookX hk;
    g_loader<KC, 16, 64, K2_NSQ, 16, 1, false>(qbuf, Q, wid - 4, lane, hk);
    __builtin_amdgcn_s_waitcnt(0x0F70);
    return;
  }
  Gate gate;
  {
    int tq1, tp1;
    tile_map<16, 16, 8, 4>(BID, tq1, tp1);
    gate.prep(wid, ctl + C_OCNT + (tq1 * NR_O + lane) * RS1 + tp1, (1 << NR_O) - 1, ctl + C_OCNT + (tq * NR_O + (tp * 2 + kh) % NR_O) * RS1 + (lane & 15), 0xFFFFull, 1);
    gate.m1 = (1 << NP2) - 1;
    gate.seam();
  }
  f32x4 acc[4][2];
  const int wp = wid >> 1, wq = wid & 1, row0 = q0 + wq * 32;
  ResTile rt;
  rt.init(lds + K2_RES + wid * 4096, lane);
  u32x2 x[8];
  unsigned long long* xw = xch + (size_t)(row0 >> 5) * 512 + (tp * 2 + wp) * 32 + (lane & 15) * 2;
  const char* gx = (const char*)(xch + (size_t)(row0 >> 5) * 512) + (lane >> 4) * 1024 + (lane & 15) * 16;
  asm volatile("" : "+v"(xw), "+v"(gx));
  rt.prep_t(lds + K2_RES + wid * 4096, wp * 64, wq * 32, lane);
  const char* hb = (const char*)hout + (size_t)(tp * 32 + (q0 >> 6)) * TB;
  asm volatile("" : "+s"(hb));
  unsigned cwa = L_COF + wid * 256 + lane * 4, cra = L_COF + wid * 256 + (lane >> 4) * 32;
  asm volatile("" : "+v"(cwa), "+v"(cra));
  const bool hi = lane >= 32;
  consumer16w<KM, KC, 4, 2, K2_NSP, 128 * 128, 64 * 128, 16, false>(pbuf, qbuf, acc, wp * 4, wq * 2, lane, gate, [&] {
    rt.load(x, l0 ? K2_RES - K2_RES0 : 0);
#pragma unroll
    for (int t = 0; t < 8; ++t) acc[t & 3][t >> 2] = f32x4{lo2f(x[t][0]), hi2f(x[t][0]), lo2f(x[t][1]), hi2f(x[t][1])};
  }, [](auto&&) {}, [](int) {});
  f32x2 z[16];
  ALIGN8;
#pragma unroll
  for (int t = 0; t < 8; ++t) x[t] = u32x2{pk2(acc[t & 3][t >> 2][0], acc[t & 3][t >> 2][1]), pk2(acc[t & 3][t >> 2][2], acc[t & 3][t >> 2][3])};
  {
    const u32x4 f[2][2] = {{u32x4{x[0][0], x[0][1], x[1][0], x[1][1]}, u32x4{x[2][0], x[2][1], x[3][0], x[3][1]}}, {u32x4{x[4][0], x[4][1], x[5][0], x[5][1]}, u32x4{x[6][0], x[6][1], x[7][0], x[7][1]}}};
    asm volatile(DIAG "global_store_dwordx4 %0, %1, off sc1\n\ts_mov_b64 exec, -1" ::"v"(xw), "v"(row_stats(f)) : "memory");
  }
  SCHED0;
  rt.store(x);
  WAITL0;
  if (XSLEEP0) __builtin_amdgcn_s_sleep(XSLEEP0);
  f32x2 s0, s1;
  u32x16 xg;
  x_issue(xg, gx);
  rt.load_t(z);
  x_gather<XSLEEP1>(xg, gx, s0, s1);
  auto add2 = [](auto r) { return __uint_as_float(r[0]) + __uint_as_float(r[1]); };
  const float sm = half_sum(add2(__builtin_amdgcn_permlane16_swap(__float_as_uint(s0[0]), __float_as_uint(s1[0]), false, false)));
  const float sq = half_sum(add2(__builtin_amdgcn_permlane16_swap(__float_as_uint(s0[1]), __float_as_uint(s1[1]), false, false)));
  const float mean = sm * (1.0f / D_);
  const float var = fmaxf(__builtin_fmaf(-mean, mean, sq * (1.0f / D_)), 0.f);
  const float m1 = rbf(mean), i1 = __builtin_amdgcn_rcpf(rbf(__builtin_amdgcn_sqrtf(rbf(rbf(var) + 1e-5f))));
  const float m2 = rbf((mean - m1) * i1), i2 = __builtin_amdgcn_rcpf(rbf(__builtin_amdgcn_sqrtf(rbf(rbf(var * i1 * i1) + 1e-5f))));
  *(LAS float*)(size_t)cwa = (hi ? -(m1 * i1 + m2) : i1) * i2;
  CFENCE;
  f32x4 cf[4];
#pragma unroll
  for (int k = 0; k < 4; ++k) cf[k] = *(const LAS f32x4*)((LAS char*)(size_t)cra + (k & 1) * 16 + (k >> 1) * 128);
#pragma unroll
  for (int k = 0; k < 4; ++k) {
    u32x4 y;
#pragma unroll
    for (int e = 0; e < 4; ++e) {
      const f32x2 v = z[4 * k + e] * f32x2{cf[e >> 1][2 * (e & 1)], cf[e >> 1][2 * (e & 1) + 1]} + f32x2{cf[2 + (e >> 1)][2 * (e & 1)], cf[2 + (e >> 1)][2 * (e & 1) + 1]};
      y[e] = pk2(v[0], v[1]);
    }
    asm volatile("global_store_dwordx4 %0, %1, %2 sc1\n\ts_nop 1" ::"v"(rt.go[k]), "v"(y), "s"(hb) : "memory");
    SCHED0;
  }
}

constexpr int K4_NS = 4;
constexpr int K4_RES = L_C;
__device__ __forceinline__ void w2_init(Stream<KM, 16, 128>& st, const u16* w2, const u16* cw2, int gw, int lane) {
  OPAQUE_BID;
  int kh, tp, tq;
  k4_map(BID, kh, tp, tq);
  st.init(Operand{w2, S_, tq * 128 + kh * 64, 64, F_ * S_ * 2, kh * 28, false, cw2, 56}, gw, lane);
  st.twice();
  st.pin();
}
__device__ __forceinline__ void u_init(Stream<KC, 16, 128, true>& st, const u16* u, int gw, int lane) {
  OPAQUE_BID;
  int kh, tp, tq;
  k4_map(BID, kh, tp, tq);
  st.init(Operand{u, 56, tp * 128, 64, 8 * 56 * TB, kh * 28, true, nullptr, 0}, gw, lane);
  st.twice();
  st.pin();
}
template <class T> __device__ __forceinline__ T* OPV(T* p) { return (T*)(__attribute__((address_space(1))) T*)(size_t)p; }
struct UFlags {
  u64* set; const u64* get; u64 tag;
  __device__ __forceinline__ void init(LAS char* lds, u64* flags, int gw, int lane) {
    OPAQUE_BID;
    int kh, tp, tq, tq3, tp3;
    k4_map(BID, kh, tp, tq);
    tile_map<8, 28, 4, 7>(BID, tq3, tp3);
    u64* b = flags + (gw ? C_UCN2 : C_UCNT);
    set = b + ((tq3 * 2 + tp3 / 14) * NR_U + lane) * RS1 + tp3 % 14;
    get = b + ((tp * 2 + (gw ? (lane >> 3) & 1 : kh)) * NR_U + tq % NR_U) * RS1 + (gw ? min((lane & 7) * 2, 12) : min(lane, 13));
    tag = TAG;
    asm volatile("" : "+v"(set), "+v"(get), "+v"(tag));
  }
  __device__ __forceinline__ void publish() { asm volatile("s_mov_b64 exec, %2\n\tglobal_atomic_swap_x2 %0, %1, off sc1\n\ts_mov_b64 exec, -1" ::"v"(set), "v"(tag), "i"((1 << NR_U) - 1) : "memory"); }
};
struct K4NextHook : NoHook {
  static constexpr int AUX = A1;
  static constexpr int NEXT = 8;
  Stream<KM, 16, 128>* st; LAS char* lds; int gw;
  template <int T> __device__ __forceinline__ void next() { st->stage<A2>(lds + L_A + gw * 1024 + T * 16384); }
};
struct W2Base : NoHook { static constexpr int AUX = A2; };
struct UHook : WaitHookP { static constexpr bool EVOD = true; int kh; };
template <class Base>
struct W2Hook : Base {
  static constexpr bool PREALL = true, EVOD = true, OWNP = true;
  int kh;
  UFlags* uf; bool own; int gww;
  __device__ __forceinline__ void setup() { Base::first(); Base::afterr(); }
  __device__ __forceinline__ void first() {
    if (gww == 0) {
      u64 a, b, c, d, t, z = vz(), m1 = (1 << NP4) - 1;
      unsigned n;
      asm volatile("s_mov_b64 exec, 0x3FFF\n\ts_mov_b32 %[n], 0\n\t" POLL1(a) POLL1(b) POLL1(c) POLL0(d)
                   "1:\n\t" POLL3(a, 4) POLL3(b, 5) POLL3(c, 6) POLL3(d, 7) PEND
                   : [a] "=&v"(a), [b] "=&v"(b), [c] "=&v"(c), [d] "=&v"(d), [n] "=&s"(n), [t] "=&s"(t), [m1] "+s"(m1)
                   : [q] "v"(uf->get), [z] "v"(z), [tag] "v"(uf->tag), [sl] "i"(PSLP) : "memory", "vcc", "scc");
      asm volatile("s_barrier\n\ts_waitcnt vmcnt(0)" ::"v"(a), "v"(b), "v"(c), "v"(d) : "memory");
    } else { BARRIER; WAITV(0); BARRIER; }
  }
  int nev;
  __device__ __forceinline__ void step(int s) {
    if (__builtin_expect(s == nev, 0)) {
      if (s == 0) { if (own) uf->publish(); }
      else if (s == 4) fetch_flags<0>(uf->get);
      else if (!flags_set(L_FLG + Base::lanen * 16, uf->tag)) wait_tags<2>(OPV(uf->get), uf->tag);
      nev = s >= 8 ? 99 : s + 4;
      asm volatile("" : "+s"(nev));
    }
  }
};
template <class Base>
struct K1NextHook : Base {
  static constexpr int NEXT = 8;
  Stream<KM, 16, 192> sn;
  bool on; const u16* wq; LAS char* dstn; int gwn, lanen;
  __device__ __forceinline__ void afterr() {
    OPAQUE_BID;
    int tq, tp;
    tile_map<16, 16, 8, 4>(BID, tq, tp);
    sn.init(Operand{wq, 3 * D_, tp * 64, D_, 3 * D_ * D_ * 2, 0, false, nullptr, 0}, gwn, lanen);
#pragma unroll
    for (int i = 0; i < 4; ++i) asm volatile("" : "+v"(sn.voff[i]));
  }
  template <int U> __device__ __forceinline__ void unit() {
    const auto rsrc = __builtin_amdgcn_make_buffer_rsrc((void*)sn.gbase[0], 0, sn.gbytes[0], 0x00020000);
#pragma unroll
    for (int i = 0; i < 4; ++i) {
#if defined(__HIP_DEVICE_COMPILE__)
      __builtin_amdgcn_raw_ptr_buffer_load_lds(rsrc, (LAS void*)(dstn + U * 8192 + i * 2048), 16, sn.voff[i], sn.so[0] + (U / 3) * sn.inc[0] + (U % 3) * D_ * 2, 0, 0);
#endif
    }
  }
  __device__ __forceinline__ void last() { Base::last(); if (on) unit<8>(); }
  template <int S, int N> __device__ __forceinline__ void wait() {
    if (on) waitv<N + (S < 26 + KD ? 4 : 0)>(); else waitv<N - (S - 24 - KD) * NEXT>();
  }
  template <int T> __device__ __forceinline__ void next() { if (on) { unit<2 * ((KD + T) % 4)>(); unit<2 * ((KD + T) % 4) + 1>(); } }
};
__device__ __forceinline__ void k4_consumer(LAS char* lds, u16* out, u32x2* pstat, float* part, u64* flags, int wid) {
  LAS char* pbuf = lds + L_B;
  LAS char* qbuf = lds + L_A;
  OPAQUE_BID;
  const int lane = lane_id();
  int kh, tp, tq;
  k4_map(BID, kh, tp, tq);
  const int p0 = tp * 128, q0 = tq * 128;
  DrainGate gate;
  const int wp = wid >> 1, wq = wid & 1;
  f32x4 acc[4][4];
  const int row0 = q0 + kh * 64 + wq * 32;
  const size_t obase = (size_t)row0 * D_ + p0 + wp * 64;
#pragma unroll
  for (int j = 0; j < 4; ++j) {
    const float bq = j < 2 ? bf2f(*(const LAS u16*)(lds + L_BIA + 256 + (kh * 64 + wq * 32 + j * 16 + (lane & 15)) * 2)) : 0.f;
#pragma unroll
    for (int i = 0; i < 4; ++i) acc[i][j] = f32x4{bq, bq, bq, bq};
  }
  u32x4 x[2][2];
#define GLD "global_load_dwordx4 %[g0], %[go], %[gp] sc1\n\tglobal_load_dwordx4 %[g1], %[go], %[gp] offset:1024 sc1\n\t" \
            "global_load_dwordx4 %[g2], %[go], %[gp] offset:2048 sc1\n\tglobal_load_dwordx4 %[g3], %[go], %[gp] offset:3072 sc1"
  u64 ptag, a, a2, miss;
  u32x4 g[4];
  const char* gp = (const char*)part + (size_t)((BID ^ 1) * 4 + wid) * 4096;
  unsigned v0;
  const u64* myf = flags + (BID * 4 + wid) * RSP;
  const u64* paf = flags + ((BID ^ 1) * 4 + wid) * RSP;
  u64 zz;
  auto poll1 = [&](u64& r) {
    asm volatile("s_mov_b64 exec, 1\n\tglobal_atomic_or_x2 %0, %1, %2, %3 sc0 sc1\n\ts_mov_b64 exec, -1" : "=&v"(r) : "v"(v0), "v"(zz), "s"(paf) : "memory");
  };
  consumer16w<KC, KM, 4, 4, K4_NS, 128 * 128, 128 * 128, 28 - KD, true>(pbuf, qbuf, acc, wp * 4, wq * 2, lane, gate, [&] {
    const int fr = lane & 15, fq = lane >> 4;
#pragma unroll
    for (int t = 0; t < 4; ++t) x[t >> 1][t & 1] = *(const LAS u32x4*)(lds + K4_RES + wid * 4096 + (t >> 1) * 2048 + fr * 128 + ((((t & 1) * 4 + fq) ^ (fr >> 1)) * 16));
  }, [&](int t) {
    const auto rs = __builtin_amdgcn_make_buffer_rsrc((void*)part, 0, 256 * 16384, 0x00020000);
    const f32x4 a = acc[t][2], b = acc[t][3];
    __builtin_amdgcn_raw_buffer_store_b128(u32x4{pk2(a[0], a[1]), pk2(a[2], a[3]), pk2(b[0], b[1]), pk2(b[2], b[3])}, rs, lane * 16 + t * 1024, (BID * 4 + wid) * 4096, 16);
  }, [&](int k) {
    if (k == 0) asm volatile("s_nop 3\n\tv_mov_b32 %0, %3\n\tv_mov_b64 %2, 0\n\tds_read_b64 %1, %0" : "=&v"(v0), "=&v"(ptag), "=&v"(zz) : "i"(L_TAG) : "memory");
    if (k == FBK) {
      asm volatile("v_mov_b32 %0, 0\n\ts_waitcnt vmcnt(0)\n\ts_mov_b64 exec, 1\n\tglobal_atomic_swap_x2 %0, %1, %2 sc1\n\ts_mov_b64 exec, -1" : "+v"(v0), "+v"(ptag) : "s"(myf) : "memory");
    }
    if (k == CKK - 2) poll1(a);
    if (k == CKK - 1) poll1(a2);
    if (k == CKK)
      asm volatile("s_mov_b64 exec, 1\n\ts_waitcnt vmcnt(1)\n\tv_cmp_ne_u64 vcc, %[tag], %[a]\n\ts_mov_b64 exec, -1\n\ts_mov_b64 %[m], vcc\n\ts_cbranch_vccnz 1f\n\t" GLD "\n1:"
                   : [g0] "=&v"(g[0]), [g1] "=&v"(g[1]), [g2] "=&v"(g[2]), [g3] "=&v"(g[3]), [m] "=&s"(miss) : [tag] "v"(ptag), [a] "v"(a), [go] "v"(lane * 16), [gp] "s"(gp) : "memory", "vcc");
    if (k == CKK + 1)
      asm volatile("s_cmp_eq_u64 %[m], 0\n\ts_cbranch_scc1 1f\n\ts_mov_b64 exec, 1\n\ts_waitcnt vmcnt(0)\n\tv_cmp_ne_u64 vcc, %[tag], %[a]\n\ts_mov_b64 exec, -1\n\ts_mov_b64 %[m], vcc\n\ts_cbranch_vccnz 1f\n\t" GLD "\n1:"
                   : [g0] "+v"(g[0]), [g1] "+v"(g[1]), [g2] "+v"(g[2]), [g3] "+v"(g[3]), [m] "+s"(miss), [a] "+v"(a2) : [tag] "v"(ptag), [go] "v"(lane * 16), [gp] "s"(gp) : "memory", "vcc", "scc");
  });
  SCHED0;
  f32x2 z[16];
#pragma unroll
  for (int e = 0; e < 16; ++e) {
    const unsigned r = x[e >> 3][(e >> 2) & 1][e & 3];
    const f32x4 v = acc[(e >> 1) & 3][e >> 3];
    z[e] = f32x2{lo2f(r), hi2f(r)} + f32x2{v[2 * (e & 1)], v[2 * (e & 1) + 1]};
    asm volatile("" : "+v"(z[e]));
  }
  unsigned vo[4];
#pragma unroll
  for (int k = 0; k < 4; ++k) { vo[k] = (unsigned)(((lane & 7) + 8 * k) * D_ * 2 + (lane & 8) * 8 + (lane >> 4) * 16); asm volatile("" : "+v"(vo[k])); }
  const u16* ob = out + obase;
  asm volatile("" : "+s"(ob));
  {
    u64 c, d, sv;
    unsigned n;
    asm volatile("s_cmp_eq_u64 %[m], 0\n\ts_cbranch_scc1 9f\n\t" POLL_LOOP GLD "\n9:\n\ts_waitcnt vmcnt(0)"
        : [a] "=&v"(a), [b] "+v"(a2), [c] "=&v"(c), [d] "=&v"(d), [sv] "=&s"(sv), [n] "=&s"(n), [g0] "+v"(g[0]), [g1] "+v"(g[1]), [g2] "+v"(g[2]), [g3] "+v"(g[3])
        : [q] "v"(paf), [z] "v"(zz), [tag] "v"(ptag), [em] "s"(1ull), [sl] "i"(2), [m] "s"(miss), [go] "v"(lane * 16), [gp] "s"(gp) : "memory", "vcc", "scc");
  }
  {
#pragma unroll
    for (int e = 0; e < 16; ++e) {
      const unsigned r = g[(e >> 1) & 3][2 * (e >> 3) + (e & 1)];
      z[e] += f32x2{lo2f(r), hi2f(r)};
      x[e >> 3][(e >> 2) & 1][e & 3] = pk2(z[e][0], z[e][1]);
      if ((e & 7) == 7) {
        u32x4 n0, n1;
#pragma unroll
        for (int k = 0; k < 4; ++k) {
          n0[k] = __builtin_amdgcn_update_dpp(x[e >> 3][0][k], x[e >> 3][1][k], 0x128, 0xF, 0xC, false);
          n1[k] = __builtin_amdgcn_update_dpp(x[e >> 3][1][k], x[e >> 3][0][k], 0x128, 0xF, 0x3, false);
        }
        asm volatile("global_store_dwordx4 %0, %2, %4 sc1\n\tglobal_store_dwordx4 %1, %3, %4 sc1\n\ts_nop 1" ::"v"(vo[(e >> 3) * 2]), "v"(vo[(e >> 3) * 2 + 1]), "v"(n0), "v"(n1), "s"(ob) : "memory");
      }
    }
    {
      if (pstat != nullptr) {
        const u32x4 c = row_stats(x);
        asm volatile(DIAG "global_store_dwordx2 %0, %1, %3 sc1\n\tglobal_store_dwordx2 %0, %2, %3 offset:128 sc1\n\ts_mov_b64 exec, -1"
                     ::"v"((lane & 15) * 8), "v"(u32x2{c[0], c[1]}), "v"(u32x2{c[2], c[3]}), "s"(pstat + (size_t)(row0 >> 5) * 512 + (tp * 2 + wp) * 32) : "memory");
        CFENCE;
#pragma unroll
        for (int t = 0; t < 4; ++t) *(LAS u32x4*)(lds + K4_RES + wid * 4096 + (t >> 1) * 2048 + (lane & 15) * 128 + ((((t & 1) * 4 + (lane >> 4)) ^ ((lane & 15) >> 1)) * 16)) = x[t >> 1][t & 1];
      }
    }
  }
}

constexpr int K3_NSP = 4, K3_NSQ = 4;
__device__ __forceinline__ u64* h_flag(u64* ctl, unsigned BID, int lane) {
  int kh, tq, tp;
  k4_map(BID, kh, tp, tq);
  return ctl + C_HCNT + (tp * NR_H + lane) * RS2 + tq * 2 + kh;
}
template <class T> __device__ __forceinline__ T* OPQ(T* p) {
  auto q = (__attribute__((address_space(1))) T*)p;
  asm volatile("" : "+s"(q));
  return (T*)q;
}
#define APOS 2
__device__ __forceinline__ void k34_body(LAS char* lds, int wid, bool l0, const u16* __restrict__ w1, const u16* h, u16* u, const u16* __restrict__ cw1, u64* flags,
                                         const u16* __restrict__ w2, u16* out, u32x2* pstat, float* part, const u16* __restrict__ wq_) {
  LAS char* pbuf = lds + L_A;
  LAS char* qbuf = lds + L_B;
  OPAQUE_BID;
  const int lane = lane_id();
  const bool own = BID < 224;
  if (!own) {
    WAITV(0);
    BARRIER;
    if (wid == 0 && lane < NR_H) flag_set(h_flag(flags, BID, lane), TAG);
    if (l0) {
      if (wid == 1) {
        u64 a, b, c, d, sv, z = vz(), tag = TAG;
        unsigned n;
        const u64* q = flags + C_HCNT + ((BID & 7) * NR_H + ((BID >> 3) & 3) * 2) * RS2 + (lane & 31);
        asm volatile(POLL_LOOP "s_waitcnt vmcnt(0)" : [a] "=&v"(a), [b] "=&v"(b), [c] "=&v"(c), [d] "=&v"(d), [n] "=&s"(n), [sv] "=&s"(sv)
                     : [q] "v"(q), [z] "v"(z), [tag] "v"(tag), [em] "s"(0xFFFFFFFFull), [sl] "i"(8) : "memory", "vcc", "scc");
      }
      BARRIER;
      for (int k = 0; k < WT0; ++k) __builtin_amdgcn_s_sleep(8);
      for (int k = 0; k < wid; ++k) __builtin_amdgcn_s_sleep(WSTG);
#pragma unroll 1
      for (int i = 0; i < 7; ++i) {
        const int g = 2 * i + (lane >> 5), par = g >= 7;
        touch_line(noslow(w2, (((BID >> 4) & 1) * 1792 + (g - 7 * par) * 256 + (BID & 15) * 16 + par * 8 + wid) * 32 + (lane & 31)), L_SCR);
        __builtin_amdgcn_s_sleep(WPER);
      }
    } else if (wid == 4) {
      __builtin_amdgcn_s_sleep(127); __builtin_amdgcn_s_sleep(127); __builtin_amdgcn_s_sleep(70);
    }
  }
  int tq, tp;
  tile_map<8, 28, 4, 7>(BID, tq, tp);
  const int p0 = tp * 128, q0 = tq * 128;
  if (wid >= 6) {
    Stream<KC, 16, 128, true> st;
    u_init(st, u, wid - 6, lane);
    if (own) {
      Operand Q{h, 32, q0, 64, 8 * 32 * TB, 0, true, nullptr, 0};
      WaitHookX hk;
      g_loader<KC, 16, 128, K3_NSQ, 32, -1, false>(qbuf, Q, wid - 6, lane, hk);
      BARRIER;
    }
    UHook hk;
    hk.kh = BID & 1;
    g_run<KC, 16, 128, K4_NS, 28 + KD, 3, false>(qbuf, st, wid - 6, lane, hk, false);
    __builtin_amdgcn_s_waitcnt(0x0F70);
    return;
  }
  if (wid >= 4) {
    Stream<KM, 16, 128> st;
    UFlags uf;
    w2_init(st, w2, cw1 + 224 * CW_BLK / 2, wid - 4, lane);
    uf.init(lds, flags, wid - 4, lane);
    int kh, tp4, tq4;
    k4_map(OPQ((int)BID), kh, tp4, tq4);
    W2Hook<K1NextHook<W2Base>> h4;
    h4.on = l0 && ((((int)((size_t)wq_ >> 7)) + (BID & 1) * 4 + (BID >> 6)) & 7) != 3; h4.wq = wq_; h4.dstn = lds + L_A + (wid - 4) * 1024; h4.gwn = wid - 4; h4.lanen = lane;
    h4.kh = kh; h4.uf = &uf; h4.own = own; h4.gww = wid - 4; h4.nev = wid == 5 ? 0 : 99;
    h4.setup();
    if (own) {
      Operand P{w1, F_, p0, 64, S_ * F_ * 2, 0, false, cw1, 32};
      SyncHook<K4NextHook, 2> hk;
      hk.st = &st; hk.lds = lds; hk.gw = wid - 4;
      g_loader<KM, 16, 128, K3_NSP, 32, 2, false>(pbuf, P, wid - 4, lane, hk, true);
      BARRIER;
      if (wid == 4) {
        uf.publish();
      }
    }
    g_run<KM, 16, 128, K4_NS, 28 + KD, -1, false>(pbuf, st, wid - 4, lane, h4, own);
    __builtin_amdgcn_s_waitcnt(0x0F70);
    return;
  }
  if (own) {
    Gate gate;
    gate.prep(wid, h_flag(flags, BID, lane), (1 << NR_H) - 1, flags + C_HCNT + (tq * NR_H + tp % NR_H) * RS2 + (lane & 31), 0xFFFFFFFFull, 1);
    gate.m1 = (1 << NP3) - 1;
    gate.seam();
    const int wp = wid >> 1, wq = wid & 1;
    f32x4 acc[4][4];
#pragma unroll
    for (int i = 0; i < 4; ++i) {
      const u32x2 bv = *(const LAS u32x2*)(lds + L_BIA + (wp * 64 + (i >> 1) * 8 + (lane >> 4) * 16 + ((i ^ (lane >> 5)) & 1) * 4) * 2);
#pragma unroll
      for (int j = 0; j < 4; ++j) acc[i][j] = f32x4{lo2f(bv[0]), hi2f(bv[0]), lo2f(bv[1]), hi2f(bv[1])};
    }
    consumer16w<KM, KC, 4, 4, K3_NSP, 128 * 128, 128 * 128, 32, false>(pbuf, qbuf, acc, wp * 4, wq * 4, lane, gate, [&] { }, [&](auto&& mm) {
      int ln = lane;
      asm volatile("" : "+v"(ln));
      const int fr = ln & 15, fq = (ln & 63) >> 4;
      const char* ub = (const char*)u + (size_t)(tq * 56 + tp * 2) * TB + wp * (TB / 2);
      unsigned uo[4];
      {
        const unsigned s1 = pow2sel((fr >> 1) == 3, 7, 10);
        uo[0] = phys<64>(wq * 32 + (fr >> 1)) * 128 + (fr & 1) * 64 + fq * 16; uo[1] = uo[0] + s1; uo[2] = uo[1] + s1; uo[3] = uo[2] + s1;
      }
      mm(0, 8);
#pragma unroll
      for (int n = 0; n < 8; ++n) { __builtin_amdgcn_sched_group_barrier(0x8, 1, 0); __builtin_amdgcn_sched_group_barrier(0x2, 2, 0); }
      asm volatile("" : "+v"(uo[0]), "+v"(uo[1]), "+v"(uo[2]), "+v"(uo[3]));
      const float c0 = -2.0f * 0.7978845608028654f * 1.4426950408889634f, c1 = c0 * 0.044715f;
      auto shade = [&](int n0) {
        if (n0 < 32) {
          mm(n0, n0 + GM);
#pragma unroll
          for (int n = 0; n < GM; ++n) { __builtin_amdgcn_sched_group_barrier(0x8, 1, 0); __builtin_amdgcn_sched_group_barrier(0x400, 1, 0); }
          __builtin_amdgcn_sched_group_barrier(0x400, 16 - GM, 0);
        }
      };
#pragma unroll
      for (int g = 0; g < 4; ++g) {
        f32x2 x[8], z[8];
        auto abar = [&](int pos) {
          if (g == 3 && pos == APOS) {
            if (pos > 0) asm volatile("" : "+v"(z[0]), "+v"(z[1]), "+v"(z[2]), "+v"(z[3]), "+v"(z[4]), "+v"(z[5]), "+v"(z[6]), "+v"(z[7]));
            SCHED0; WAITV(2); BARRIER; SCHED0;
          }
        };
        abar(0);
        ALIGN8;
#pragma unroll
        for (int e = 0; e < 8; ++e) {
          const f32x4 v = acc[(g >> 1) * 2 + ((e >> 1) & 1)][(g & 1) * 2 + (e >> 2)];
          x[e] = f32x2{v[2 * (e & 1)], v[2 * (e & 1) + 1]};
          z[e] = x[e] * x[e];
        }
#pragma unroll
        for (int e = 0; e < 8; ++e) z[e] = z[e] * c1 + c0;
#pragma unroll
        for (int e = 0; e < 8; ++e) z[e] *= x[e];
        SCHED0;
        abar(1);
#pragma unroll
        for (int e = 0; e < 8; ++e) z[e] = f32x2{__builtin_amdgcn_exp2f(z[e][0]), __builtin_amdgcn_exp2f(z[e][1])};
        shade(8 + g * GM);
        abar(2);
        ALIGN8;
#pragma unroll
        for (int e = 0; e < 8; ++e) z[e] += 1.0f;
        SCHED0;
#pragma unroll
        for (int e = 0; e < 8; ++e) z[e] = f32x2{__builtin_amdgcn_rcpf(z[e][0]), __builtin_amdgcn_rcpf(z[e][1])};
        abar(3);
        ALIGN8;
        u32x4 o[2];
#pragma unroll
        for (int e = 0; e < 8; ++e) z[e] *= x[e];
#pragma unroll
        for (int e = 0; e < 8; ++e) o[e >> 2][e & 3] = pk2(z[e][0], z[e][1]);
#pragma unroll
        for (int a = 0; a < 2; ++a)
          asm volatile("global_store_dwordx4 %0, %1, %2 sc1\n\ts_nop 1" ::"v"(uo[(g & 1) * 2 + a]), "v"(o[a]), "s"(ub + (g >> 1) * TB) : "memory");
      }
    }, [](int) {});
  }
  k4_consumer(lds, OPQ(out), pstat, OPQ(part), OPQ(flags), wid);
}

extern "C" __global__ __launch_bounds__(512, 2) void k_all(const u16* x0_, const u16* wqkv_, const u16* wo_, const u16* w1_, const u16* b1_, const u16* w2_,
                                                           const u16* b2_, u16* out_, char* ws_) {
  LAS char* lds = (LAS char*)(size_t)0;
  {
    unsigned long long pc;
    unsigned off;
    asm volatile("s_cmp_lt_u32 %3, %5\n\ts_cbranch_scc0 1f\n\ts_getpc_b64 %0\n\tv_lshlrev_b32 %1, 7, %2\n\ts_mov_b32 m0, %4\n\t"
                 "v_min_u32 %1, k9_end-k_all-256, %1\n\tglobal_load_lds_dword %1, %0\n1:"
                 : "=&s"(pc), "=&v"(off) : "v"(threadIdx.x), "s"(blockIdx.x), "i"(L_SCR), "i"(CWN) : "memory", "scc", "m0");
  }
  auto ka = (const __attribute__((address_space(4))) u64*)__builtin_amdgcn_kernarg_segment_ptr();
  asm volatile("" : "+s"(ka));
  typedef __attribute__((address_space(1))) u16 gu16;
#define ARGS auto kb = ka; asm volatile("" : "+s"(kb)); \
  const u16 *x0 = (u16*)(gu16*)kb[0], *wqkv = (u16*)(gu16*)kb[1], *wo = (u16*)(gu16*)kb[2], *w1 = (u16*)(gu16*)kb[3], *b1 = (u16*)(gu16*)kb[4], *w2 = (u16*)(gu16*)kb[5], *b2 = (u16*)(gu16*)kb[6]; \
  u16* out = (u16*)(gu16*)kb[7]; char* w = OPQ(ws)
  char* ws = (char*)(u16*)(gu16*)ka[8];
  const int wid = __builtin_amdgcn_readfirstlane(threadIdx.x >> 6);
  {
    const u16 *x0 = (u16*)(gu16*)ka[0], *wqkv = (u16*)(gu16*)ka[1];
    const int lane = threadIdx.x & 63;
    int tq, tp;
    tile_map<16, 16, 8, 4>(blockIdx.x, tq, tp);
    if (wid >= 6) {
      Stream<KC, 16, 144> st;
      st.init(Operand{x0, D_, tq * 128 - 16, 64, S_ * D_ * 2, 0, false, nullptr, 0}, wid - 6, lane);
      st.stage<16>(lds + K1_QOFF + (wid - 6) * 1024, lane >= 8);
    } else if (wid >= 4) {
      Stream<KM, 16, 192> st;
      st.init(Operand{wqkv, 3 * D_, tp * 64, D_, 3 * D_ * D_ * 2, 0, false, nullptr, 0}, wid - 4, lane);
      st.stage<0>(lds + L_A + (wid - 4) * 1024);
    } else {
      const u16* xb = x0 + (size_t)(tq * 128 + wid * 32) * D_ + tp * 64;
      int t3q, t3p, kh4, t4p, t4q;
      tile_map<8, 28, 4, 7>(blockIdx.x, t3q, t3p);
      k4_map(blockIdx.x, kh4, t4p, t4q);
      const char* bp = wid >= 2 ? ws + W_EPOCH : (const char*)(u16*)(gu16*)ka[4 + 2 * (wid & 1)] + (wid ? t4q : min(t3p, 27)) * 256;
      asm volatile("s_mov_b32 m0, %[lx]\n\ts_nop 0\n\tglobal_load_lds_dwordx4 %[xo], %[xb]\n\ts_add_u32 m0, %[lx], 1024 - 64\n\ts_nop 0\n\tglobal_load_lds_dwordx4 %[xo], %[xb] offset:64\n\t"
                   "s_add_u32 m0, %[lx], 2048\n\ts_nop 0\n\tglobal_load_lds_dwordx4 %[xo], %[xc]\n\ts_add_u32 m0, %[lx], 3072 - 64\n\ts_nop 0\n\tglobal_load_lds_dwordx4 %[xo], %[xc] offset:64\n\t"
                   "s_mov_b32 m0, %[lb]\n\ts_nop 0\n\tglobal_load_lds_dword %[bo], %[bp] sc1"
                   :: [xo] "v"((lane & 15) * (D_ * 2) + (lane & 48)), [xb] "s"(xb), [xc] "s"(xb + 16 * D_), [bo] "v"(lane * 4), [bp] "s"(bp),
                      [lx] "s"(L_C + wid * 4096), [lb] "s"(wid >= 2 ? L_RS + wid * 256 : L_BIA + wid * 256) : "memory", "m0", "scc");
      if (wid == 1) {
        const int b = blockIdx.x, l = min(lane, 35), r = l / 12, c = l - r * 12;
        touch_line((const char*)wqkv + (64 + 64 * r + ((b >> 2) & 1) * 32 + (b >> 3)) * 6144 + (c >> 2) * 2048 + ((b & 3) * 4 + (c & 3)) * 128, L_SCR);
      }
      if (wid == 3) {
        SlowColHook<NoHook, 3, 2 * D_, 4, 32, 16> hs;
        hs.setup2(wqkv, 3 * D_, (tp & ~3) * 128, 0, blockIdx.x >> 3, lane, true, L_SCR);
        touch_line(hs.p != nullptr ? hs.p : (const char*)wqkv, L_SCR);
      }
    }
    CFENCE;
  }
#pragma unroll 1
  for (int layer = 0; layer < 2; ++layer) {
    {
      ARGS;
      const int l = OPQ(layer);
      k1_body(lds, OPQ(wid), l == 0, wqkv, (u16*)(gu16*)kb[l * 7], (u16*)(w + W_O), (u32x4*)(w + W_XCH),
              (u64*)(w + W_CTL) + l * C_WORDS, (u32x2*)(w + W_PST), wo, w1, w2, (u16*)(w + W_CW));
    }
    {
      ARGS;
      const int l = OPQ(layer);
      k2_body(lds, OPQ(wid), wo, (const u16*)(w + W_O), x0, (u16*)(w + W_H),
              (unsigned long long*)(w + W_XCH), (u64*)(w + W_CTL) + l * C_WORDS, l == 0, w1, (const u16*)(w + W_CW));
    }
    {
      ARGS;
      const int l = OPQ(layer);
      k34_body(lds, OPQ(wid), l == 0, w1, (const u16*)(w + W_H), (u16*)(w + W_U), (const u16*)(w + W_CW), (u64*)(w + W_CTL) + l * C_WORDS,
               w2, out, l == 0 ? (u32x2*)(w + W_PST) : nullptr, (float*)(w + W_PART), wqkv);
    }
  }
  ARGS;
  if (blockIdx.x == 0 && wid == 7 && lane_id() == 0) __hip_atomic_store((u64*)(w + W_EPOCH), TAG, __ATOMIC_RELAXED, __HIP_MEMORY_SCOPE_AGENT);
}

extern "C" __global__ void k9_end() {}

extern "C" void kernel_launch(void* const* d_in, const int* in_sizes, int n_in, void* d_out, int out_size, void* d_ws,
                              size_t ws_size, hipStream_t stream) {
  (void)hipFuncSetAttribute((const void*)k_all, hipFuncAttributeMaxDynamicSharedMemorySize, L_SHM);
  k_all<<<256, 512, L_SHM, stream>>>((const u16*)d_in[0], (const u16*)d_in[1], (const u16*)d_in[2], (const u16*)d_in[4], (const u16*)d_in[5],
                                     (const u16*)d_in[6], (const u16*)d_in[7], (u16*)d_out, (char*)d_ws);
}
```
